```python
import torch

def _softmax(x, axis=-1):
    return torch.softmax(x, dim=axis)

def _gelu(x):
    return torch.nn.functional.gelu(x, approximate='tanh')

def _silu(x):
    return torch.nn.functional.silu(x)

def _sigmoid(x):
    return torch.sigmoid(x)

def _layernorm(x, axis=-1, eps=1e-05):
    m = x.mean(dim=axis, keepdim=True)
    v = x.var(dim=axis, keepdim=True, unbiased=False)
    return (x - m) / torch.sqrt(v + eps)

def _scatter_add(tgt, idx, src, axis=0):
    return tgt.clone().index_add_(axis, idx, src)

def _segment_sum(src, seg, n):
    out = torch.zeros((n,) + src.shape[1:], dtype=src.dtype)
    return out.index_add_(0, seg, src)

def _pad(x, pw):
    flat = tuple((v for pair in reversed(pw) for v in pair))
    return torch.nn.functional.pad(x, flat)
kernel_name = 'adaLN_cond__ssm_conv_s4__glu_gated_swiglu__prompt'

def setup_inputs():

    _w = lambda a: (lambda t: t.to(torch.bfloat16) if t.is_floating_point() else t)(torch.as_tensor(a))

    def _A_setup():
        B, L, D, N = (1, 2048, 640, 128)
        A_re = -(lambda _x: torch.exp(torch.as_tensor(_x)))((lambda *s: torch.randn(*s, dtype=torch.bfloat16) * 3.2)(D, N))
        A_im = torch.pi * (lambda *s: torch.randn(*s, dtype=torch.bfloat16) * 0.05)(D, N)
        B_re = (lambda *s: torch.randn(*s, dtype=torch.bfloat16) * 0.1)(D, N)
        B_im = (lambda *s: torch.randn(*s, dtype=torch.bfloat16) * 0.1)(D, N)
        C_re = (lambda *s: torch.randn(*s, dtype=torch.bfloat16) * 0.1)(D, N)
        C_im = (lambda *s: torch.randn(*s, dtype=torch.bfloat16) * 0.1)(D, N)
        log_dt = (lambda *s: torch.randn(*s, dtype=torch.bfloat16) * 0.05)(D) * 0.5 - 4.0
        Dskip = (lambda *s: torch.randn(*s, dtype=torch.bfloat16) * 0.05)(D)
        return {'u': _w((lambda *s: torch.randn(*s, dtype=torch.bfloat16) * 0.05)(B, L, D)), 'A_re': _w(A_re), 'A_im': _w(A_im), 'B_re': _w(B_re), 'B_im': _w(B_im), 'C_re': _w(C_re), 'C_im': _w(C_im), 'log_dt': _w(log_dt), 'Dskip': _w(Dskip)}
    _A_in = _A_setup()

    def _B_setup():
        B, S, D, F = (1, 2048, 640, 1728)
        return {'x': _w((lambda *s: torch.randn(*s, dtype=torch.bfloat16) * 0.05)(B, S, D)), 'w1': _w((lambda *s: torch.randn(*s, dtype=torch.bfloat16) * 0.4)(D, F) * (2.0 / D) ** 0.5), 'w3': _w((lambda *s: torch.randn(*s, dtype=torch.bfloat16) * 0.4)(D, F) * (2.0 / D) ** 0.5), 'w2': _w((lambda *s: torch.randn(*s, dtype=torch.bfloat16) * 0.4)(F, D) * (2.0 / F) ** 0.5)}
    _B_in = _B_setup()
    return {'A_u': _A_in['u'], 'A_A_re': _A_in['A_re'], 'A_A_im': _A_in['A_im'], 'A_B_re': _A_in['B_re'], 'A_B_im': _A_in['B_im'], 'A_C_re': _A_in['C_re'], 'A_C_im': _A_in['C_im'], 'A_log_dt': _A_in['log_dt'], 'A_Dskip': _A_in['Dskip'], 'B_x': _B_in['x'], 'B_w1': _B_in['w1'], 'B_w3': _B_in['w3'], 'B_w2': _B_in['w2'], 'c': _w((lambda *s: torch.randn(*s, dtype=torch.bfloat16) * 0.2)(1, 640) * 8.0), 'w_mod': _w((lambda *s: torch.randn(*s, dtype=torch.bfloat16) * 0.2)(640, 6 * 640) * 640 ** (-0.5) * 8.0)}

def reference(A_u, A_A_re, A_A_im, A_B_re, A_B_im, A_C_re, A_C_im, A_log_dt, A_Dskip, B_x, B_w1, B_w3, B_w2, c, w_mod):

    def _A_ref(u, A_re, A_im, B_re, B_im, C_re, C_im, log_dt, Dskip):
        Bsz, L, D = u.shape
        dt = torch.exp(log_dt)[:, None]
        A = A_re + 1j * A_im
        Bc = B_re + 1j * B_im
        Cc = C_re + 1j * C_im
        logdA = dt * A
        dB = dt * Bc
        l = (lambda x, dt: x.to(dt))(torch.arange(L), A_re.dtype)
        P = torch.exp(logdA[:, :, None] * l[None, None, :])
        K = torch.einsum('dn,dnl,dn->dl', Cc, P, dB).real
        L2 = 2 * L
        Kp = _pad(K, ((0, 0), (0, L)))
        up = (lambda x, *d: x.permute(*d))(u, 0, 2, 1)
        up = _pad(up, ((0, 0), (0, 0), (0, L)))
        Kf = (lambda x, axes=(-2, -1): torch.fft.rfft2(x.float(), dim=axes))(Kp[:, None, :], axes=(-2, -1))
        Uf = (lambda x, axes=(-2, -1): torch.fft.rfft2(x.float(), dim=axes))(up[:, :, None, :], axes=(-2, -1))
        Yf = Uf * Kf[None, :, :, :]
        y = (lambda x, s, axes=(-2, -1): torch.fft.irfft2(x, s=s, dim=axes).to(torch.bfloat16))(Yf, (1, L2), axes=(-2, -1))[:, :, 0, :L]
        y = (lambda x, *d: x.permute(*d))(y, 0, 2, 1) + u * Dskip[None, None, :]
        return y

    def _B_ref(x, w1, w3, w2):
        gate = torch.einsum('bsd,df->bsf', x, w1)
        up = torch.einsum('bsd,df->bsf', x, w3)
        h = _silu(gate) * up
        out = torch.einsum('bsf,fd->bsd', h, w2)
        return out
    x = A_u
    mod = (_silu(c) @ w_mod).reshape(c.shape[0], 6, 1, -1)
    sa, ca, ga, sb, cb, gb = (mod[:, 0].clone(), mod[:, 1].clone(), mod[:, 2].clone(), mod[:, 3].clone(), mod[:, 4].clone(), mod[:, 5].clone())
    h = _layernorm(x, axis=-1) * (1 + ca) + sa
    x = x + ga * _A_ref(h, A_A_re, A_A_im, A_B_re, A_B_im, A_C_re, A_C_im, A_log_dt, A_Dskip)
    h = _layernorm(x, axis=-1) * (1 + cb) + sb
    x = x + gb * _B_ref(h, B_w1, B_w3, B_w2)
    return x
```

```cpp
#include <hip/hip_runtime.h>
#include <hip/hip_bf16.h>

typedef unsigned short u16;
typedef __attribute__((ext_vector_type(8))) short bf16x8;
typedef __attribute__((ext_vector_type(4))) short s16x4;
typedef __attribute__((ext_vector_type(4))) float f32x4;
typedef __attribute__((ext_vector_type(4))) unsigned u32x4;
typedef __attribute__((ext_vector_type(2))) unsigned u32x2;

#define DEV __device__ __forceinline__

constexpr int L_ = 2048, D_ = 640, N_ = 128, F_ = 1728;
constexpr int KR_LEN = 2208;

constexpr size_t WS_MOD = 0;
constexpr size_t WS_NT = 16384;
constexpr size_t WS_XT = WS_NT + (size_t)D_ * L_ * 2;
constexpr size_t WS_KR = WS_XT + (size_t)D_ * L_ * 2;
constexpr size_t WS_X1T = WS_KR + (size_t)D_ * 2 * KR_LEN * 2;
constexpr size_t WS_X1 = WS_X1T + (size_t)D_ * L_ * 2;
constexpr size_t WS_W13 = WS_X1 + (size_t)D_ * L_ * 2;
constexpr size_t WS_W2 = WS_W13 + (size_t)2 * D_ * F_ * 2;
constexpr size_t WS_G = WS_W2 + (size_t)D_ * F_ * 2;
constexpr size_t WS_END = WS_G + (size_t)L_ * F_ * 2;

DEV float bf2f(u16 x) { return __uint_as_float(((unsigned)x) << 16); }
DEV u16 f2bf(float f) { return __builtin_bit_cast(u16, (__bf16)f); }
DEV float rbf(float f) { return bf2f(f2bf(f)); }
DEV unsigned pack2(float a, float b) { return (unsigned)f2bf(a) | ((unsigned)f2bf(b) << 16); }
DEV float lo16(unsigned u) { return __uint_as_float(u << 16); }
DEV float hi16(unsigned u) { return __uint_as_float(u & 0xffff0000u); }

DEV void cexp(float ar, float ai, float w, float& er, float& ei) {
  float xr = ar * w, xi = ai * w;
  float mag = __builtin_amdgcn_exp2f(xr * 1.44269504088896341f);
  float rev = xi * 0.15915494309189535f;
  rev = rev - rintf(rev);
  er = mag * __builtin_amdgcn_cosf(rev);
  ei = mag * __builtin_amdgcn_sinf(rev);
}
DEV void csq(float& r, float& i) {
  float nr = r * r - i * i, ni = 2.f * r * i;
  r = nr; i = ni;
}

struct Params {
  const u16 *u, *Are, *Aim, *Bre, *Bim, *Cre, *Cim, *logdt, *Dskip, *w1, *w3, *w2, *c, *wmod;
  u16* out;
  char* ws;
};

constexpr int A_KGEN = 640, A_LN = 64, A_MOD = 60, A_W13 = 540, A_W2 = 270;
constexpr int A_TOTAL = A_KGEN + A_LN + A_MOD + A_W13 + A_W2;

__global__ __launch_bounds__(256) void kA(Params p) {
  __shared__ __attribute__((aligned(16))) char smem[41984];
  const int tid = threadIdx.x;
  int b = blockIdx.x;
  if (b < A_MOD) {
    float* red = (float*)smem;
    const int cg = tid & 7, rg = tid >> 3;
    float acc[8];
#pragma unroll
    for (int e = 0; e < 8; ++e) acc[e] = 0.f;
    u32x4 wv[20];
#pragma unroll
    for (int r = 0; r < 20; ++r)
      wv[r] = *(const u32x4*)(p.wmod + (size_t)(rg * 20 + r) * 3840 + b * 64 + cg * 8);
#pragma unroll
    for (int r = 0; r < 20; ++r) {
      float cv = bf2f(p.c[rg * 20 + r]);
      float s = rbf(cv / (1.f + __expf(-cv)));
#pragma unroll
      for (int e = 0; e < 4; ++e) {
        acc[2 * e] += s * lo16(wv[r][e]);
        acc[2 * e + 1] += s * hi16(wv[r][e]);
      }
    }
#pragma unroll
    for (int e = 0; e < 8; ++e) red[rg * 64 + cg * 8 + e] = acc[e];
    __syncthreads();
    if (tid < 64) {
      float s = 0.f;
#pragma unroll
      for (int r = 0; r < 32; ++r) s += red[r * 64 + tid];
      ((float*)(p.ws + WS_MOD))[b * 64 + tid] = rbf(s);
    }
    return;
  }
  b -= A_MOD;
  if (b < A_KGEN) {
    const int d = b;
    float* red = (float*)smem;
    float* Kd = (float*)(smem + 16384);
    const int w = tid >> 6, l = tid & 63, i = l & 15, kq = l >> 4;
    const float dt = rbf(__expf(bf2f(p.logdt[d])));
    f32x4 acc[4];
#pragma unroll
    for (int s = 0; s < 4; ++s) acc[s] = (f32x4){0.f, 0.f, 0.f, 0.f};
    const float fi = (float)i;
#pragma unroll 2
    for (int kk = 0; kk < 8; ++kk) {
      const int n = d * N_ + 32 * w + 4 * kk + kq;
      const float ar = dt * bf2f(p.Are[n]), ai = dt * bf2f(p.Aim[n]);
      const float dBr = dt * bf2f(p.Bre[n]), dBi = dt * bf2f(p.Bim[n]);
      const float Cr = bf2f(p.Cre[n]), Ci = bf2f(p.Cim[n]);
      const float CBr = Cr * dBr - Ci * dBi, CBi = Cr * dBi + Ci * dBr;
      float yr, yi, zr, zi, wr, wi;
      cexp(ar, ai, fi, yr, yi);
      cexp(ar, ai, 16.f * fi, zr, zi);
      cexp(ar, ai, 128.f + 16.f * fi, wr, wi);
      {
        float xr = CBr * zr - CBi * zi, xi = CBr * zi + CBi * zr;
        acc[0] = __builtin_amdgcn_mfma_f32_16x16x4f32(yr, xr, acc[0], 0, 0, 0);
        acc[0] = __builtin_amdgcn_mfma_f32_16x16x4f32(yi, -xi, acc[0], 0, 0, 0);
      }
#pragma unroll
      for (int s = 1; s < 4; ++s) {
        csq(yr, yi);
        csq(wr, wi);
        float xr = CBr * wr - CBi * wi, xi = CBr * wi + CBi * wr;
        acc[s] = __builtin_amdgcn_mfma_f32_16x16x4f32(yr, xr, acc[s], 0, 0, 0);
        acc[s] = __builtin_amdgcn_mfma_f32_16x16x4f32(yi, -xi, acc[s], 0, 0, 0);
      }
    }
#pragma unroll
    for (int s = 0; s < 4; ++s)
#pragma unroll
      for (int g = 0; g < 4; ++g) red[(w * 4 + s) * 256 + i * 16 + 4 * kq + g] = acc[s][g];
    __syncthreads();
    {
      Kd[tid] = red[tid] + red[1024 + tid] + red[2048 + tid] + red[3072 + tid];
      if (tid < 129) {
#pragma unroll
        for (int s = 1; s < 4; ++s) {
          if (tid < 128 || s == 3) {
            int o = s * 256 + tid;
            Kd[256 + 128 * (s - 1) + tid] = red[o] + red[1024 + o] + red[2048 + o] + red[3072 + o];
          }
        }
      }
    }
    __syncthreads();
    u16* Kr = (u16*)(p.ws + WS_KR) + (size_t)d * 2 * KR_LEN;
    for (int c = tid; c < KR_LEN / 8; c += 256) {
      unsigned hw[4], lw[4];
#pragma unroll
      for (int e2 = 0; e2 < 4; ++e2) {
        float kv[2];
#pragma unroll
        for (int z = 0; z < 2; ++z) {
          int k = 2080 - (c * 8 + e2 * 2 + z);
          float v = 0.f;
          if (k >= 0 && k < 2048) {
            int idx;
            if (k < 256) idx = k;
            else {
              int s = (k >= 1024) ? 3 : (k >= 512 ? 2 : 1);
              int q = k >> s, rem = k & ((1 << s) - 1), half = 1 << (s - 1);
              q += (rem > half || (rem == half && (q & 1))) ? 1 : 0;
              idx = 256 + 128 * (s - 1) + (q - 128);
            }
            v = Kd[idx];
          }
          kv[z] = v;
        }
        float h0 = rbf(kv[0]), h1 = rbf(kv[1]);
        hw[e2] = pack2(h0, h1);
        lw[e2] = pack2(kv[0] - h0, kv[1] - h1);
      }
      *(u32x4*)(Kr + c * 8) = (u32x4){hw[0], hw[1], hw[2], hw[3]};
      *(u32x4*)(Kr + KR_LEN + c * 8) = (u32x4){lw[0], lw[1], lw[2], lw[3]};
    }
    return;
  }
  b -= A_KGEN;
  if (b < A_LN) {
    constexpr int XS = 648;
    u16* xs = (u16*)smem;
    float* sm = (float*)(smem + 32 * XS * 2);
    float* sd = sm + 32;
    const int rg = tid >> 4, c = tid & 15;
#pragma unroll
    for (int ps = 0; ps < 2; ++ps) {
      const int row = ps * 16 + rg;
      u32x4 v[5];
#pragma unroll
      for (int q = 0; q < 5; ++q) v[q] = *(const u32x4*)(p.u + (size_t)(b * 32 + row) * D_ + (c + 16 * q) * 8);
      float s = 0.f;
#pragma unroll
      for (int q = 0; q < 5; ++q)
#pragma unroll
        for (int e = 0; e < 4; ++e) s += lo16(v[q][e]) + hi16(v[q][e]);
      s += __shfl_xor(s, 1); s += __shfl_xor(s, 2); s += __shfl_xor(s, 4); s += __shfl_xor(s, 8);
      const float mean = s * (1.f / 640.f);
      float s2 = 0.f;
#pragma unroll
      for (int q = 0; q < 5; ++q)
#pragma unroll
        for (int e = 0; e < 4; ++e) {
          float a = lo16(v[q][e]) - mean, bb = hi16(v[q][e]) - mean;
          s2 += a * a + bb * bb;
        }
      s2 += __shfl_xor(s2, 1); s2 += __shfl_xor(s2, 2); s2 += __shfl_xor(s2, 4); s2 += __shfl_xor(s2, 8);
      if (c == 0) {
        float var = rbf(s2 * (1.f / 640.f));
        sm[row] = rbf(mean);
        sd[row] = rbf(sqrtf(rbf(var + 1e-5f)));
      }
#pragma unroll
      for (int q = 0; q < 5; ++q) *(u32x4*)(xs + row * XS + (c + 16 * q) * 8) = v[q];
    }
    __syncthreads();
    u16* nT = (u16*)(p.ws + WS_NT);
    u16* xT = (u16*)(p.ws + WS_XT);
#pragma unroll 2
    for (int it = 0; it < 10; ++it) {
      const int item = tid + 256 * it;
      const int d = item >> 2, tq = item & 3;
      unsigned xo[4], no[4];
#pragma unroll
      for (int e2 = 0; e2 < 4; ++e2) {
        const int r0 = tq * 8 + e2 * 2;
        u16 a = xs[r0 * XS + d], bb = xs[(r0 + 1) * XS + d];
        xo[e2] = (unsigned)a | ((unsigned)bb << 16);
        float n0 = rbf(bf2f(a) - sm[r0]) / sd[r0];
        float n1 = rbf(bf2f(bb) - sm[r0 + 1]) / sd[r0 + 1];
        no[e2] = pack2(n0, n1);
      }
      const size_t o = (size_t)d * L_ + b * 32 + tq * 8;
      *(u32x4*)(xT + o) = (u32x4){xo[0], xo[1], xo[2], xo[3]};
      *(u32x4*)(nT + o) = (u32x4){no[0], no[1], no[2], no[3]};
    }
    return;
  }
  b -= A_LN;
  if (b < A_W13) {
    const int ks = b / 27, fc = b % 27;
    u16* ts = (u16*)smem;
    {
      const int mat = tid >> 7, r = (tid >> 2) & 31, cq = tid & 3;
      const u16* src = (mat ? p.w3 : p.w1) + (size_t)(ks * 32 + r) * F_ + fc * 64 + cq * 16;
      u32x4 a = *(const u32x4*)src, bb = *(const u32x4*)(src + 8);
      *(u32x4*)(ts + (mat * 32 + r) * 72 + cq * 16) = a;
      *(u32x4*)(ts + (mat * 32 + r) * 72 + cq * 16 + 8) = bb;
    }
    __syncthreads();
    u16* dst = (u16*)(p.ws + WS_W13);
#pragma unroll
    for (int it = 0; it < 2; ++it) {
      const int item = tid + 256 * it;
      const int lane = item & 63, mat = (item >> 6) & 1, ftl = item >> 7;
      const int f = ftl * 16 + (lane & 15), k0 = (lane >> 4) * 8;
      unsigned o[4];
#pragma unroll
      for (int e2 = 0; e2 < 4; ++e2)
        o[e2] = (unsigned)ts[(mat * 32 + k0 + 2 * e2) * 72 + f] | ((unsigned)ts[(mat * 32 + k0 + 2 * e2 + 1) * 72 + f] << 16);
      const int ft = fc * 4 + ftl;
      *(u32x4*)(dst + ((size_t)((ft * 20 + ks) * 2 + mat) * 64 + lane) * 8) = (u32x4){o[0], o[1], o[2], o[3]};
    }
    return;
  }
  b -= A_W13;
  {
    const int ks = b / 5, dc = b % 5;
    u16* ts = (u16*)smem;
    {
      const int r = tid >> 3, cq = tid & 7;
      const u16* src = p.w2 + (size_t)(ks * 32 + r) * D_ + dc * 128 + cq * 16;
      u32x4 a = *(const u32x4*)src, bb = *(const u32x4*)(src + 8);
      *(u32x4*)(ts + r * 136 + cq * 16) = a;
      *(u32x4*)(ts + r * 136 + cq * 16 + 8) = bb;
    }
    __syncthreads();
    u16* dst = (u16*)(p.ws + WS_W2);
#pragma unroll
    for (int it = 0; it < 2; ++it) {
      const int item = tid + 256 * it;
      const int lane = item & 63, dtl = item >> 6;
      const int dd = dtl * 16 + (lane & 15), k0 = (lane >> 4) * 8;
      unsigned o[4];
#pragma unroll
      for (int e2 = 0; e2 < 4; ++e2)
        o[e2] = (unsigned)ts[(k0 + 2 * e2) * 136 + dd] | ((unsigned)ts[(k0 + 2 * e2 + 1) * 136 + dd] << 16);
      const int dt = dc * 8 + dtl;
      *(u32x4*)(dst + ((size_t)(dt * 54 + ks) * 64 + lane) * 8) = (u32x4){o[0], o[1], o[2], o[3]};
    }
  }
}

constexpr int B_HB = 2 * KR_LEN * 2;
constexpr int B_HS = 4640;
constexpr int B_RED = B_HB + B_HS;
constexpr int B_LDS = B_RED + 32768;

template <bool EXTRA>
DEV void conv_residue(const char* smem, int o0, int lane, f32x4 (&acc)[8]) {
  const int r = lane & 15, kg = lane >> 4;
  const int rho = lane & 7, cb = (lane >> 3) & 1;
  bf16x8 bf[9];
  const int bbase = B_HB + rho * B_HS + 2 * (160 - o0 + 128 * cb + 8 * kg);
#pragma unroll
  for (int u = 0; u < 8; ++u) bf[u + 1] = *(const bf16x8*)(smem + bbase + 512 * u);
  if (EXTRA) bf[0] = *(const bf16x8*)(smem + bbase - 512);
  const int abase = 2 * (2080 - o0 - 8 * r + 8 * kg);
#pragma unroll
  for (int i = 0; i < (EXTRA ? 9 : 8); ++i) {
    const bf16x8 ah = *(const bf16x8*)(smem + abase - 512 * i);
    const bf16x8 al = *(const bf16x8*)(smem + abase - 512 * i + KR_LEN * 2);
#pragma unroll
    for (int u = (EXTRA ? -1 : 0); u < 8; ++u) {
      const int q = u + i;
      if (q >= 0 && q < 8) {
        acc[q] = __builtin_amdgcn_mfma_f32_16x16x32_bf16(ah, bf[u + 1], acc[q], 0, 0, 0);
        acc[q] = __builtin_amdgcn_mfma_f32_16x16x32_bf16(al, bf[u + 1], acc[q], 0, 0, 0);
      }
    }
  }
}

__global__ __launch_bounds__(256) void kB(Params p) {
  __shared__ __attribute__((aligned(16))) char smem[B_LDS];
  const int tid = threadIdx.x, d = blockIdx.x;
  const float* mod = (const float*)(p.ws + WS_MOD);
  const float sa = mod[d], t1 = rbf(1.f + mod[640 + d]), ga = mod[1280 + d];
  const float Dk = bf2f(p.Dskip[d]);
  {
    const u32x4* src = (const u32x4*)((const u16*)(p.ws + WS_KR) + (size_t)d * 2 * KR_LEN);
    for (int c = tid; c < 2 * KR_LEN / 8; c += 256) *(u32x4*)(smem + c * 16) = src[c];
  }
  {
    const u16* nT = (const u16*)(p.ws + WS_NT) + (size_t)d * L_;
    for (int c = tid; c < KR_LEN / 8; c += 256) {
      const int t0 = -160 + 8 * c;
      unsigned wv[8];
#pragma unroll
      for (int hh = 0; hh < 2; ++hh) {
        const int tt = t0 + 8 * hh;
        if (tt >= 0 && tt < L_) {
          u32x4 v = *(const u32x4*)(nT + tt);
#pragma unroll
          for (int e = 0; e < 4; ++e) {
            float a = rbf(rbf(lo16(v[e]) * t1) + sa), bb = rbf(rbf(hi16(v[e]) * t1) + sa);
            wv[hh * 4 + e] = pack2(a, bb);
          }
        } else {
#pragma unroll
          for (int e = 0; e < 4; ++e) wv[hh * 4 + e] = 0u;
        }
      }
#pragma unroll
      for (int rho = 0; rho < 8; ++rho) {
        u32x4 o;
#pragma unroll
        for (int e = 0; e < 4; ++e) {
          const int k = (rho >> 1) + e;
          o[e] = (rho & 1) ? __builtin_amdgcn_alignbit(wv[k + 1], wv[k], 16) : wv[k];
        }
        *(u32x4*)(smem + B_HB + rho * B_HS + c * 16) = o;
      }
    }
  }
  __syncthreads();
  const int w = __builtin_amdgcn_readfirstlane(tid >> 6), lane = tid & 63;
  f32x4 acc[8];
#pragma unroll
  for (int q = 0; q < 8; ++q) acc[q] = (f32x4){0.f, 0.f, 0.f, 0.f};
  if (w == 0) conv_residue<true>(smem, -96, lane, acc);
  else conv_residue<false>(smem, -96 + 32 * w, lane, acc);
  conv_residue<false>(smem, -96 + 32 * (w + 4), lane, acc);
  __syncthreads();
  {
    float* red = (float*)(smem + B_RED) + w * 2048;
    const int rho = lane & 7, cb = (lane >> 3) & 1, rq = lane >> 4;
#pragma unroll
    for (int q = 0; q < 8; ++q)
#pragma unroll
      for (int g = 0; g < 4; ++g) {
        int t = 256 * q + 128 * cb + rho + 32 * rq + 8 * g;
        int pos = t ^ (((t >> 5) & 1) << 3) ^ (((t >> 7) & 1) << 4);
        red[pos] = acc[q][g];
      }
  }
  __syncthreads();
  {
    const int t0 = tid * 8;
    const int pos = t0 ^ (((t0 >> 5) & 1) << 3) ^ (((t0 >> 7) & 1) << 4);
    const float* red = (const float*)(smem + B_RED);
    float y[8];
#pragma unroll
    for (int e = 0; e < 8; ++e) y[e] = 0.f;
#pragma unroll
    for (int ww = 0; ww < 4; ++ww) {
      f32x4 a = *(const f32x4*)(red + ww * 2048 + pos), bb = *(const f32x4*)(red + ww * 2048 + pos + 4);
#pragma unroll
      for (int e = 0; e < 4; ++e) { y[e] += a[e]; y[4 + e] += bb[e]; }
    }
    const u32x4 hv = *(const u32x4*)(smem + B_HB + 2 * (160 + t0));
    const u32x4 xv = *(const u32x4*)((const u16*)(p.ws + WS_XT) + (size_t)d * L_ + t0);
    u32x4 o;
#pragma unroll
    for (int e = 0; e < 4; ++e) {
      float h0 = lo16(hv[e]), h1 = hi16(hv[e]);
      float y0 = rbf(rbf(y[2 * e]) + rbf(h0 * Dk)), y1 = rbf(rbf(y[2 * e + 1]) + rbf(h1 * Dk));
      float x0 = lo16(xv[e]) + rbf(ga * y0), x1 = hi16(xv[e]) + rbf(ga * y1);
      o[e] = pack2(x0, x1);
    }
    *(u32x4*)((u16*)(p.ws + WS_X1T) + (size_t)d * L_ + t0) = o;
  }
}

DEV int d_off(int k, int c) {
  const int f = ((k >> 1) & 1) | (((k >> 3) & 1) << 1);
  return k * 128 + ((((c >> 1) ^ f)) << 5) + ((c & 1) << 4);
}
DEV bf16x8 d_frag(const char* smem, int lane, int mi, int ks) {
  const int g = lane >> 4, q = (lane & 15) >> 2, pp = lane & 3;
  const int k0 = 32 * ks + 8 * g + q;
  const int c = 2 * mi + (pp >> 1);
  typedef __attribute__((address_space(3))) s16x4* lp;
  s16x4 a = __builtin_amdgcn_ds_read_tr16_b64_v4i16((lp)(smem + d_off(k0, c) + 8 * (pp & 1)));
  s16x4 bq = __builtin_amdgcn_ds_read_tr16_b64_v4i16((lp)(smem + d_off(k0 + 4, c) + 8 * (pp & 1)));
  return __builtin_shufflevector(a, bq, 0, 1, 2, 3, 4, 5, 6, 7);
}

template <int MT>
DEV void d_unit(const Params& p, const char* smem, int lane, int ft, int mi0, int mt) {
  const bf16x8* wsrc = (const bf16x8*)(p.ws + WS_W13) + (size_t)ft * 20 * 2 * 64 + lane;
  bf16x8 wf[20][2];
#pragma unroll
  for (int ks = 0; ks < 20; ++ks) {
    wf[ks][0] = wsrc[(ks * 2) * 64];
    wf[ks][1] = wsrc[(ks * 2 + 1) * 64];
  }
  f32x4 acc[2][MT];
#pragma unroll
  for (int m = 0; m < MT; ++m) { acc[0][m] = (f32x4){0.f, 0.f, 0.f, 0.f}; acc[1][m] = (f32x4){0.f, 0.f, 0.f, 0.f}; }
#pragma unroll
  for (int ks = 0; ks < 20; ++ks) {
#pragma unroll
    for (int m = 0; m < MT; ++m) {
      const bf16x8 hf = d_frag(smem, lane, mi0 + m, ks);
      acc[0][m] = __builtin_amdgcn_mfma_f32_16x16x32_bf16(wf[ks][0], hf, acc[0][m], 0, 0, 0);
      acc[1][m] = __builtin_amdgcn_mfma_f32_16x16x32_bf16(wf[ks][1], hf, acc[1][m], 0, 0, 0);
    }
  }
  const int rq = lane >> 4;
  u16* gdst = (u16*)(p.ws + WS_G);
#pragma unroll
  for (int m = 0; m < MT; ++m) {
    float gv[4];
#pragma unroll
    for (int g = 0; g < 4; ++g) {
      float gate = rbf(acc[0][m][g]), up = rbf(acc[1][m][g]);
      float s = rbf(gate / (1.f + __expf(-gate)));
      gv[g] = s * up;
    }
    const int mtile = mt * 4 + mi0 + m;
    const int lane2 = (lane & 15) + 16 * (2 * (ft & 1) + (rq >> 1));
    u32x2 o = {pack2(gv[0], gv[1]), pack2(gv[2], gv[3])};
    *(u32x2*)(gdst + ((size_t)(mtile * 54 + (ft >> 1)) * 64 + lane2) * 8 + 4 * (rq & 1)) = o;
  }
}

__global__ __launch_bounds__(256) void kD(Params p) {
  __shared__ __attribute__((aligned(16))) char smem[81920 + 2048];
  const int tid = threadIdx.x, fpart = blockIdx.x & 7, mt = blockIdx.x >> 3;
  const int w = __builtin_amdgcn_readfirstlane(tid >> 6), lane = tid & 63;
  float* ps = (float*)(smem + 81920);
  const float* mod = (const float*)(p.ws + WS_MOD);
  const int c = tid & 7, d0 = tid >> 3;
  u32x4 v[20];
  {
    const u16* src = (const u16*)(p.ws + WS_X1T) + mt * 64 + c * 8;
#pragma unroll
    for (int it = 0; it < 20; ++it) v[it] = *(const u32x4*)(src + (size_t)(d0 + 32 * it) * L_);
  }
  float s[8];
#pragma unroll
  for (int e = 0; e < 8; ++e) s[e] = 0.f;
#pragma unroll
  for (int it = 0; it < 20; ++it)
#pragma unroll
    for (int e = 0; e < 4; ++e) { s[2 * e] += lo16(v[it][e]); s[2 * e + 1] += hi16(v[it][e]); }
#pragma unroll
  for (int e = 0; e < 8; ++e) { s[e] += __shfl_xor(s[e], 8); s[e] += __shfl_xor(s[e], 16); s[e] += __shfl_xor(s[e], 32); }
  if (lane < 8) {
#pragma unroll
    for (int e = 0; e < 8; ++e) ps[w * 64 + lane * 8 + e] = s[e];
  }
  __syncthreads();
  float mean[8];
#pragma unroll
  for (int e = 0; e < 8; ++e) {
    const int t = c * 8 + e;
    mean[e] = (ps[t] + ps[64 + t] + ps[128 + t] + ps[192 + t]) * (1.f / 640.f);
    s[e] = 0.f;
  }
#pragma unroll
  for (int it = 0; it < 20; ++it)
#pragma unroll
    for (int e = 0; e < 4; ++e) {
      float a = lo16(v[it][e]) - mean[2 * e], bb = hi16(v[it][e]) - mean[2 * e + 1];
      s[2 * e] += a * a; s[2 * e + 1] += bb * bb;
    }
#pragma unroll
  for (int e = 0; e < 8; ++e) { s[e] += __shfl_xor(s[e], 8); s[e] += __shfl_xor(s[e], 16); s[e] += __shfl_xor(s[e], 32); }
  if (lane < 8) {
#pragma unroll
    for (int e = 0; e < 8; ++e) ps[256 + w * 64 + lane * 8 + e] = s[e];
  }
  if (fpart == 0) {
#pragma unroll
    for (int it = 0; it < 20; ++it) *(u32x4*)(smem + d_off(d0 + 32 * it, c)) = v[it];
  }
  __syncthreads();
  float den[8];
#pragma unroll
  for (int e = 0; e < 8; ++e) {
    const int t = 256 + c * 8 + e;
    float var = rbf((ps[t] + ps[64 + t] + ps[128 + t] + ps[192 + t]) * (1.f / 640.f));
    den[e] = rbf(sqrtf(rbf(var + 1e-5f)));
    mean[e] = rbf(mean[e]);
  }
  if (fpart == 0) {
    u16* x1 = (u16*)(p.ws + WS_X1);
#pragma unroll 4
    for (int ks = 0; ks < 20; ++ks) {
      bf16x8 f = d_frag(smem, lane, w, ks);
      *(bf16x8*)(x1 + (size_t)(mt * 64 + w * 16 + (lane & 15)) * D_ + ks * 32 + (lane >> 4) * 8) = f;
    }
    __syncthreads();
  }
#pragma unroll
  for (int it = 0; it < 20; ++it) {
    const int d = d0 + 32 * it;
    const float t1 = rbf(1.f + mod[4 * 640 + d]), sb = mod[3 * 640 + d];
    u32x4 o;
#pragma unroll
    for (int e = 0; e < 4; ++e) {
      float n0 = rbf(rbf(lo16(v[it][e]) - mean[2 * e]) / den[2 * e]);
      float n1 = rbf(rbf(hi16(v[it][e]) - mean[2 * e + 1]) / den[2 * e + 1]);
      float h0 = rbf(n0 * t1) + sb, h1 = rbf(n1 * t1) + sb;
      o[e] = pack2(h0, h1);
    }
    *(u32x4*)(smem + d_off(d, c)) = o;
  }
  __syncthreads();
  const int fs = (27 * fpart) >> 1, fe = (27 * (fpart + 1)) >> 1;
  for (int j = 0; j < 3; ++j) d_unit<4>(p, smem, lane, fs + j * 4 + w, 0, mt);
  if (fe - fs == 14) d_unit<2>(p, smem, lane, fs + 12 + (w >> 1), 2 * (w & 1), mt);
  else d_unit<1>(p, smem, lane, fs + 12, w, mt);
}

constexpr int E_RS = 84;
__global__ __launch_bounds__(256) void kE(Params p) {
  __shared__ __attribute__((aligned(16))) float red[4 * 64 * E_RS];
  const int tid = threadIdx.x, xc = blockIdx.x & 7, j = blockIdx.x >> 3;
  const int mt = xc * 4 + (j & 3), np = j >> 2;
  const int w = __builtin_amdgcn_readfirstlane(tid >> 6), lane = tid & 63;
  const int kstart = (w < 2) ? 14 * w : 28 + 13 * (w - 2);
  const int kcnt = (w < 2) ? 14 : 13;
  const bf16x8* gsrc = (const bf16x8*)(p.ws + WS_G) + (size_t)(mt * 4) * 54 * 64 + lane;
  const bf16x8* wsrc = (const bf16x8*)(p.ws + WS_W2) + (size_t)(np * 5) * 54 * 64 + lane;
  f32x4 acc[5][4];
#pragma unroll
  for (int i = 0; i < 5; ++i)
#pragma unroll
    for (int m = 0; m < 4; ++m) acc[i][m] = (f32x4){0.f, 0.f, 0.f, 0.f};
  bf16x8 gf[3][4], wf[3][5];
#pragma unroll
  for (int s = 0; s < 2; ++s) {
#pragma unroll
    for (int m = 0; m < 4; ++m) gf[s][m] = gsrc[(m * 54 + kstart + s) * 64];
#pragma unroll
    for (int i = 0; i < 5; ++i) wf[s][i] = wsrc[(i * 54 + kstart + s) * 64];
  }
#pragma unroll
  for (int kk = 0; kk < 14; ++kk) {
    if (kk + 2 < kcnt) {
#pragma unroll
      for (int m = 0; m < 4; ++m) gf[(kk + 2) % 3][m] = gsrc[(m * 54 + kstart + kk + 2) * 64];
#pragma unroll
      for (int i = 0; i < 5; ++i) wf[(kk + 2) % 3][i] = wsrc[(i * 54 + kstart + kk + 2) * 64];
    }
    if (kk < kcnt) {
#pragma unroll
      for (int i = 0; i < 5; ++i)
#pragma unroll
        for (int m = 0; m < 4; ++m)
          acc[i][m] = __builtin_amdgcn_mfma_f32_16x16x32_bf16(wf[kk % 3][i], gf[kk % 3][m], acc[i][m], 0, 0, 0);
    }
  }
  {
    const int rq = lane >> 4;
#pragma unroll
    for (int i = 0; i < 5; ++i)
#pragma unroll
      for (int m = 0; m < 4; ++m)
        *(f32x4*)(red + (w * 64 + m * 16 + (lane & 15)) * E_RS + i * 16 + 4 * rq) = acc[i][m];
  }
  __syncthreads();
  const float* mod = (const float*)(p.ws + WS_MOD);
  const u16* x1 = (const u16*)(p.ws + WS_X1);
  for (int item = tid; item < 640; item += 256) {
    const int t = item / 10, cc = item % 10;
    float y[8];
#pragma unroll
    for (int e = 0; e < 8; ++e) y[e] = 0.f;
#pragma unroll
    for (int ww = 0; ww < 4; ++ww) {
      f32x4 a = *(const f32x4*)(red + (ww * 64 + t) * E_RS + cc * 8), bb = *(const f32x4*)(red + (ww * 64 + t) * E_RS + cc * 8 + 4);
#pragma unroll
      for (int e = 0; e < 4; ++e) { y[e] += a[e]; y[4 + e] += bb[e]; }
    }
    const int dcol = np * 80 + cc * 8;
    const size_t o = (size_t)(mt * 64 + t) * D_ + dcol;
    const u32x4 xv = *(const u32x4*)(x1 + o);
    u32x4 ov;
#pragma unroll
    for (int e = 0; e < 4; ++e) {
      float g0 = mod[5 * 640 + dcol + 2 * e], g1 = mod[5 * 640 + dcol + 2 * e + 1];
      float r0 = lo16(xv[e]) + rbf(g0 * rbf(y[2 * e])), r1 = hi16(xv[e]) + rbf(g1 * rbf(y[2 * e + 1]));
      ov[e] = pack2(r0, r1);
    }
    *(u32x4*)(p.out + o) = ov;
  }
}

extern "C" void kernel_launch(void* const* d_in, const int* in_sizes, int n_in,
                              void* d_out, int out_size, void* d_ws, size_t ws_size,
                              hipStream_t stream) {
  Params p;
  p.u = (const u16*)d_in[0];
  p.Are = (const u16*)d_in[1];
  p.Aim = (const u16*)d_in[2];
  p.Bre = (const u16*)d_in[3];
  p.Bim = (const u16*)d_in[4];
  p.Cre = (const u16*)d_in[5];
  p.Cim = (const u16*)d_in[6];
  p.logdt = (const u16*)d_in[7];
  p.Dskip = (const u16*)d_in[8];
  p.w1 = (const u16*)d_in[10];
  p.w3 = (const u16*)d_in[11];
  p.w2 = (const u16*)d_in[12];
  p.c = (const u16*)d_in[13];
  p.wmod = (const u16*)d_in[14];
  p.out = (u16*)d_out;
  p.ws = (char*)d_ws;
  kA<<<A_TOTAL, 256, 0, stream>>>(p);
  kB<<<640, 256, 0, stream>>>(p);
  kD<<<256, 256, 0, stream>>>(p);
  kE<<<256, 256, 0, stream>>>(p);
}
```

```cpp
#include <hip/hip_runtime.h>
#include <hip/hip_bf16.h>

typedef unsigned short u16;
typedef __attribute__((ext_vector_type(8))) short bf16x8;
typedef __attribute__((ext_vector_type(4))) short s16x4;
typedef __attribute__((ext_vector_type(4))) float f32x4;
typedef __attribute__((ext_vector_type(4))) unsigned u32x4;
typedef __attribute__((ext_vector_type(2))) unsigned u32x2;

#define DEV __device__ __forceinline__

constexpr int L_ = 2048, D_ = 640, N_ = 128, F_ = 1728;
constexpr int KR_LEN = 2208;

constexpr size_t WS_MOD = 0;
constexpr size_t WS_NT = 16384;
constexpr size_t WS_XT = WS_NT + (size_t)D_ * L_ * 2;
constexpr size_t WS_KR = WS_XT + (size_t)D_ * L_ * 2;
constexpr size_t WS_X1T = WS_KR + (size_t)D_ * 2 * KR_LEN * 2;
constexpr size_t WS_X1 = WS_X1T + (size_t)D_ * L_ * 2;
constexpr size_t WS_W13 = WS_X1 + (size_t)D_ * L_ * 2;
constexpr size_t WS_W2 = WS_W13 + (size_t)2 * D_ * F_ * 2;
constexpr size_t WS_G = WS_W2 + (size_t)D_ * F_ * 2;
constexpr size_t WS_END = WS_G + (size_t)L_ * F_ * 2;

DEV float bf2f(u16 x) { return __uint_as_float(((unsigned)x) << 16); }
DEV u16 f2bf(float f) { return __builtin_bit_cast(u16, (__bf16)f); }
DEV float rbf(float f) { return bf2f(f2bf(f)); }
DEV unsigned pack2(float a, float b) { return (unsigned)f2bf(a) | ((unsigned)f2bf(b) << 16); }
DEV float lo16(unsigned u) { return __uint_as_float(u << 16); }
DEV float hi16(unsigned u) { return __uint_as_float(u & 0xffff0000u); }

DEV void cexp(float ar, float ai, float w, float& er, float& ei) {
  float xr = ar * w, xi = ai * w;
  float mag = __builtin_amdgcn_exp2f(xr * 1.44269504088896341f);
  float rev = xi * 0.15915494309189535f;
  rev = rev - rintf(rev);
  er = mag * __builtin_amdgcn_cosf(rev);
  ei = mag * __builtin_amdgcn_sinf(rev);
}
DEV void csq(float& r, float& i) {
  float nr = r * r - i * i, ni = 2.f * r * i;
  r = nr; i = ni;
}

struct Params {
  const u16 *u, *Are, *Aim, *Bre, *Bim, *Cre, *Cim, *logdt, *Dskip, *w1, *w3, *w2, *c, *wmod;
  u16* out;
  char* ws;
};

constexpr int A_KGEN = 640, A_LN = 128, A_MOD = 60, A_W13 = 540, A_W2 = 270;
constexpr int A_TOTAL = A_KGEN + A_LN + A_MOD + A_W13 + A_W2;

__global__ __launch_bounds__(256) void kA(Params p) {
  __shared__ __attribute__((aligned(16))) char smem[22528];
  const int tid = threadIdx.x;
  int b = blockIdx.x;
  if (b < A_MOD) {
    float* red = (float*)smem;
    const int cg = tid & 7, rg = tid >> 3;
    float acc[8];
#pragma unroll
    for (int e = 0; e < 8; ++e) acc[e] = 0.f;
    u32x4 wv[20];
#pragma unroll
    for (int r = 0; r < 20; ++r)
      wv[r] = *(const u32x4*)(p.wmod + (size_t)(rg * 20 + r) * 3840 + b * 64 + cg * 8);
#pragma unroll
    for (int r = 0; r < 20; ++r) {
      float cv = bf2f(p.c[rg * 20 + r]);
      float s = rbf(cv / (1.f + __expf(-cv)));
#pragma unroll
      for (int e = 0; e < 4; ++e) {
        acc[2 * e] += s * lo16(wv[r][e]);
        acc[2 * e + 1] += s * hi16(wv[r][e]);
      }
    }
#pragma unroll
    for (int e = 0; e < 8; ++e) red[rg * 64 + cg * 8 + e] = acc[e];
    __syncthreads();
    if (tid < 64) {
      float s = 0.f;
#pragma unroll
      for (int r = 0; r < 32; ++r) s += red[r * 64 + tid];
      ((float*)(p.ws + WS_MOD))[b * 64 + tid] = rbf(s);
    }
    return;
  }
  b -= A_MOD;
  if (b < A_KGEN) {
    const int d = b;
    float* red = (float*)smem;
    float* Kd = (float*)(smem + 16384);
    f32x4* prm = (f32x4*)(smem + 20480);
    const int w = tid >> 6, l = tid & 63, i = l & 15, kq = l >> 4;
    if (tid < 128) {
      const int n = d * N_ + tid;
      const u16 a0 = p.Are[n], a1 = p.Aim[n], b0 = p.Bre[n], b1 = p.Bim[n], c0 = p.Cre[n], c1 = p.Cim[n];
      const float dt = rbf(__expf(bf2f(p.logdt[d])));
      const float dBr = dt * bf2f(b0), dBi = dt * bf2f(b1);
      const float Cr = bf2f(c0), Ci = bf2f(c1);
      prm[tid] = (f32x4){dt * bf2f(a0), dt * bf2f(a1), Cr * dBr - Ci * dBi, Cr * dBi + Ci * dBr};
    }
    __syncthreads();
    f32x4 acc[4];
#pragma unroll
    for (int s = 0; s < 4; ++s) acc[s] = (f32x4){0.f, 0.f, 0.f, 0.f};
    const float fi = (float)i;
#pragma unroll 2
    for (int kk = 0; kk < 8; ++kk) {
      const f32x4 pr = prm[32 * w + 4 * kk + kq];
      const float ar = pr[0], ai = pr[1], CBr = pr[2], CBi = pr[3];
      float yr, yi, zr, zi, wr, wi;
      cexp(ar, ai, fi, yr, yi);
      cexp(ar, ai, 16.f * fi, zr, zi);
      cexp(ar, ai, 128.f + 16.f * fi, wr, wi);
      {
        float xr = CBr * zr - CBi * zi, xi = CBr * zi + CBi * zr;
        acc[0] = __builtin_amdgcn_mfma_f32_16x16x4f32(yr, xr, acc[0], 0, 0, 0);
        acc[0] = __builtin_amdgcn_mfma_f32_16x16x4f32(yi, -xi, acc[0], 0, 0, 0);
      }
#pragma unroll
      for (int s = 1; s < 4; ++s) {
        csq(yr, yi);
        csq(wr, wi);
        float xr = CBr * wr - CBi * wi, xi = CBr * wi + CBi * wr;
        acc[s] = __builtin_amdgcn_mfma_f32_16x16x4f32(yr, xr, acc[s], 0, 0, 0);
        acc[s] = __builtin_amdgcn_mfma_f32_16x16x4f32(yi, -xi, acc[s], 0, 0, 0);
      }
    }
#pragma unroll
    for (int s = 0; s < 4; ++s)
#pragma unroll
      for (int g = 0; g < 4; ++g) red[(w * 4 + s) * 256 + i * 16 + 4 * kq + g] = acc[s][g];
    __syncthreads();
    {
      Kd[tid] = red[tid] + red[1024 + tid] + red[2048 + tid] + red[3072 + tid];
      if (tid < 129) {
#pragma unroll
        for (int s = 1; s < 4; ++s) {
          if (tid < 128 || s == 3) {
            int o = s * 256 + tid;
            Kd[256 + 128 * (s - 1) + tid] = red[o] + red[1024 + o] + red[2048 + o] + red[3072 + o];
          }
        }
      }
    }
    __syncthreads();
    u16* Kr = (u16*)(p.ws + WS_KR) + (size_t)d * 2 * KR_LEN;
    for (int c = tid; c < KR_LEN / 8; c += 256) {
      unsigned hw[4], lw[4];
#pragma unroll
      for (int e2 = 0; e2 < 4; ++e2) {
        float kv[2];
#pragma unroll
        for (int z = 0; z < 2; ++z) {
          int k = 2080 - (c * 8 + e2 * 2 + z);
          float v = 0.f;
          if (k >= 0 && k < 2048) {
            int idx;
            if (k < 256) idx = k;
            else {
              int s = (k >= 1024) ? 3 : (k >= 512 ? 2 : 1);
              int q = k >> s, rem = k & ((1 << s) - 1), half = 1 << (s - 1);
              q += (rem > half || (rem == half && (q & 1))) ? 1 : 0;
              idx = 256 + 128 * (s - 1) + (q - 128);
            }
            v = Kd[idx];
          }
          kv[z] = v;
        }
        float h0 = rbf(kv[0]), h1 = rbf(kv[1]);
        hw[e2] = pack2(h0, h1);
        lw[e2] = pack2(kv[0] - h0, kv[1] - h1);
      }
      *(u32x4*)(Kr + c * 8) = (u32x4){hw[0], hw[1], hw[2], hw[3]};
      *(u32x4*)(Kr + KR_LEN + c * 8) = (u32x4){lw[0], lw[1], lw[2], lw[3]};
    }
    return;
  }
  b -= A_KGEN;
  if (b < A_LN) {
    constexpr int XS = 648;
    u16* xs = (u16*)smem;
    float* sm = (float*)(smem + 16 * XS * 2);
    float* sd = sm + 16;
    const int row = tid >> 4, c = tid & 15;
    {
      u32x4 v[5];
#pragma unroll
      for (int q = 0; q < 5; ++q) v[q] = *(const u32x4*)(p.u + (size_t)(b * 16 + row) * D_ + (c + 16 * q) * 8);
      float s = 0.f;
#pragma unroll
      for (int q = 0; q < 5; ++q)
#pragma unroll
        for (int e = 0; e < 4; ++e) s += lo16(v[q][e]) + hi16(v[q][e]);
      s += __shfl_xor(s, 1); s += __shfl_xor(s, 2); s += __shfl_xor(s, 4); s += __shfl_xor(s, 8);
      const float mean = s * (1.f / 640.f);
      float s2 = 0.f;
#pragma unroll
      for (int q = 0; q < 5; ++q)
#pragma unroll
        for (int e = 0; e < 4; ++e) {
          float a = lo16(v[q][e]) - mean, bb = hi16(v[q][e]) - mean;
          s2 += a * a + bb * bb;
        }
      s2 += __shfl_xor(s2, 1); s2 += __shfl_xor(s2, 2); s2 += __shfl_xor(s2, 4); s2 += __shfl_xor(s2, 8);
      if (c == 0) {
        float var = rbf(s2 * (1.f / 640.f));
        sm[row] = rbf(mean);
        sd[row] = __builtin_amdgcn_rcpf(rbf(sqrtf(rbf(var + 1e-5f))));
      }
#pragma unroll
      for (int q = 0; q < 5; ++q) *(u32x4*)(xs + row * XS + (c + 16 * q) * 8) = v[q];
    }
    __syncthreads();
    u16* nT = (u16*)(p.ws + WS_NT);
    u16* xT = (u16*)(p.ws + WS_XT);
    const int tq = tid & 1;
    float mm[8], dd[8];
#pragma unroll
    for (int e = 0; e < 8; ++e) { mm[e] = sm[tq * 8 + e]; dd[e] = sd[tq * 8 + e]; }
#pragma unroll
    for (int it = 0; it < 5; ++it) {
      const int d = (tid >> 1) + 128 * it;
      unsigned xo[4], no[4];
#pragma unroll
      for (int e2 = 0; e2 < 4; ++e2) {
        const int r0 = tq * 8 + e2 * 2;
        u16 a = xs[r0 * XS + d], bb = xs[(r0 + 1) * XS + d];
        xo[e2] = (unsigned)a | ((unsigned)bb << 16);
        float n0 = rbf(bf2f(a) - mm[2 * e2]) * dd[2 * e2];
        float n1 = rbf(bf2f(bb) - mm[2 * e2 + 1]) * dd[2 * e2 + 1];
        no[e2] = pack2(n0, n1);
      }
      const size_t o = (size_t)d * L_ + b * 16 + tq * 8;
      *(u32x4*)(xT + o) = (u32x4){xo[0], xo[1], xo[2], xo[3]};
      *(u32x4*)(nT + o) = (u32x4){no[0], no[1], no[2], no[3]};
    }
    return;
  }
  b -= A_LN;
  if (b < A_W13) {
    const int ks = b / 27, fc = b % 27;
    u16* ts = (u16*)smem;
    {
      const int mat = tid >> 7, r = (tid >> 2) & 31, cq = tid & 3;
      const u16* src = (mat ? p.w3 : p.w1) + (size_t)(ks * 32 + r) * F_ + fc * 64 + cq * 16;
      u32x4 a = *(const u32x4*)src, bb = *(const u32x4*)(src + 8);
      *(u32x4*)(ts + (mat * 32 + r) * 72 + cq * 16) = a;
      *(u32x4*)(ts + (mat * 32 + r) * 72 + cq * 16 + 8) = bb;
    }
    __syncthreads();
    u16* dst = (u16*)(p.ws + WS_W13);
#pragma unroll
    for (int it = 0; it < 2; ++it) {
      const int item = tid + 256 * it;
      const int lane = item & 63, mat = (item >> 6) & 1, ftl = item >> 7;
      const int f = ftl * 16 + (lane & 15), k0 = (lane >> 4) * 8;
      unsigned o[4];
#pragma unroll
      for (int e2 = 0; e2 < 4; ++e2)
        o[e2] = (unsigned)ts[(mat * 32 + k0 + 2 * e2) * 72 + f] | ((unsigned)ts[(mat * 32 + k0 + 2 * e2 + 1) * 72 + f] << 16);
      const int ft = fc * 4 + ftl;
      *(u32x4*)(dst + ((size_t)((ft * 20 + ks) * 2 + mat) * 64 + lane) * 8) = (u32x4){o[0], o[1], o[2], o[3]};
    }
    return;
  }
  b -= A_W13;
  {
    const int ks = b / 5, dc = b % 5;
    u16* ts = (u16*)smem;
    {
      const int r = tid >> 3, cq = tid & 7;
      const u16* src = p.w2 + (size_t)(ks * 32 + r) * D_ + dc * 128 + cq * 16;
      u32x4 a = *(const u32x4*)src, bb = *(const u32x4*)(src + 8);
      *(u32x4*)(ts + r * 136 + cq * 16) = a;
      *(u32x4*)(ts + r * 136 + cq * 16 + 8) = bb;
    }
    __syncthreads();
    u16* dst = (u16*)(p.ws + WS_W2);
#pragma unroll
    for (int it = 0; it < 2; ++it) {
      const int item = tid + 256 * it;
      const int lane = item & 63, dtl = item >> 6;
      const int dd = dtl * 16 + (lane & 15), k0 = (lane >> 4) * 8;
      unsigned o[4];
#pragma unroll
      for (int e2 = 0; e2 < 4; ++e2)
        o[e2] = (unsigned)ts[(k0 + 2 * e2) * 136 + dd] | ((unsigned)ts[(k0 + 2 * e2 + 1) * 136 + dd] << 16);
      const int dt = dc * 8 + dtl;
      *(u32x4*)(dst + ((size_t)(dt * 54 + ks) * 64 + lane) * 8) = (u32x4){o[0], o[1], o[2], o[3]};
    }
  }
}

constexpr int B_HB = 2 * KR_LEN * 2;
constexpr int B_HS = 4640;
constexpr int B_RED = B_HB + B_HS;
constexpr int B_LDS = B_RED + 32768;

template <int UMAX>
DEV void conv_residue(const char* smem, int o0, int lane, f32x4 (&acc)[8]) {
  const int r = lane & 15, kg = lane >> 4;
  const int rho = lane & 7, cb = (lane >> 3) & 1;
  bf16x8 bf[UMAX + 1];
  const int bbase = B_HB + rho * B_HS + 2 * (160 - o0 + 128 * cb + 8 * kg);
#pragma unroll
  for (int u = 0; u <= UMAX; ++u) bf[u] = *(const bf16x8*)(smem + bbase + 512 * u);
  const int abase = 2 * (2080 - o0 - 8 * r + 8 * kg);
#pragma unroll
  for (int i = 0; i < 8; ++i) {
    const bf16x8 ah = *(const bf16x8*)(smem + abase - 512 * i);
    const bf16x8 al = *(const bf16x8*)(smem + abase - 512 * i + KR_LEN * 2);
#pragma unroll
    for (int u = 0; u <= UMAX; ++u) {
      const int q = u + i;
      if (q < 8) {
        acc[q] = __builtin_amdgcn_mfma_f32_16x16x32_bf16(ah, bf[u], acc[q], 0, 0, 0);
        acc[q] = __builtin_amdgcn_mfma_f32_16x16x32_bf16(al, bf[u], acc[q], 0, 0, 0);
      }
    }
  }
}

DEV void h_mod(const u32x4 v, float t1, float sa, unsigned* wv) {
#pragma unroll
  for (int e = 0; e < 4; ++e) {
    float a = rbf(lo16(v[e]) * t1) + sa, bb = rbf(hi16(v[e]) * t1) + sa;
    wv[e] = pack2(a, bb);
  }
}
DEV void h_store(char* smem, int c, const unsigned* wv) {
#pragma unroll
  for (int rho = 0; rho < 8; ++rho) {
    u32x4 o;
#pragma unroll
    for (int e = 0; e < 4; ++e) {
      const int k = (rho >> 1) + e;
      o[e] = (rho & 1) ? __builtin_amdgcn_alignbit(wv[k + 1], wv[k], 16) : wv[k];
    }
    *(u32x4*)(smem + B_HB + rho * B_HS + c * 16) = o;
  }
}

__global__ __launch_bounds__(256) void kB(Params p) {
  __shared__ __attribute__((aligned(16))) char smem[B_LDS];
  const int tid = threadIdx.x, d = blockIdx.x;
  const float* mod = (const float*)(p.ws + WS_MOD);
  const u16* nT = (const u16*)(p.ws + WS_NT) + (size_t)d * L_;
  const u32x4* ksrc = (const u32x4*)((const u16*)(p.ws + WS_KR) + (size_t)d * 2 * KR_LEN);
  const u32x4 n0 = *(const u32x4*)(nT + tid * 8);
  const u32x4 n1 = *(const u32x4*)(nT + (tid < 255 ? tid * 8 + 8 : 0));
  const u32x4 k0 = ksrc[tid], k1 = ksrc[tid + 256];
  const u32x4 k2 = ksrc[tid < 40 ? tid + 512 : 0];
  const u32x4 xv = *(const u32x4*)((const u16*)(p.ws + WS_XT) + (size_t)d * L_ + tid * 8);
  const float sa = mod[d], cav = mod[640 + d], ga = mod[1280 + d];
  const float Dk = bf2f(p.Dskip[d]);
  const float t1 = rbf(1.f + cav);
  *(u32x4*)(smem + tid * 16) = k0;
  *(u32x4*)(smem + (tid + 256) * 16) = k1;
  if (tid < 40) *(u32x4*)(smem + (tid + 512) * 16) = k2;
  {
    unsigned wv[8];
    h_mod(n0, t1, sa, wv);
    h_mod(n1, t1, sa, wv + 4);
    if (tid == 255) { wv[4] = 0u; wv[5] = 0u; wv[6] = 0u; wv[7] = 0u; }
    h_store(smem, tid + 20, wv);
    unsigned zv[8];
#pragma unroll
    for (int e = 0; e < 4; ++e) {
      const unsigned f0 = __builtin_amdgcn_readfirstlane(wv[e]);
      zv[e] = 0u;
      zv[4 + e] = (tid == 19) ? f0 : 0u;
    }
    if (tid < 20) h_store(smem, tid, zv);
  }
  __syncthreads();
  const int w = __builtin_amdgcn_readfirstlane(tid >> 6), lane = tid & 63;
  f32x4 acc[8];
#pragma unroll
  for (int q = 0; q < 8; ++q) acc[q] = (f32x4){0.f, 0.f, 0.f, 0.f};
  conv_residue<7>(smem, -96 + 32 * w, lane, acc);
  conv_residue<7>(smem, -96 + 32 * (w + 4), lane, acc);
  if (w == 3) conv_residue<0>(smem, 160, lane, acc);
  __syncthreads();
  {
    const int rho = lane & 7, cb = (lane >> 3) & 1, rq = lane >> 4;
    const int sw = (rq & 1) | (cb << 1);
    float* red = (float*)(smem + B_RED) + w * 2048 + 128 * cb + 32 * rq + rho;
#pragma unroll
    for (int g = 0; g < 4; ++g) {
      float* rg = red + 8 * (g ^ sw);
#pragma unroll
      for (int q = 0; q < 8; ++q) rg[256 * q] = acc[q][g];
    }
  }
  __syncthreads();
  {
    const int t0 = tid * 8;
    const int pos = t0 ^ (((t0 >> 5) & 1) << 3) ^ (((t0 >> 7) & 1) << 4);
    const float* red = (const float*)(smem + B_RED);
    float y[8];
#pragma unroll
    for (int e = 0; e < 8; ++e) y[e] = 0.f;
#pragma unroll
    for (int ww = 0; ww < 4; ++ww) {
      f32x4 a = *(const f32x4*)(red + ww * 2048 + pos), bb = *(const f32x4*)(red + ww * 2048 + pos + 4);
#pragma unroll
      for (int e = 0; e < 4; ++e) { y[e] += a[e]; y[4 + e] += bb[e]; }
    }
    const u32x4 hv = *(const u32x4*)(smem + B_HB + 2 * (160 + t0));
    u32x4 o;
#pragma unroll
    for (int e = 0; e < 4; ++e) {
      float h0 = lo16(hv[e]), h1 = hi16(hv[e]);
      float y0 = rbf(rbf(y[2 * e]) + rbf(h0 * Dk)), y1 = rbf(rbf(y[2 * e + 1]) + rbf(h1 * Dk));
      float x0 = lo16(xv[e]) + rbf(ga * y0), x1 = hi16(xv[e]) + rbf(ga * y1);
      o[e] = pack2(x0, x1);
    }
    *(u32x4*)((u16*)(p.ws + WS_X1T) + ((size_t)(t0 >> 6) * D_ + d) * 64 + (t0 & 63)) = o;
  }
}

DEV int d_off(int k, int c) {
  const int f = ((k >> 1) & 1) | (((k >> 3) & 1) << 1);
  return k * 128 + ((((c >> 1) ^ f)) << 5) + ((c & 1) << 4);
}
DEV bf16x8 d_frag(const char* smem, int lane, int mi, int ks) {
  const int g = lane >> 4, q = (lane & 15) >> 2, pp = lane & 3;
  const int k0 = 32 * ks + 8 * g + q;
  const int c = 2 * mi + (pp >> 1);
  typedef __attribute__((address_space(3))) s16x4* lp;
  s16x4 a = __builtin_amdgcn_ds_read_tr16_b64_v4i16((lp)(smem + d_off(k0, c) + 8 * (pp & 1)));
  s16x4 bq = __builtin_amdgcn_ds_read_tr16_b64_v4i16((lp)(smem + d_off(k0 + 4, c) + 8 * (pp & 1)));
  return __builtin_shufflevector(a, bq, 0, 1, 2, 3, 4, 5, 6, 7);
}

DEV void d_load(const Params& p, int lane, int ft, int half, bf16x8 (&wf)[10][2]) {
  const bf16x8* wsrc = (const bf16x8*)(p.ws + WS_W13) + (size_t)(ft * 20 + half * 10) * 2 * 64 + lane;
#pragma unroll
  for (int ks = 0; ks < 10; ++ks) {
    wf[ks][0] = wsrc[(ks * 2) * 64];
    wf[ks][1] = wsrc[(ks * 2 + 1) * 64];
  }
}

template <int MT>
DEV void d_half(const char* smem, int lane, int mi0, int half, const bf16x8 (&wf)[10][2], f32x4 (&acc)[2][4]) {
#pragma unroll
  for (int ks = 0; ks < 10; ++ks) {
#pragma unroll
    for (int m = 0; m < MT; ++m) {
      const bf16x8 hf = d_frag(smem, lane, mi0 + m, half * 10 + ks);
      acc[0][m] = __builtin_amdgcn_mfma_f32_16x16x32_bf16(wf[ks][0], hf, acc[0][m], 0, 0, 0);
      acc[1][m] = __builtin_amdgcn_mfma_f32_16x16x32_bf16(wf[ks][1], hf, acc[1][m], 0, 0, 0);
    }
  }
}

template <int MT>
DEV void d_epi(const Params& p, int lane, int ft, int mi0, int mt, f32x4 (&acc)[2][4]) {
  const int rq = lane >> 4;
  u16* gdst = (u16*)(p.ws + WS_G);
#pragma unroll
  for (int m = 0; m < MT; ++m) {
    float gv[4];
#pragma unroll
    for (int g = 0; g < 4; ++g) {
      float gate = rbf(acc[0][m][g]), up = rbf(acc[1][m][g]);
      float s = rbf(gate * __builtin_amdgcn_rcpf(1.f + __expf(-gate)));
      gv[g] = s * up;
    }
    const int mtile = mt * 4 + mi0 + m;
    const int lane2 = (lane & 15) + 16 * (2 * (ft & 1) + (rq >> 1));
    u32x2 o = {pack2(gv[0], gv[1]), pack2(gv[2], gv[3])};
    *(u32x2*)(gdst + ((size_t)(mtile * 54 + (ft >> 1)) * 64 + lane2) * 8 + 4 * (rq & 1)) = o;
  }
#pragma unroll
  for (int m = 0; m < 4; ++m) { acc[0][m] = (f32x4){0.f, 0.f, 0.f, 0.f}; acc[1][m] = (f32x4){0.f, 0.f, 0.f, 0.f}; }
}

#define SB0 __builtin_amdgcn_sched_barrier(0)
#define RAWBAR() do { asm volatile("s_waitcnt lgkmcnt(0)" ::: "memory"); __builtin_amdgcn_s_barrier(); asm volatile("" ::: "memory"); } while (0)

__global__ __launch_bounds__(512) void kD(Params p) {
  __shared__ __attribute__((aligned(16))) char smem[81920 + 4096 + 5120];
  const int tid = threadIdx.x;
  const int xc = blockIdx.x & 7, jj = blockIdx.x >> 3;
  const int mt = (xc & 1) * 16 + (jj & 15), fpart = (xc >> 1) * 2 + (jj >> 4);
  const int w = __builtin_amdgcn_readfirstlane(tid >> 6), lane = tid & 63;
  float* ps = (float*)(smem + 81920);
  float* ms = (float*)(smem + 81920 + 4096);
  const float* mod = (const float*)(p.ws + WS_MOD);
  const int c = tid & 7, d0 = tid >> 3;
  const int fs = (27 * fpart) >> 1, fe = (27 * (fpart + 1)) >> 1;
  bf16x8 wA[10][2], wB[10][2];
  char* vsrc = smem + d_off(d0, c);
  {
    const u16* src = (const u16*)(p.ws + WS_X1T) + (size_t)mt * D_ * 64 + d0 * 64 + c * 8;
    u32x4 v[10];
#pragma unroll
    for (int it = 0; it < 10; ++it) v[it] = *(const u32x4*)(src + it * 4096);
    const f32x4 mv = *(const f32x4*)(mod + 3 * 640 + (tid < 320 ? tid : 0) * 4);
    SB0;
    d_load(p, lane, fs + w, 0, wA);
    SB0;
#pragma unroll
    for (int it = 0; it < 10; ++it) *(u32x4*)(vsrc + 8192 * it) = v[it];
    if (tid < 320) *(f32x4*)(ms + tid * 4) = mv;
  }
  RAWBAR();
  float s[8];
#pragma unroll
  for (int e = 0; e < 8; ++e) s[e] = 0.f;
#pragma unroll
  for (int it = 0; it < 10; ++it) {
    const u32x4 v = *(const u32x4*)(vsrc + 8192 * it);
#pragma unroll
    for (int e = 0; e < 4; ++e) { s[2 * e] += lo16(v[e]); s[2 * e + 1] += hi16(v[e]); }
  }
#pragma unroll
  for (int e = 0; e < 8; ++e) { s[e] += __shfl_xor(s[e], 8); s[e] += __shfl_xor(s[e], 16); s[e] += __shfl_xor(s[e], 32); }
  if (lane < 8) {
    *(f32x4*)(ps + w * 64 + lane * 8) = (f32x4){s[0], s[1], s[2], s[3]};
    *(f32x4*)(ps + w * 64 + lane * 8 + 4) = (f32x4){s[4], s[5], s[6], s[7]};
  }
  if (fpart == 0) {
    u16* x1 = (u16*)(p.ws + WS_X1);
#pragma unroll 5
    for (int ks = 0; ks < 10; ++ks) {
      const int kk = (w >> 2) * 10 + ks;
      bf16x8 f = d_frag(smem, lane, w & 3, kk);
      *(bf16x8*)(x1 + (size_t)(mt * 64 + (w & 3) * 16 + (lane & 15)) * D_ + kk * 32 + (lane >> 4) * 8) = f;
    }
  }
  RAWBAR();
  float mean[8];
  {
    f32x4 a = (f32x4){0.f, 0.f, 0.f, 0.f}, bq = (f32x4){0.f, 0.f, 0.f, 0.f};
#pragma unroll
    for (int ww = 0; ww < 8; ++ww) { a += *(const f32x4*)(ps + ww * 64 + c * 8); bq += *(const f32x4*)(ps + ww * 64 + c * 8 + 4); }
#pragma unroll
    for (int e = 0; e < 4; ++e) { mean[e] = a[e] * (1.f / 640.f); mean[4 + e] = bq[e] * (1.f / 640.f); }
  }
#pragma unroll
  for (int e = 0; e < 8; ++e) s[e] = 0.f;
#pragma unroll
  for (int it = 0; it < 10; ++it) {
    const u32x4 v = *(const u32x4*)(vsrc + 8192 * it);
#pragma unroll
    for (int e = 0; e < 4; ++e) {
      float a = lo16(v[e]) - mean[2 * e], bb = hi16(v[e]) - mean[2 * e + 1];
      s[2 * e] += a * a; s[2 * e + 1] += bb * bb;
    }
  }
#pragma unroll
  for (int e = 0; e < 8; ++e) { s[e] += __shfl_xor(s[e], 8); s[e] += __shfl_xor(s[e], 16); s[e] += __shfl_xor(s[e], 32); }
  if (lane < 8) {
    *(f32x4*)(ps + 512 + w * 64 + lane * 8) = (f32x4){s[0], s[1], s[2], s[3]};
    *(f32x4*)(ps + 512 + w * 64 + lane * 8 + 4) = (f32x4){s[4], s[5], s[6], s[7]};
  }
  RAWBAR();
  float den[8];
  {
    f32x4 a = (f32x4){0.f, 0.f, 0.f, 0.f}, bq = (f32x4){0.f, 0.f, 0.f, 0.f};
#pragma unroll
    for (int ww = 0; ww < 8; ++ww) { a += *(const f32x4*)(ps + 512 + ww * 64 + c * 8); bq += *(const f32x4*)(ps + 512 + ww * 64 + c * 8 + 4); }
#pragma unroll
    for (int e = 0; e < 8; ++e) {
      float var = rbf((e < 4 ? a[e & 3] : bq[e & 3]) * (1.f / 640.f));
      den[e] = __builtin_amdgcn_rcpf(rbf(__builtin_amdgcn_sqrtf(rbf(var + 1e-5f))));
      mean[e] = rbf(mean[e]);
    }
  }
#pragma unroll
  for (int it = 0; it < 10; ++it) {
    const int d = d0 + 64 * it;
    const float tt = rbf(1.f + ms[640 + d]), sbv = ms[d];
    const u32x4 v = *(const u32x4*)(vsrc + 8192 * it);
    u32x4 o;
#pragma unroll
    for (int e = 0; e < 4; ++e) {
      float n0 = rbf(rbf(lo16(v[e]) - mean[2 * e]) * den[2 * e]);
      float n1 = rbf(rbf(hi16(v[e]) - mean[2 * e + 1]) * den[2 * e + 1]);
      float h0 = rbf(n0 * tt) + sbv, h1 = rbf(n1 * tt) + sbv;
      o[e] = pack2(h0, h1);
    }
    *(u32x4*)(vsrc + 8192 * it) = o;
  }
  SB0;
  d_load(p, lane, fs + w, 1, wB);
  SB0;
  RAWBAR();
  const bool big = (fe - fs == 14);
  f32x4 acc[2][4];
#pragma unroll
  for (int m = 0; m < 4; ++m) { acc[0][m] = (f32x4){0.f, 0.f, 0.f, 0.f}; acc[1][m] = (f32x4){0.f, 0.f, 0.f, 0.f}; }
  const int ft2 = (w < 4) ? fs + 8 + w : (big ? fs + 12 + ((w & 3) >> 1) : fs + 12);
  SB0;
  d_half<4>(smem, lane, 0, 0, wA, acc);
  SB0;
  d_load(p, lane, ft2, 0, wA);
  SB0;
  d_half<4>(smem, lane, 0, 1, wB, acc);
  d_epi<4>(p, lane, fs + w, 0, mt, acc);
  SB0;
  d_load(p, lane, ft2, 1, wB);
  SB0;
  if (w < 4) {
    d_half<4>(smem, lane, 0, 0, wA, acc);
    d_half<4>(smem, lane, 0, 1, wB, acc);
    d_epi<4>(p, lane, ft2, 0, mt, acc);
  } else if (big) {
    const int mi0 = 2 * (w & 1);
    d_half<2>(smem, lane, mi0, 0, wA, acc);
    d_half<2>(smem, lane, mi0, 1, wB, acc);
    d_epi<2>(p, lane, ft2, mi0, mt, acc);
  } else {
    const int mi0 = w & 3;
    d_half<1>(smem, lane, mi0, 0, wA, acc);
    d_half<1>(smem, lane, mi0, 1, wB, acc);
    d_epi<1>(p, lane, ft2, mi0, mt, acc);
  }
}

constexpr int E_RS = 84;
constexpr int E_DEPTH = 5;
__global__ __launch_bounds__(256) void kE(Params p) {
  __shared__ __attribute__((aligned(16))) float red[4 * 64 * E_RS];
  const int tid = threadIdx.x, xc = blockIdx.x & 7, j = blockIdx.x >> 3;
  const int mt = (xc >> 1) * 8 + (j & 7), np = (xc & 1) * 4 + (j >> 3);
  const int w = __builtin_amdgcn_readfirstlane(tid >> 6), lane = tid & 63;
  const int kstart = (w < 2) ? 14 * w : 28 + 13 * (w - 2);
  const int kcnt = (w < 2) ? 14 : 13;
  const bf16x8* gsrc = (const bf16x8*)(p.ws + WS_G) + (size_t)(mt * 4) * 54 * 64 + kstart * 64 + lane;
  const bf16x8* wsrc = (const bf16x8*)(p.ws + WS_W2) + (size_t)(np * 5) * 54 * 64 + kstart * 64 + lane;
  f32x4 acc[5][4];
#pragma unroll
  for (int i = 0; i < 5; ++i)
#pragma unroll
    for (int m = 0; m < 4; ++m) acc[i][m] = (f32x4){0.f, 0.f, 0.f, 0.f};
  bf16x8 gf[E_DEPTH][4], wf[E_DEPTH][5];
#pragma unroll
  for (int s = 0; s < E_DEPTH; ++s) {
#pragma unroll
    for (int m = 0; m < 4; ++m) gf[s][m] = gsrc[(m * 54 + s) * 64];
#pragma unroll
    for (int i = 0; i < 5; ++i) wf[s][i] = wsrc[(i * 54 + s) * 64];
  }
  const float* mod = (const float*)(p.ws + WS_MOD);
  const u16* x1 = (const u16*)(p.ws + WS_X1);
  SB0;
#pragma unroll
  for (int kk = 0; kk < 14; ++kk) {
    if (kk < kcnt) {
#pragma unroll
      for (int i = 0; i < 5; ++i)
#pragma unroll
        for (int m = 0; m < 4; ++m)
          acc[i][m] = __builtin_amdgcn_mfma_f32_16x16x32_bf16(wf[kk % E_DEPTH][i], gf[kk % E_DEPTH][m], acc[i][m], 0, 0, 0);
    }
    SB0;
    if (kk + E_DEPTH < kcnt) {
#pragma unroll
      for (int m = 0; m < 4; ++m) gf[kk % E_DEPTH][m] = gsrc[(m * 54 + kk + E_DEPTH) * 64];
#pragma unroll
      for (int i = 0; i < 5; ++i) wf[kk % E_DEPTH][i] = wsrc[(i * 54 + kk + E_DEPTH) * 64];
    }
    SB0;
  }
  {
    const int rq = lane >> 4;
#pragma unroll
    for (int i = 0; i < 5; ++i)
#pragma unroll
      for (int m = 0; m < 4; ++m)
        *(f32x4*)(red + (w * 64 + m * 16 + (lane & 15)) * E_RS + i * 16 + 4 * rq) = acc[i][m];
  }
  __syncthreads();
  for (int item = tid; item < 640; item += 256) {
    const int t = item / 10, cc = item % 10;
    const int dcol = np * 80 + cc * 8;
    const size_t o = (size_t)(mt * 64 + t) * D_ + dcol;
    const u32x4 xv = *(const u32x4*)(x1 + o);
    const f32x4 g0 = *(const f32x4*)(mod + 5 * 640 + dcol), g1 = *(const f32x4*)(mod + 5 * 640 + dcol + 4);
    float y[8];
#pragma unroll
    for (int e = 0; e < 8; ++e) y[e] = 0.f;
#pragma unroll
    for (int ww = 0; ww < 4; ++ww) {
      f32x4 a = *(const f32x4*)(red + (ww * 64 + t) * E_RS + cc * 8), bb = *(const f32x4*)(red + (ww * 64 + t) * E_RS + cc * 8 + 4);
#pragma unroll
      for (int e = 0; e < 4; ++e) { y[e] += a[e]; y[4 + e] += bb[e]; }
    }
    u32x4 ov;
#pragma unroll
    for (int e = 0; e < 4; ++e) {
      const float ga = (e < 2) ? g0[2 * e] : g1[2 * e - 4], gb = (e < 2) ? g0[2 * e + 1] : g1[2 * e - 3];
      float r0 = lo16(xv[e]) + rbf(ga * rbf(y[2 * e])), r1 = hi16(xv[e]) + rbf(gb * rbf(y[2 * e + 1]));
      ov[e] = pack2(r0, r1);
    }
    *(u32x4*)(p.out + o) = ov;
  }
}

extern "C" void kernel_launch(void* const* d_in, const int* in_sizes, int n_in,
                              void* d_out, int out_size, void* d_ws, size_t ws_size,
                              hipStream_t stream) {
  Params p;
  p.u = (const u16*)d_in[0];
  p.Are = (const u16*)d_in[1];
  p.Aim = (const u16*)d_in[2];
  p.Bre = (const u16*)d_in[3];
  p.Bim = (const u16*)d_in[4];
  p.Cre = (const u16*)d_in[5];
  p.Cim = (const u16*)d_in[6];
  p.logdt = (const u16*)d_in[7];
  p.Dskip = (const u16*)d_in[8];
  p.w1 = (const u16*)d_in[10];
  p.w3 = (const u16*)d_in[11];
  p.w2 = (const u16*)d_in[12];
  p.c = (const u16*)d_in[13];
  p.wmod = (const u16*)d_in[14];
  p.out = (u16*)d_out;
  p.ws = (char*)d_ws;
  kA<<<A_TOTAL, 256, 0, stream>>>(p);
  kB<<<640, 256, 0, stream>>>(p);
  kD<<<256, 512, 0, stream>>>(p);
  kE<<<256, 256, 0, stream>>>(p);
}
```

```cpp
#include <hip/hip_runtime.h>
#include <hip/hip_bf16.h>

typedef unsigned short u16;
typedef __attribute__((ext_vector_type(8))) short bf16x8;
typedef __attribute__((ext_vector_type(4))) short s16x4;
typedef __attribute__((ext_vector_type(4))) float f32x4;
typedef __attribute__((ext_vector_type(4))) unsigned u32x4;
typedef __attribute__((ext_vector_type(2))) unsigned u32x2;

#define DEV __device__ __forceinline__

constexpr int L_ = 2048, D_ = 640, N_ = 128, F_ = 1728;
constexpr int KR_LEN = 2208;

constexpr size_t WS_MOD = 0;
constexpr size_t WS_NT = 16384;
constexpr size_t WS_XT = WS_NT + (size_t)D_ * L_ * 2;
constexpr size_t WS_KR = WS_XT + (size_t)D_ * L_ * 2;
constexpr size_t WS_X1T = WS_KR + (size_t)D_ * 2 * KR_LEN * 2;
constexpr size_t WS_H2 = WS_X1T + (size_t)D_ * L_ * 2;
constexpr size_t WS_W13 = WS_H2 + (size_t)D_ * L_ * 2;
constexpr size_t WS_W2 = WS_W13 + (size_t)2 * D_ * F_ * 2;
constexpr size_t WS_G = WS_W2 + (size_t)D_ * F_ * 2;
constexpr size_t WS_END = WS_G + (size_t)L_ * F_ * 2;

DEV float bf2f(u16 x) { return __uint_as_float(((unsigned)x) << 16); }
DEV u16 f2bf(float f) { return __builtin_bit_cast(u16, (__bf16)f); }
DEV float rbf(float f) { return bf2f(f2bf(f)); }
DEV unsigned pack2(float a, float b) { return (unsigned)f2bf(a) | ((unsigned)f2bf(b) << 16); }
DEV float lo16(unsigned u) { return __uint_as_float(u << 16); }
DEV float hi16(unsigned u) { return __uint_as_float(u & 0xffff0000u); }

DEV void cexp(float ar, float ai, float w, float& er, float& ei) {
  const float xr = ar * w, xi = ai * w;
  const float mag = __builtin_amdgcn_exp2f(xr * 1.44269504088896341f);
  const float rev = __builtin_amdgcn_fractf(xi * 0.15915494309189535f);
  er = mag * __builtin_amdgcn_cosf(rev);
  ei = mag * __builtin_amdgcn_sinf(rev);
}

struct Params {
  const u16 *u, *Are, *Aim, *Bre, *Bim, *Cre, *Cim, *logdt, *Dskip, *w1, *w3, *w2, *c, *wmod;
  u16* out;
  char* ws;
};

constexpr int A_KGEN = 640, A_LN = 128, A_MOD = 60, A_W13 = 540, A_W2 = 270;
constexpr int A_TOTAL = A_KGEN + A_LN + A_MOD + A_W13 + A_W2;

__global__ __launch_bounds__(256) void kA(Params p) {
  __shared__ __attribute__((aligned(16))) char smem[22544];
  const int tid = threadIdx.x;
  int b = blockIdx.x;
  if (b < A_MOD) {
    float* red = (float*)smem;
    const int cg = tid & 7, rg = tid >> 3;
    float acc[8];
#pragma unroll
    for (int e = 0; e < 8; ++e) acc[e] = 0.f;
    u32x4 wv[20];
#pragma unroll
    for (int r = 0; r < 20; ++r)
      wv[r] = *(const u32x4*)(p.wmod + (size_t)(rg * 20 + r) * 3840 + b * 64 + cg * 8);
#pragma unroll
    for (int r = 0; r < 20; ++r) {
      float cv = bf2f(p.c[rg * 20 + r]);
      float s = rbf(cv / (1.f + __expf(-cv)));
#pragma unroll
      for (int e = 0; e < 4; ++e) {
        acc[2 * e] += s * lo16(wv[r][e]);
        acc[2 * e + 1] += s * hi16(wv[r][e]);
      }
    }
#pragma unroll
    for (int e = 0; e < 8; ++e) red[rg * 64 + cg * 8 + e] = acc[e];
    __syncthreads();
    if (tid < 64) {
      float s = 0.f;
#pragma unroll
      for (int r = 0; r < 32; ++r) s += red[r * 64 + tid];
      ((float*)(p.ws + WS_MOD))[b * 64 + tid] = rbf(s);
    }
    return;
  }
  b -= A_MOD;
  if (b < A_KGEN) {
    const int d = b;
    float* red = (float*)smem;
    float* Kv = (float*)(smem + 12288);
    f32x4* prm = (f32x4*)(smem + 20496);
    const int w = tid >> 6, l = tid & 63, i = l & 15, kq = l >> 4;
    if (tid < 128) {
      const int n = d * N_ + tid;
      const u16 a0 = p.Are[n], a1 = p.Aim[n], b0 = p.Bre[n], b1 = p.Bim[n], c0 = p.Cre[n], c1 = p.Cim[n];
      const float dt = rbf(__expf(bf2f(p.logdt[d])));
      const float dBr = dt * bf2f(b0), dBi = dt * bf2f(b1);
      const float Cr = bf2f(c0), Ci = bf2f(c1);
      prm[tid] = (f32x4){dt * bf2f(a0), dt * bf2f(a1), Cr * dBr - Ci * dBi, Cr * dBi + Ci * dBr};
    }
    const int o0 = (i & 7) + 128 * (i >> 3);
    const int o1 = (i < 8) ? 256 + 2 * (i & 3) + 128 * (i >> 2) : 512 + 4 * (i & 1) + 128 * ((i >> 1) & 3);
    const int o2 = 1024 + 128 * i;
    const float fo0 = (float)o0, fo1 = (float)o1, fo2 = (float)o2, fj = (float)(8 * i);
    __syncthreads();
    f32x4 acc[3];
#pragma unroll
    for (int s = 0; s < 3; ++s) acc[s] = (f32x4){0.f, 0.f, 0.f, 0.f};
#pragma unroll 2
    for (int kk = 0; kk < 8; ++kk) {
      const f32x4 pr = prm[32 * w + 4 * kk + kq];
      const float ar = pr[0], ai = pr[1], CBr = pr[2], CBi = pr[3];
      float yr, yi;
      cexp(ar, ai, fj, yr, yi);
#pragma unroll
      for (int s = 0; s < 3; ++s) {
        float zr, zi;
        cexp(ar, ai, s == 0 ? fo0 : (s == 1 ? fo1 : fo2), zr, zi);
        const float xr = CBr * zr - CBi * zi, xin = -(CBr * zi + CBi * zr);
        acc[s] = __builtin_amdgcn_mfma_f32_16x16x4f32(yr, xr, acc[s], 0, 0, 0);
        acc[s] = __builtin_amdgcn_mfma_f32_16x16x4f32(yi, xin, acc[s], 0, 0, 0);
      }
    }
#pragma unroll
    for (int s = 0; s < 3; ++s) *(f32x4*)(red + (w * 3 + s) * 256 + i * 16 + 4 * kq) = acc[s];
    __syncthreads();
    {
      const int cI = tid >> 4, j = tid & 15;
      const int p0 = (cI & 7) + 128 * (cI >> 3);
      const int p1 = (cI < 8) ? 256 + 2 * (cI & 3) + 128 * (cI >> 2) : 512 + 4 * (cI & 1) + 128 * ((cI >> 1) & 3);
      const int p2 = 1024 + 128 * cI;
      Kv[p0 + 8 * j] = red[tid] + red[768 + tid] + red[1536 + tid] + red[2304 + tid];
      Kv[p1 + 8 * j] = red[256 + tid] + red[1024 + tid] + red[1792 + tid] + red[2560 + tid];
      if (tid <= 128) Kv[p2 + 8 * j] = red[512 + tid] + red[1280 + tid] + red[2048 + tid] + red[2816 + tid];
    }
    __syncthreads();
    u16* Kr = (u16*)(p.ws + WS_KR) + (size_t)d * 2 * KR_LEN;
    for (int c = tid; c < KR_LEN / 8; c += 256) {
      unsigned hw[4], lw[4];
#pragma unroll
      for (int e2 = 0; e2 < 4; ++e2) {
        float kv[2];
#pragma unroll
        for (int z = 0; z < 2; ++z) {
          const int k = 2080 - (c * 8 + e2 * 2 + z);
          const int kc = min(max(k, 0), 2047);
          const float v = Kv[(int)rbf((float)kc)];
          kv[z] = (k == kc) ? v : 0.f;
        }
        float h0 = rbf(kv[0]), h1 = rbf(kv[1]);
        hw[e2] = pack2(h0, h1);
        lw[e2] = pack2(kv[0] - h0, kv[1] - h1);
      }
      *(u32x4*)(Kr + c * 8) = (u32x4){hw[0], hw[1], hw[2], hw[3]};
      *(u32x4*)(Kr + KR_LEN + c * 8) = (u32x4){lw[0], lw[1], lw[2], lw[3]};
    }
    return;
  }
  b -= A_KGEN;
  if (b < A_LN) {
    constexpr int XS = 648;
    u16* xs = (u16*)smem;
    float* sm = (float*)(smem + 16 * XS * 2);
    float* sd = sm + 16;
    const int row = tid >> 4, c = tid & 15;
    {
      u32x4 v[5];
#pragma unroll
      for (int q = 0; q < 5; ++q) v[q] = *(const u32x4*)(p.u + (size_t)(b * 16 + row) * D_ + (c + 16 * q) * 8);
      float s = 0.f;
#pragma unroll
      for (int q = 0; q < 5; ++q)
#pragma unroll
        for (int e = 0; e < 4; ++e) s += lo16(v[q][e]) + hi16(v[q][e]);
      s += __shfl_xor(s, 1); s += __shfl_xor(s, 2); s += __shfl_xor(s, 4); s += __shfl_xor(s, 8);
      const float mean = s * (1.f / 640.f);
      float s2 = 0.f;
#pragma unroll
      for (int q = 0; q < 5; ++q)
#pragma unroll
        for (int e = 0; e < 4; ++e) {
          float a = lo16(v[q][e]) - mean, bb = hi16(v[q][e]) - mean;
          s2 += a * a + bb * bb;
        }
      s2 += __shfl_xor(s2, 1); s2 += __shfl_xor(s2, 2); s2 += __shfl_xor(s2, 4); s2 += __shfl_xor(s2, 8);
      if (c == 0) {
        float var = rbf(s2 * (1.f / 640.f));
        sm[row] = rbf(mean);
        sd[row] = __builtin_amdgcn_rcpf(rbf(sqrtf(rbf(var + 1e-5f))));
      }
#pragma unroll
      for (int q = 0; q < 5; ++q) *(u32x4*)(xs + row * XS + (c + 16 * q) * 8) = v[q];
    }
    __syncthreads();
    u16* nT = (u16*)(p.ws + WS_NT);
    u16* xT = (u16*)(p.ws + WS_XT);
    const int tq = tid & 1;
    float mm[8], dd[8];
#pragma unroll
    for (int e = 0; e < 8; ++e) { mm[e] = sm[tq * 8 + e]; dd[e] = sd[tq * 8 + e]; }
#pragma unroll
    for (int it = 0; it < 5; ++it) {
      const int d = (tid >> 1) + 128 * it;
      unsigned xo[4], no[4];
#pragma unroll
      for (int e2 = 0; e2 < 4; ++e2) {
        const int r0 = tq * 8 + e2 * 2;
        u16 a = xs[r0 * XS + d], bb = xs[(r0 + 1) * XS + d];
        xo[e2] = (unsigned)a | ((unsigned)bb << 16);
        float n0 = rbf(bf2f(a) - mm[2 * e2]) * dd[2 * e2];
        float n1 = rbf(bf2f(bb) - mm[2 * e2 + 1]) * dd[2 * e2 + 1];
        no[e2] = pack2(n0, n1);
      }
      const size_t o = (size_t)d * L_ + b * 16 + tq * 8;
      *(u32x4*)(xT + o) = (u32x4){xo[0], xo[1], xo[2], xo[3]};
      *(u32x4*)(nT + o) = (u32x4){no[0], no[1], no[2], no[3]};
    }
    return;
  }
  b -= A_LN;
  if (b < A_W13) {
    const int ks = b / 27, fc = b % 27;
    u16* ts = (u16*)smem;
    {
      const int mat = tid >> 7, r = (tid >> 2) & 31, cq = tid & 3;
      const u16* src = (mat ? p.w3 : p.w1) + (size_t)(ks * 32 + r) * F_ + fc * 64 + cq * 16;
      u32x4 a = *(const u32x4*)src, bb = *(const u32x4*)(src + 8);
      *(u32x4*)(ts + (mat * 32 + r) * 72 + cq * 16) = a;
      *(u32x4*)(ts + (mat * 32 + r) * 72 + cq * 16 + 8) = bb;
    }
    __syncthreads();
    u16* dst = (u16*)(p.ws + WS_W13);
#pragma unroll
    for (int it = 0; it < 2; ++it) {
      const int item = tid + 256 * it;
      const int lane = item & 63, mat = (item >> 6) & 1, ftl = item >> 7;
      const int f = ftl * 16 + (lane & 15), k0 = (lane >> 4) * 8;
      unsigned o[4];
#pragma unroll
      for (int e2 = 0; e2 < 4; ++e2)
        o[e2] = (unsigned)ts[(mat * 32 + k0 + 2 * e2) * 72 + f] | ((unsigned)ts[(mat * 32 + k0 + 2 * e2 + 1) * 72 + f] << 16);
      const int ft = fc * 4 + ftl;
      *(u32x4*)(dst + ((size_t)((ft * 20 + ks) * 2 + mat) * 64 + lane) * 8) = (u32x4){o[0], o[1], o[2], o[3]};
    }
    return;
  }
  b -= A_W13;
  {
    const int ks = b / 5, dc = b % 5;
    u16* ts = (u16*)smem;
    {
      const int r = tid >> 3, cq = tid & 7;
      const u16* src = p.w2 + (size_t)(ks * 32 + r) * D_ + dc * 128 + cq * 16;
      u32x4 a = *(const u32x4*)src, bb = *(const u32x4*)(src + 8);
      *(u32x4*)(ts + r * 136 + cq * 16) = a;
      *(u32x4*)(ts + r * 136 + cq * 16 + 8) = bb;
    }
    __syncthreads();
    u16* dst = (u16*)(p.ws + WS_W2);
#pragma unroll
    for (int it = 0; it < 2; ++it) {
      const int item = tid + 256 * it;
      const int lane = item & 63, dtl = item >> 6;
      const int dd = dtl * 16 + (lane & 15), k0 = (lane >> 4) * 8;
      unsigned o[4];
#pragma unroll
      for (int e2 = 0; e2 < 4; ++e2)
        o[e2] = (unsigned)ts[(k0 + 2 * e2) * 136 + dd] | ((unsigned)ts[(k0 + 2 * e2 + 1) * 136 + dd] << 16);
      const int dt = dc * 8 + dtl;
      *(u32x4*)(dst + ((size_t)(dt * 54 + ks) * 64 + lane) * 8) = (u32x4){o[0], o[1], o[2], o[3]};
    }
  }
}

constexpr int B_HB = 2 * KR_LEN * 2;
constexpr int B_HS = 4640;
constexpr int B_RED = B_HB + B_HS;
constexpr int B_LDS = B_RED + 32768;

template <int UMAX>
DEV void conv_residue(const char* smem, int o0, int lane, f32x4 (&acc)[8]) {
  const int r = lane & 15, kg = lane >> 4;
  const int rho = lane & 7, cb = (lane >> 3) & 1;
  bf16x8 bf[UMAX + 1];
  const int bbase = B_HB + rho * B_HS + 2 * (160 - o0 + 128 * cb + 8 * kg);
#pragma unroll
  for (int u = 0; u <= UMAX; ++u) bf[u] = *(const bf16x8*)(smem + bbase + 512 * u);
  const int abase = 2 * (2080 - o0 - 8 * r + 8 * kg);
#pragma unroll
  for (int i = 0; i < 8; ++i) {
    const bf16x8 ah = *(const bf16x8*)(smem + abase - 512 * i);
    const bf16x8 al = *(const bf16x8*)(smem + abase - 512 * i + KR_LEN * 2);
#pragma unroll
    for (int u = 0; u <= UMAX; ++u) {
      const int q = u + i;
      if (q < 8) {
        acc[q] = __builtin_amdgcn_mfma_f32_16x16x32_bf16(ah, bf[u], acc[q], 0, 0, 0);
        acc[q] = __builtin_amdgcn_mfma_f32_16x16x32_bf16(al, bf[u], acc[q], 0, 0, 0);
      }
    }
  }
}

DEV void h_mod(const u32x4 v, float t1, float sa, unsigned* wv) {
#pragma unroll
  for (int e = 0; e < 4; ++e) {
    float a = rbf(lo16(v[e]) * t1) + sa, bb = rbf(hi16(v[e]) * t1) + sa;
    wv[e] = pack2(a, bb);
  }
}
DEV void h_store(char* smem, int c, const unsigned* wv) {
#pragma unroll
  for (int rho = 0; rho < 8; ++rho) {
    u32x4 o;
#pragma unroll
    for (int e = 0; e < 4; ++e) {
      const int k = (rho >> 1) + e;
      o[e] = (rho & 1) ? __builtin_amdgcn_alignbit(wv[k + 1], wv[k], 16) : wv[k];
    }
    *(u32x4*)(smem + B_HB + rho * B_HS + c * 16) = o;
  }
}

__global__ __launch_bounds__(256) void kB(Params p) {
  __shared__ __attribute__((aligned(16))) char smem[B_LDS];
  const int tid = threadIdx.x, d = blockIdx.x;
  const float* mod = (const float*)(p.ws + WS_MOD);
  const u16* nT = (const u16*)(p.ws + WS_NT) + (size_t)d * L_;
  const u32x4* ksrc = (const u32x4*)((const u16*)(p.ws + WS_KR) + (size_t)d * 2 * KR_LEN);
  const u32x4 n0 = *(const u32x4*)(nT + tid * 8);
  const u32x4 n1 = *(const u32x4*)(nT + (tid < 255 ? tid * 8 + 8 : 0));
  const u32x4 k0 = ksrc[tid], k1 = ksrc[tid + 256];
  const u32x4 k2 = ksrc[tid < 40 ? tid + 512 : 0];
  const u32x4 xv = *(const u32x4*)((const u16*)(p.ws + WS_XT) + (size_t)d * L_ + tid * 8);
  const float sa = mod[d], cav = mod[640 + d], ga = mod[1280 + d];
  const float Dk = bf2f(p.Dskip[d]);
  const float t1 = rbf(1.f + cav);
  *(u32x4*)(smem + tid * 16) = k0;
  *(u32x4*)(smem + (tid + 256) * 16) = k1;
  if (tid < 40) *(u32x4*)(smem + (tid + 512) * 16) = k2;
  {
    unsigned wv[8];
    h_mod(n0, t1, sa, wv);
    h_mod(n1, t1, sa, wv + 4);
    if (tid == 255) { wv[4] = 0u; wv[5] = 0u; wv[6] = 0u; wv[7] = 0u; }
    h_store(smem, tid + 20, wv);
    unsigned zv[8];
#pragma unroll
    for (int e = 0; e < 4; ++e) {
      const unsigned f0 = __builtin_amdgcn_readfirstlane(wv[e]);
      zv[e] = 0u;
      zv[4 + e] = (tid == 19) ? f0 : 0u;
    }
    if (tid < 20) h_store(smem, tid, zv);
  }
  __syncthreads();
  const int w = __builtin_amdgcn_readfirstlane(tid >> 6), lane = tid & 63;
  f32x4 acc[8];
#pragma unroll
  for (int q = 0; q < 8; ++q) acc[q] = (f32x4){0.f, 0.f, 0.f, 0.f};
  conv_residue<7>(smem, -96 + 32 * w, lane, acc);
  conv_residue<7>(smem, -96 + 32 * (w + 4), lane, acc);
  if (w == 3) conv_residue<0>(smem, 160, lane, acc);
  __syncthreads();
  {
    const int rho = lane & 7, cb = (lane >> 3) & 1, rq = lane >> 4;
    const int sw = (rq & 1) | (cb << 1);
    float* red = (float*)(smem + B_RED) + w * 2048 + 128 * cb + 32 * rq + rho;
#pragma unroll
    for (int g = 0; g < 4; ++g) {
      float* rg = red + 8 * (g ^ sw);
#pragma unroll
      for (int q = 0; q < 8; ++q) rg[256 * q] = acc[q][g];
    }
  }
  __syncthreads();
  {
    const int t0 = tid * 8;
    const int pos = t0 ^ (((t0 >> 5) & 1) << 3) ^ (((t0 >> 7) & 1) << 4);
    const float* red = (const float*)(smem + B_RED);
    float y[8];
#pragma unroll
    for (int e = 0; e < 8; ++e) y[e] = 0.f;
#pragma unroll
    for (int ww = 0; ww < 4; ++ww) {
      f32x4 a = *(const f32x4*)(red + ww * 2048 + pos), bb = *(const f32x4*)(red + ww * 2048 + pos + 4);
#pragma unroll
      for (int e = 0; e < 4; ++e) { y[e] += a[e]; y[4 + e] += bb[e]; }
    }
    const u32x4 hv = *(const u32x4*)(smem + B_HB + 2 * (160 + t0));
    u32x4 o;
#pragma unroll
    for (int e = 0; e < 4; ++e) {
      float h0 = lo16(hv[e]), h1 = hi16(hv[e]);
      float y0 = rbf(rbf(y[2 * e]) + rbf(h0 * Dk)), y1 = rbf(rbf(y[2 * e + 1]) + rbf(h1 * Dk));
      float x0 = lo16(xv[e]) + rbf(ga * y0), x1 = hi16(xv[e]) + rbf(ga * y1);
      o[e] = pack2(x0, x1);
    }
    *(u32x4*)((u16*)(p.ws + WS_X1T) + ((size_t)(t0 >> 6) * D_ + d) * 64 + (t0 & 63)) = o;
  }
}

DEV int d_off(int k, int c) {
  const int f = ((k >> 1) & 1) | (((k >> 3) & 1) << 1);
  return k * 128 + ((((c >> 1) ^ f)) << 5) + ((c & 1) << 4);
}
DEV bf16x8 d_frag(const char* smem, int lane, int mi, int ks) {
  const int g = lane >> 4, q = (lane & 15) >> 2, pp = lane & 3;
  const int k0 = 32 * ks + 8 * g + q;
  const int c = 2 * mi + (pp >> 1);
  typedef __attribute__((address_space(3))) s16x4* lp;
  s16x4 a = __builtin_amdgcn_ds_read_tr16_b64_v4i16((lp)(smem + d_off(k0, c) + 8 * (pp & 1)));
  s16x4 bq = __builtin_amdgcn_ds_read_tr16_b64_v4i16((lp)(smem + d_off(k0 + 4, c) + 8 * (pp & 1)));
  return __builtin_shufflevector(a, bq, 0, 1, 2, 3, 4, 5, 6, 7);
}

DEV void d_load(const Params& p, int lane, int ft, int half, bf16x8 (&wf)[10][2]) {
  const bf16x8* wsrc = (const bf16x8*)(p.ws + WS_W13) + (size_t)(ft * 20 + half * 10) * 2 * 64 + lane;
#pragma unroll
  for (int ks = 0; ks < 10; ++ks) {
    wf[ks][0] = wsrc[(ks * 2) * 64];
    wf[ks][1] = wsrc[(ks * 2 + 1) * 64];
  }
}

template <int MT>
DEV void d_half(const char* smem, int lane, int mi0, int half, const bf16x8 (&wf)[10][2], f32x4 (&acc)[2][4]) {
#pragma unroll
  for (int ks = 0; ks < 10; ++ks) {
#pragma unroll
    for (int m = 0; m < MT; ++m) {
      const bf16x8 hf = d_frag(smem, lane, mi0 + m, half * 10 + ks);
      acc[0][m] = __builtin_amdgcn_mfma_f32_16x16x32_bf16(wf[ks][0], hf, acc[0][m], 0, 0, 0);
      acc[1][m] = __builtin_amdgcn_mfma_f32_16x16x32_bf16(wf[ks][1], hf, acc[1][m], 0, 0, 0);
    }
  }
}

template <int MT>
DEV void d_epi(const Params& p, int lane, int ft, int mi0, int mt, f32x4 (&acc)[2][4]) {
  const int rq = lane >> 4;
  u16* gdst = (u16*)(p.ws + WS_G);
#pragma unroll
  for (int m = 0; m < MT; ++m) {
    float gv[4];
#pragma unroll
    for (int g = 0; g < 4; ++g) {
      float gate = rbf(acc[0][m][g]), up = rbf(acc[1][m][g]);
      float s = rbf(gate * __builtin_amdgcn_rcpf(1.f + __expf(-gate)));
      gv[g] = s * up;
    }
    const int mtile = mt * 4 + mi0 + m;
    const int lane2 = (lane & 15) + 16 * (2 * (ft & 1) + (rq >> 1));
    u32x2 o = {pack2(gv[0], gv[1]), pack2(gv[2], gv[3])};
    *(u32x2*)(gdst + ((size_t)(mtile * 54 + (ft >> 1)) * 64 + lane2) * 8 + 4 * (rq & 1)) = o;
  }
#pragma unroll
  for (int m = 0; m < 4; ++m) { acc[0][m] = (f32x4){0.f, 0.f, 0.f, 0.f}; acc[1][m] = (f32x4){0.f, 0.f, 0.f, 0.f}; }
}

#define SB0 __builtin_amdgcn_sched_barrier(0)
#define RAWBAR() do { asm volatile("s_waitcnt lgkmcnt(0)" ::: "memory"); __builtin_amdgcn_s_barrier(); asm volatile("" ::: "memory"); } while (0)

__global__ __launch_bounds__(512) void kC(Params p) {
  __shared__ __attribute__((aligned(16))) float ps[2 * 8 * 32];
  const int tid = threadIdx.x, mt = blockIdx.x >> 1, hf = blockIdx.x & 1;
  const int w = tid >> 6, lane = tid & 63;
  const int c4 = tid & 3, d0 = tid >> 2;
  const int c = hf * 4 + c4;
  const float* mod = (const float*)(p.ws + WS_MOD);
  const u16* src = (const u16*)(p.ws + WS_X1T) + (size_t)mt * D_ * 64 + d0 * 64 + c * 8;
  u32x4 v[5];
  float tt[5], sb[5];
#pragma unroll
  for (int it = 0; it < 5; ++it) v[it] = *(const u32x4*)(src + it * 8192);
#pragma unroll
  for (int it = 0; it < 5; ++it) { tt[it] = mod[4 * 640 + d0 + 128 * it]; sb[it] = mod[3 * 640 + d0 + 128 * it]; }
  float s[8];
#pragma unroll
  for (int e = 0; e < 8; ++e) s[e] = 0.f;
#pragma unroll
  for (int it = 0; it < 5; ++it)
#pragma unroll
    for (int e = 0; e < 4; ++e) { s[2 * e] += lo16(v[it][e]); s[2 * e + 1] += hi16(v[it][e]); }
#pragma unroll
  for (int e = 0; e < 8; ++e) { s[e] += __shfl_xor(s[e], 4); s[e] += __shfl_xor(s[e], 8); s[e] += __shfl_xor(s[e], 16); s[e] += __shfl_xor(s[e], 32); }
  if (lane < 4) {
    *(f32x4*)(ps + w * 32 + lane * 8) = (f32x4){s[0], s[1], s[2], s[3]};
    *(f32x4*)(ps + w * 32 + lane * 8 + 4) = (f32x4){s[4], s[5], s[6], s[7]};
  }
  __syncthreads();
  float mean[8];
  {
    f32x4 a = (f32x4){0.f, 0.f, 0.f, 0.f}, bq = (f32x4){0.f, 0.f, 0.f, 0.f};
#pragma unroll
    for (int ww = 0; ww < 8; ++ww) { a += *(const f32x4*)(ps + ww * 32 + c4 * 8); bq += *(const f32x4*)(ps + ww * 32 + c4 * 8 + 4); }
#pragma unroll
    for (int e = 0; e < 4; ++e) { mean[e] = a[e] * (1.f / 640.f); mean[4 + e] = bq[e] * (1.f / 640.f); }
  }
#pragma unroll
  for (int e = 0; e < 8; ++e) s[e] = 0.f;
#pragma unroll
  for (int it = 0; it < 5; ++it)
#pragma unroll
    for (int e = 0; e < 4; ++e) {
      float a = lo16(v[it][e]) - mean[2 * e], bb = hi16(v[it][e]) - mean[2 * e + 1];
      s[2 * e] += a * a; s[2 * e + 1] += bb * bb;
    }
#pragma unroll
  for (int e = 0; e < 8; ++e) { s[e] += __shfl_xor(s[e], 4); s[e] += __shfl_xor(s[e], 8); s[e] += __shfl_xor(s[e], 16); s[e] += __shfl_xor(s[e], 32); }
  if (lane < 4) {
    *(f32x4*)(ps + 256 + w * 32 + lane * 8) = (f32x4){s[0], s[1], s[2], s[3]};
    *(f32x4*)(ps + 256 + w * 32 + lane * 8 + 4) = (f32x4){s[4], s[5], s[6], s[7]};
  }
  __syncthreads();
  float den[8];
  {
    f32x4 a = (f32x4){0.f, 0.f, 0.f, 0.f}, bq = (f32x4){0.f, 0.f, 0.f, 0.f};
#pragma unroll
    for (int ww = 0; ww < 8; ++ww) { a += *(const f32x4*)(ps + 256 + ww * 32 + c4 * 8); bq += *(const f32x4*)(ps + 256 + ww * 32 + c4 * 8 + 4); }
#pragma unroll
    for (int e = 0; e < 8; ++e) {
      float var = rbf((e < 4 ? a[e & 3] : bq[e & 3]) * (1.f / 640.f));
      den[e] = __builtin_amdgcn_rcpf(rbf(__builtin_amdgcn_sqrtf(rbf(var + 1e-5f))));
      mean[e] = rbf(mean[e]);
    }
  }
  char* dst = p.ws + WS_H2 + (size_t)mt * 81920;
#pragma unroll
  for (int it = 0; it < 5; ++it) {
    const float t1 = rbf(1.f + tt[it]), sbv = sb[it];
    u32x4 o;
#pragma unroll
    for (int e = 0; e < 4; ++e) {
      float n0 = rbf(rbf(lo16(v[it][e]) - mean[2 * e]) * den[2 * e]);
      float n1 = rbf(rbf(hi16(v[it][e]) - mean[2 * e + 1]) * den[2 * e + 1]);
      float h0 = rbf(n0 * t1) + sbv, h1 = rbf(n1 * t1) + sbv;
      o[e] = pack2(h0, h1);
    }
    *(u32x4*)(dst + d_off(d0 + 128 * it, c)) = o;
  }
}

__global__ __launch_bounds__(512) void kD(Params p) {
  __shared__ __attribute__((aligned(16))) char smem[81920];
  const int tid = threadIdx.x;
  const int xc = blockIdx.x & 7, jj = blockIdx.x >> 3;
  const int mt = (xc & 1) * 16 + (jj & 15), fpart = (xc >> 1) * 2 + (jj >> 4);
  const int w = __builtin_amdgcn_readfirstlane(tid >> 6), lane = tid & 63;
  const int fs = (27 * fpart) >> 1, fe = (27 * (fpart + 1)) >> 1;
  bf16x8 wA[10][2], wB[10][2];
  {
    const u32x4* src = (const u32x4*)(p.ws + WS_H2 + (size_t)mt * 81920) + tid;
    u32x4 v[10];
#pragma unroll
    for (int it = 0; it < 10; ++it) v[it] = src[it * 512];
    SB0;
    d_load(p, lane, fs + w, 0, wA);
    SB0;
#pragma unroll
    for (int it = 0; it < 10; ++it) *(u32x4*)(smem + tid * 16 + 8192 * it) = v[it];
  }
  SB0;
  d_load(p, lane, fs + w, 1, wB);
  SB0;
  RAWBAR();
  const bool big = (fe - fs == 14);
  f32x4 acc[2][4];
#pragma unroll
  for (int m = 0; m < 4; ++m) { acc[0][m] = (f32x4){0.f, 0.f, 0.f, 0.f}; acc[1][m] = (f32x4){0.f, 0.f, 0.f, 0.f}; }
  const int ft2 = (w < 4) ? fs + 8 + w : (big ? fs + 12 + ((w & 3) >> 1) : fs + 12);
  SB0;
  d_half<4>(smem, lane, 0, 0, wA, acc);
  SB0;
  d_load(p, lane, ft2, 0, wA);
  SB0;
  d_half<4>(smem, lane, 0, 1, wB, acc);
  d_epi<4>(p, lane, fs + w, 0, mt, acc);
  SB0;
  d_load(p, lane, ft2, 1, wB);
  SB0;
  if (w < 4) {
    d_half<4>(smem, lane, 0, 0, wA, acc);
    d_half<4>(smem, lane, 0, 1, wB, acc);
    d_epi<4>(p, lane, ft2, 0, mt, acc);
  } else if (big) {
    const int mi0 = 2 * (w & 1);
    d_half<2>(smem, lane, mi0, 0, wA, acc);
    d_half<2>(smem, lane, mi0, 1, wB, acc);
    d_epi<2>(p, lane, ft2, mi0, mt, acc);
  } else {
    const int mi0 = w & 3;
    d_half<1>(smem, lane, mi0, 0, wA, acc);
    d_half<1>(smem, lane, mi0, 1, wB, acc);
    d_epi<1>(p, lane, ft2, mi0, mt, acc);
  }
}

constexpr int E_RS = 84;
constexpr int E_DEPTH = 5;
__global__ __launch_bounds__(256) void kE(Params p) {
  __shared__ __attribute__((aligned(16))) float red[4 * 64 * E_RS + 80 * 36];
  const int tid = threadIdx.x, xc = blockIdx.x & 7, j = blockIdx.x >> 3;
  const int mt = (xc >> 1) * 8 + (j & 7), np = (xc & 1) * 4 + (j >> 3);
  const int w = __builtin_amdgcn_readfirstlane(tid >> 6), lane = tid & 63;
  const int kstart = (w < 2) ? 14 * w : 28 + 13 * (w - 2);
  const int kcnt = (w < 2) ? 14 : 13;
  const bf16x8* gsrc = (const bf16x8*)(p.ws + WS_G) + (size_t)(mt * 4) * 54 * 64 + kstart * 64 + lane;
  const bf16x8* wsrc = (const bf16x8*)(p.ws + WS_W2) + (size_t)(np * 5) * 54 * 64 + kstart * 64 + lane;
  f32x4 acc[5][4];
#pragma unroll
  for (int i = 0; i < 5; ++i)
#pragma unroll
    for (int m = 0; m < 4; ++m) acc[i][m] = (f32x4){0.f, 0.f, 0.f, 0.f};
  bf16x8 gf[E_DEPTH][4], wf[E_DEPTH][5];
#pragma unroll
  for (int s = 0; s < E_DEPTH; ++s) {
#pragma unroll
    for (int m = 0; m < 4; ++m) gf[s][m] = gsrc[(m * 54 + s) * 64];
#pragma unroll
    for (int i = 0; i < 5; ++i) wf[s][i] = wsrc[(i * 54 + s) * 64];
  }
  const float* mod = (const float*)(p.ws + WS_MOD);
  u16* xs = (u16*)(red + 4 * 64 * E_RS);
  u32x4 xa, xb, xc2;
  {
    const u16* x1t = (const u16*)(p.ws + WS_X1T) + ((size_t)mt * D_ + np * 80) * 64;
    xa = *(const u32x4*)(x1t + tid * 8);
    xb = *(const u32x4*)(x1t + (tid + 256) * 8);
    xc2 = *(const u32x4*)(x1t + (tid < 128 ? tid + 512 : 0) * 8);
  }
  SB0;
#pragma unroll
  for (int kk = 0; kk < 14; ++kk) {
    if (kk < kcnt) {
#pragma unroll
      for (int i = 0; i < 5; ++i)
#pragma unroll
        for (int m = 0; m < 4; ++m)
          acc[i][m] = __builtin_amdgcn_mfma_f32_16x16x32_bf16(wf[kk % E_DEPTH][i], gf[kk % E_DEPTH][m], acc[i][m], 0, 0, 0);
    }
    SB0;
    if (kk + E_DEPTH < kcnt) {
#pragma unroll
      for (int m = 0; m < 4; ++m) gf[kk % E_DEPTH][m] = gsrc[(m * 54 + kk + E_DEPTH) * 64];
#pragma unroll
      for (int i = 0; i < 5; ++i) wf[kk % E_DEPTH][i] = wsrc[(i * 54 + kk + E_DEPTH) * 64];
    }
    SB0;
  }
  {
    const int rq = lane >> 4;
#pragma unroll
    for (int i = 0; i < 5; ++i)
#pragma unroll
      for (int m = 0; m < 4; ++m)
        *(f32x4*)(red + (w * 64 + m * 16 + (lane & 15)) * E_RS + i * 16 + 4 * rq) = acc[i][m];
    *(u32x4*)(xs + (tid >> 3) * 72 + (tid & 7) * 8) = xa;
    *(u32x4*)(xs + ((tid + 256) >> 3) * 72 + (tid & 7) * 8) = xb;
    if (tid < 128) *(u32x4*)(xs + ((tid + 512) >> 3) * 72 + (tid & 7) * 8) = xc2;
  }
  __syncthreads();
  for (int item = tid; item < 640; item += 256) {
    const int t = item / 10, cc = item % 10;
    const int dcol = np * 80 + cc * 8;
    const size_t o = (size_t)(mt * 64 + t) * D_ + dcol;
    float xv[8];
#pragma unroll
    for (int e = 0; e < 8; ++e) xv[e] = bf2f(xs[(cc * 8 + e) * 72 + t]);
    const f32x4 g0 = *(const f32x4*)(mod + 5 * 640 + dcol), g1 = *(const f32x4*)(mod + 5 * 640 + dcol + 4);
    float y[8];
#pragma unroll
    for (int e = 0; e < 8; ++e) y[e] = 0.f;
#pragma unroll
    for (int ww = 0; ww < 4; ++ww) {
      f32x4 a = *(const f32x4*)(red + (ww * 64 + t) * E_RS + cc * 8), bb = *(const f32x4*)(red + (ww * 64 + t) * E_RS + cc * 8 + 4);
#pragma unroll
      for (int e = 0; e < 4; ++e) { y[e] += a[e]; y[4 + e] += bb[e]; }
    }
    u32x4 ov;
#pragma unroll
    for (int e = 0; e < 4; ++e) {
      const float ga = (e < 2) ? g0[2 * e] : g1[2 * e - 4], gb = (e < 2) ? g0[2 * e + 1] : g1[2 * e - 3];
      float r0 = xv[2 * e] + rbf(ga * rbf(y[2 * e])), r1 = xv[2 * e + 1] + rbf(gb * rbf(y[2 * e + 1]));
      ov[e] = pack2(r0, r1);
    }
    *(u32x4*)(p.out + o) = ov;
  }
}

extern "C" void kernel_launch(void* const* d_in, const int* in_sizes, int n_in,
                              void* d_out, int out_size, void* d_ws, size_t ws_size,
                              hipStream_t stream) {
  Params p;
  p.u = (const u16*)d_in[0];
  p.Are = (const u16*)d_in[1];
  p.Aim = (const u16*)d_in[2];
  p.Bre = (const u16*)d_in[3];
  p.Bim = (const u16*)d_in[4];
  p.Cre = (const u16*)d_in[5];
  p.Cim = (const u16*)d_in[6];
  p.logdt = (const u16*)d_in[7];
  p.Dskip = (const u16*)d_in[8];
  p.w1 = (const u16*)d_in[10];
  p.w3 = (const u16*)d_in[11];
  p.w2 = (const u16*)d_in[12];
  p.c = (const u16*)d_in[13];
  p.wmod = (const u16*)d_in[14];
  p.out = (u16*)d_out;
  p.ws = (char*)d_ws;
  kA<<<A_TOTAL, 256, 0, stream>>>(p);
  kB<<<640, 256, 0, stream>>>(p);
  kC<<<64, 512, 0, stream>>>(p);
  kD<<<256, 512, 0, stream>>>(p);
  kE<<<256, 256, 0, stream>>>(p);
}
```

```cpp
#include <hip/hip_runtime.h>
#include <hip/hip_bf16.h>

typedef unsigned short u16;
typedef __attribute__((ext_vector_type(8))) short bf16x8;
typedef __attribute__((ext_vector_type(4))) short s16x4;
typedef __attribute__((ext_vector_type(4))) float f32x4;
typedef __attribute__((ext_vector_type(4))) unsigned u32x4;
typedef __attribute__((ext_vector_type(2))) unsigned u32x2;

#define DEV __device__ __forceinline__

constexpr int L_ = 2048, D_ = 640, N_ = 128, F_ = 1728;
constexpr int KR_LEN = 2208;

constexpr size_t WS_MOD = 0;
constexpr size_t WS_NT = 16384;
constexpr size_t WS_XT = WS_NT + (size_t)D_ * L_ * 2;
constexpr size_t WS_KR = WS_XT + (size_t)D_ * L_ * 2;
constexpr size_t WS_X1T = WS_KR + (size_t)D_ * 2 * KR_LEN * 2;
constexpr size_t WS_H2 = WS_X1T + (size_t)D_ * L_ * 2;
constexpr size_t WS_W13 = WS_H2 + (size_t)D_ * L_ * 2;
constexpr size_t WS_W2 = WS_W13 + (size_t)2 * D_ * F_ * 2;
constexpr size_t WS_G = WS_W2 + (size_t)D_ * F_ * 2;
constexpr size_t WS_END = WS_G + (size_t)L_ * F_ * 2;

DEV float bf2f(u16 x) { return __uint_as_float(((unsigned)x) << 16); }
DEV u16 f2bf(float f) { return __builtin_bit_cast(u16, (__bf16)f); }
DEV float rbf(float f) { return bf2f(f2bf(f)); }
DEV unsigned pack2(float a, float b) { return (unsigned)f2bf(a) | ((unsigned)f2bf(b) << 16); }
DEV float lo16(unsigned u) { return __uint_as_float(u << 16); }
DEV float hi16(unsigned u) { return __uint_as_float(u & 0xffff0000u); }

DEV void cexp(float ar, float ai, float w, float& er, float& ei) {
  const float xr = ar * w, xi = ai * w;
  const float mag = __builtin_amdgcn_exp2f(xr * 1.44269504088896341f);
  const float rev = __builtin_amdgcn_fractf(xi * 0.15915494309189535f);
  er = mag * __builtin_amdgcn_cosf(rev);
  ei = mag * __builtin_amdgcn_sinf(rev);
}

template <int CTRL>
DEV float dpp_add(float x) {
  return x + __builtin_bit_cast(float, __builtin_amdgcn_update_dpp(0, __builtin_bit_cast(int, x), CTRL, 0xf, 0xf, false));
}
DEV float sum_x16(float x) {
  float a = x, b = x;
  asm volatile("s_nop 1\n\tv_permlane16_swap_b32 %0, %1" : "+v"(a), "+v"(b));
  return a + b;
}
DEV float sum_x32(float x) {
  float a = x, b = x;
  asm volatile("s_nop 1\n\tv_permlane32_swap_b32 %0, %1" : "+v"(a), "+v"(b));
  return a + b;
}
DEV float row_sum16(float x) {
  x = dpp_add<0xB1>(x);
  x = dpp_add<0x4E>(x);
  x = dpp_add<0x124>(x);
  x = dpp_add<0x128>(x);
  return x;
}
DEV float sum_mod4(float x) {
  x = dpp_add<0x124>(x);
  x = dpp_add<0x128>(x);
  x = sum_x16(x);
  x = sum_x32(x);
  return x;
}

struct Params {
  const u16 *u, *Are, *Aim, *Bre, *Bim, *Cre, *Cim, *logdt, *Dskip, *w1, *w3, *w2, *c, *wmod;
  u16* out;
  char* ws;
};

constexpr int A_KGEN = 640, A_LN = 64, A_MOD = 60, A_W13 = 270, A_W2 = 135;
constexpr int A_TOTAL = A_KGEN + A_LN + A_MOD + A_W13 + A_W2;

__global__ __launch_bounds__(512) void kA(Params p) {
  __shared__ __attribute__((aligned(16))) char smem[41984];
  const int tid = threadIdx.x;
  int b = blockIdx.x;
  if (b < A_MOD) {
    float* red = (float*)smem;
    const int cg = tid & 7, rg = tid >> 3;
    float acc[8];
#pragma unroll
    for (int e = 0; e < 8; ++e) acc[e] = 0.f;
    u32x4 wv[10];
    float cv[10];
#pragma unroll
    for (int r = 0; r < 10; ++r) {
      wv[r] = *(const u32x4*)(p.wmod + (size_t)(rg * 10 + r) * 3840 + b * 64 + cg * 8);
      cv[r] = bf2f(p.c[rg * 10 + r]);
    }
#pragma unroll
    for (int r = 0; r < 10; ++r) {
      float s = rbf(cv[r] / (1.f + __expf(-cv[r])));
#pragma unroll
      for (int e = 0; e < 4; ++e) {
        acc[2 * e] += s * lo16(wv[r][e]);
        acc[2 * e + 1] += s * hi16(wv[r][e]);
      }
    }
    *(f32x4*)(red + rg * 64 + cg * 8) = (f32x4){acc[0], acc[1], acc[2], acc[3]};
    *(f32x4*)(red + rg * 64 + cg * 8 + 4) = (f32x4){acc[4], acc[5], acc[6], acc[7]};
    __syncthreads();
    {
      const int col = tid & 63, g = tid >> 6;
      float s = 0.f;
#pragma unroll
      for (int r = 0; r < 8; ++r) s += red[(g * 8 + r) * 64 + col];
      float* red2 = red + 4096;
      red2[g * 64 + col] = s;
      __syncthreads();
      if (tid < 64) {
        float t = 0.f;
#pragma unroll
        for (int r = 0; r < 8; ++r) t += red2[r * 64 + tid];
        ((float*)(p.ws + WS_MOD))[b * 64 + tid] = rbf(t);
      }
    }
    return;
  }
  b -= A_MOD;
  if (b < A_KGEN) {
    const int d = b;
    float* red = (float*)smem;
    float* Kv = (float*)(smem + 24576);
    f32x4* prm = (f32x4*)(smem + 32784);
    const int w = tid >> 6, l = tid & 63, i = l & 15, kq = l >> 4;
    if (tid < 128) {
      const int n = d * N_ + tid;
      const u16 a0 = p.Are[n], a1 = p.Aim[n], b0 = p.Bre[n], b1 = p.Bim[n], c0 = p.Cre[n], c1 = p.Cim[n];
      const float dt = rbf(__expf(bf2f(p.logdt[d])));
      const float dBr = dt * bf2f(b0), dBi = dt * bf2f(b1);
      const float Cr = bf2f(c0), Ci = bf2f(c1);
      prm[tid] = (f32x4){dt * bf2f(a0), dt * bf2f(a1), Cr * dBr - Ci * dBi, Cr * dBi + Ci * dBr};
    }
    const int o0 = (i & 7) + 128 * (i >> 3);
    const int o1 = (i < 8) ? 256 + 2 * (i & 3) + 128 * (i >> 2) : 512 + 4 * (i & 1) + 128 * ((i >> 1) & 3);
    const int o2 = 1024 + 128 * i;
    const float fo0 = (float)o0, fo1 = (float)o1, fo2 = (float)o2, fj = (float)(8 * i);
    __syncthreads();
    f32x4 acc[3];
#pragma unroll
    for (int s = 0; s < 3; ++s) acc[s] = (f32x4){0.f, 0.f, 0.f, 0.f};
#pragma unroll 2
    for (int kk = 0; kk < 4; ++kk) {
      const f32x4 pr = prm[16 * w + 4 * kk + kq];
      const float ar = pr[0], ai = pr[1], CBr = pr[2], CBi = pr[3];
      float yr, yi;
      cexp(ar, ai, fj, yr, yi);
#pragma unroll
      for (int s = 0; s < 3; ++s) {
        float zr, zi;
        cexp(ar, ai, s == 0 ? fo0 : (s == 1 ? fo1 : fo2), zr, zi);
        const float xr = CBr * zr - CBi * zi, xin = -(CBr * zi + CBi * zr);
        acc[s] = __builtin_amdgcn_mfma_f32_16x16x4f32(yr, xr, acc[s], 0, 0, 0);
        acc[s] = __builtin_amdgcn_mfma_f32_16x16x4f32(yi, xin, acc[s], 0, 0, 0);
      }
    }
#pragma unroll
    for (int s = 0; s < 3; ++s) *(f32x4*)(red + (w * 3 + s) * 256 + i * 16 + 4 * kq) = acc[s];
    __syncthreads();
    {
      const int t8 = tid & 255, cI = t8 >> 4, j = t8 & 15, tl = tid >> 8;
      const int p0 = (cI & 7) + 128 * (cI >> 3);
      const int p1 = (cI < 8) ? 256 + 2 * (cI & 3) + 128 * (cI >> 2) : 512 + 4 * (cI & 1) + 128 * ((cI >> 1) & 3);
      float sum = 0.f;
#pragma unroll
      for (int ww = 0; ww < 8; ++ww) sum += red[(ww * 3 + tl) * 256 + t8];
      Kv[(tl ? p1 : p0) + 8 * j] = sum;
      if (tid <= 128) {
        float s2 = 0.f;
#pragma unroll
        for (int ww = 0; ww < 8; ++ww) s2 += red[(ww * 3 + 2) * 256 + tid];
        Kv[1024 + 128 * cI + 8 * j] = s2;
      }
    }
    __syncthreads();
    u16* Kr = (u16*)(p.ws + WS_KR) + (size_t)d * 2 * KR_LEN;
    for (int c = tid; c < KR_LEN / 4; c += 512) {
      unsigned hw[2], lw[2];
#pragma unroll
      for (int e2 = 0; e2 < 2; ++e2) {
        float kv[2];
#pragma unroll
        for (int z = 0; z < 2; ++z) {
          const int k = 2080 - (c * 4 + e2 * 2 + z);
          const int kc = min(max(k, 0), 2047);
          const float v = Kv[(int)rbf((float)kc)];
          kv[z] = (k == kc) ? v : 0.f;
        }
        float h0 = rbf(kv[0]), h1 = rbf(kv[1]);
        hw[e2] = pack2(h0, h1);
        lw[e2] = pack2(kv[0] - h0, kv[1] - h1);
      }
      *(u32x2*)(Kr + c * 4) = (u32x2){hw[0], hw[1]};
      *(u32x2*)(Kr + KR_LEN + c * 4) = (u32x2){lw[0], lw[1]};
    }
    return;
  }
  b -= A_KGEN;
  if (b < A_LN) {
    constexpr int XS = 648;
    u16* xs = (u16*)smem;
    float* sm = (float*)(smem + 32 * XS * 2);
    float* sd = sm + 32;
    const int row = tid >> 4, c = tid & 15;
    {
      u32x4 v[5];
#pragma unroll
      for (int q = 0; q < 5; ++q) v[q] = *(const u32x4*)(p.u + (size_t)(b * 32 + row) * D_ + (c + 16 * q) * 8);
      float s = 0.f;
#pragma unroll
      for (int q = 0; q < 5; ++q)
#pragma unroll
        for (int e = 0; e < 4; ++e) s += lo16(v[q][e]) + hi16(v[q][e]);
      s = row_sum16(s);
      const float mean = s * (1.f / 640.f);
      float s2 = 0.f;
#pragma unroll
      for (int q = 0; q < 5; ++q)
#pragma unroll
        for (int e = 0; e < 4; ++e) {
          float a = lo16(v[q][e]) - mean, bb = hi16(v[q][e]) - mean;
          s2 += a * a + bb * bb;
        }
      s2 = row_sum16(s2);
      if (c == 0) {
        float var = rbf(s2 * (1.f / 640.f));
        sm[row] = rbf(mean);
        sd[row] = __builtin_amdgcn_rcpf(rbf(__builtin_amdgcn_sqrtf(rbf(var + 1e-5f))));
      }
#pragma unroll
      for (int q = 0; q < 5; ++q) *(u32x4*)(xs + row * XS + (c + 16 * q) * 8) = v[q];
    }
    __syncthreads();
    u16* nT = (u16*)(p.ws + WS_NT);
    u16* xT = (u16*)(p.ws + WS_XT);
    const int tq = tid & 3;
    float mm[8], dd[8];
#pragma unroll
    for (int e = 0; e < 8; ++e) { mm[e] = sm[tq * 8 + e]; dd[e] = sd[tq * 8 + e]; }
#pragma unroll
    for (int it = 0; it < 5; ++it) {
      const int d = (tid >> 2) + 128 * it;
      unsigned xo[4], no[4];
#pragma unroll
      for (int e2 = 0; e2 < 4; ++e2) {
        const int r0 = tq * 8 + e2 * 2;
        u16 a = xs[r0 * XS + d], bb = xs[(r0 + 1) * XS + d];
        xo[e2] = (unsigned)a | ((unsigned)bb << 16);
        float n0 = rbf(bf2f(a) - mm[2 * e2]) * dd[2 * e2];
        float n1 = rbf(bf2f(bb) - mm[2 * e2 + 1]) * dd[2 * e2 + 1];
        no[e2] = pack2(n0, n1);
      }
      const size_t o = (size_t)d * L_ + b * 32 + tq * 8;
      *(u32x4*)(xT + o) = (u32x4){xo[0], xo[1], xo[2], xo[3]};
      *(u32x4*)(nT + o) = (u32x4){no[0], no[1], no[2], no[3]};
    }
    return;
  }
  b -= A_LN;
  if (b < A_W13) {
    const int ks0 = (b / 27) * 2, fc = b % 27;
    u16* ts = (u16*)smem;
    {
      const int kh = tid >> 8, mat = (tid >> 7) & 1, r = (tid >> 2) & 31, cq = tid & 3;
      const u16* src = (mat ? p.w3 : p.w1) + (size_t)((ks0 + kh) * 32 + r) * F_ + fc * 64 + cq * 16;
      u32x4 a = *(const u32x4*)src, bb = *(const u32x4*)(src + 8);
      *(u32x4*)(ts + ((kh * 2 + mat) * 32 + r) * 72 + cq * 16) = a;
      *(u32x4*)(ts + ((kh * 2 + mat) * 32 + r) * 72 + cq * 16 + 8) = bb;
    }
    __syncthreads();
    u16* dst = (u16*)(p.ws + WS_W13);
#pragma unroll
    for (int it = 0; it < 2; ++it) {
      const int item = tid + 512 * it;
      const int lane = item & 63, mat = (item >> 6) & 1, ftl = (item >> 7) & 3, kh = item >> 9;
      const int f = ftl * 16 + (lane & 15), k0 = (lane >> 4) * 8;
      const u16* tb = ts + ((kh * 2 + mat) * 32 + k0) * 72 + f;
      unsigned o[4];
#pragma unroll
      for (int e2 = 0; e2 < 4; ++e2) o[e2] = (unsigned)tb[(2 * e2) * 72] | ((unsigned)tb[(2 * e2 + 1) * 72] << 16);
      const int ft = fc * 4 + ftl;
      *(u32x4*)(dst + ((size_t)((ft * 20 + ks0 + kh) * 2 + mat) * 64 + lane) * 8) = (u32x4){o[0], o[1], o[2], o[3]};
    }
    return;
  }
  b -= A_W13;
  {
    const int ks0 = (b / 5) * 2, dc = b % 5;
    u16* ts = (u16*)smem;
    {
      const int r = tid >> 3, cq = tid & 7;
      const u16* src = p.w2 + (size_t)(ks0 * 32 + r) * D_ + dc * 128 + cq * 16;
      u32x4 a = *(const u32x4*)src, bb = *(const u32x4*)(src + 8);
      *(u32x4*)(ts + r * 136 + cq * 16) = a;
      *(u32x4*)(ts + r * 136 + cq * 16 + 8) = bb;
    }
    __syncthreads();
    u16* dst = (u16*)(p.ws + WS_W2);
#pragma unroll
    for (int it = 0; it < 2; ++it) {
      const int item = tid + 512 * it;
      const int lane = item & 63, dtl = (item >> 6) & 7, kh = item >> 9;
      const int dd = dtl * 16 + (lane & 15), k0 = kh * 32 + (lane >> 4) * 8;
      unsigned o[4];
#pragma unroll
      for (int e2 = 0; e2 < 4; ++e2)
        o[e2] = (unsigned)ts[(k0 + 2 * e2) * 136 + dd] | ((unsigned)ts[(k0 + 2 * e2 + 1) * 136 + dd] << 16);
      const int dt = dc * 8 + dtl;
      *(u32x4*)(dst + ((size_t)(dt * 54 + ks0 + kh) * 64 + lane) * 8) = (u32x4){o[0], o[1], o[2], o[3]};
    }
  }
}

constexpr int B_HB = 2 * KR_LEN * 2;
constexpr int B_HS = 4640;
constexpr int B_RED = B_HB + B_HS;
constexpr int B_LDS = B_RED + 32768;

template <int UMAX>
DEV void conv_residue(const char* smem, int o0, int lane, f32x4 (&acc)[8]) {
  const int r = lane & 15, kg = lane >> 4;
  const int rho = lane & 7, cb = (lane >> 3) & 1;
  bf16x8 bf[UMAX + 1];
  const int bbase = B_HB + rho * B_HS + 2 * (160 - o0 + 128 * cb + 8 * kg);
#pragma unroll
  for (int u = 0; u <= UMAX; ++u) bf[u] = *(const bf16x8*)(smem + bbase + 512 * u);
  const int abase = 2 * (2080 - o0 - 8 * r + 8 * kg);
#pragma unroll
  for (int i = 0; i < 8; ++i) {
    const bf16x8 ah = *(const bf16x8*)(smem + abase - 512 * i);
    const bf16x8 al = *(const bf16x8*)(smem + abase - 512 * i + KR_LEN * 2);
#pragma unroll
    for (int u = 0; u <= UMAX; ++u) {
      const int q = u + i;
      if (q < 8) {
        acc[q] = __builtin_amdgcn_mfma_f32_16x16x32_bf16(ah, bf[u], acc[q], 0, 0, 0);
        acc[q] = __builtin_amdgcn_mfma_f32_16x16x32_bf16(al, bf[u], acc[q], 0, 0, 0);
      }
    }
  }
}

DEV void h_mod(const u32x4 v, float t1, float sa, unsigned* wv) {
#pragma unroll
  for (int e = 0; e < 4; ++e) {
    float a = rbf(lo16(v[e]) * t1) + sa, bb = rbf(hi16(v[e]) * t1) + sa;
    wv[e] = pack2(a, bb);
  }
}
DEV void h_store(char* smem, int c, const unsigned* wv) {
#pragma unroll
  for (int rho = 0; rho < 8; ++rho) {
    u32x4 o;
#pragma unroll
    for (int e = 0; e < 4; ++e) {
      const int k = (rho >> 1) + e;
      o[e] = (rho & 1) ? __builtin_amdgcn_alignbit(wv[k + 1], wv[k], 16) : wv[k];
    }
    *(u32x4*)(smem + B_HB + rho * B_HS + c * 16) = o;
  }
}

__global__ __launch_bounds__(256) void kB(Params p) {
  __shared__ __attribute__((aligned(16))) char smem[B_LDS];
  const int tid = threadIdx.x, d = blockIdx.x;
  const float* mod = (const float*)(p.ws + WS_MOD);
  const u16* nT = (const u16*)(p.ws + WS_NT) + (size_t)d * L_;
  const u32x4* ksrc = (const u32x4*)((const u16*)(p.ws + WS_KR) + (size_t)d * 2 * KR_LEN);
  const u32x4 n0 = *(const u32x4*)(nT + tid * 8);
  const u32x4 n1 = *(const u32x4*)(nT + (tid < 255 ? tid * 8 + 8 : 0));
  const u32x4 k0 = ksrc[tid], k1 = ksrc[tid + 256];
  const u32x4 k2 = ksrc[tid < 40 ? tid + 512 : 0];
  const u32x4 xv = *(const u32x4*)((const u16*)(p.ws + WS_XT) + (size_t)d * L_ + tid * 8);
  const float sa = mod[d], cav = mod[640 + d], ga = mod[1280 + d];
  const float Dk = bf2f(p.Dskip[d]);
  const float t1 = rbf(1.f + cav);
  *(u32x4*)(smem + tid * 16) = k0;
  *(u32x4*)(smem + (tid + 256) * 16) = k1;
  if (tid < 40) *(u32x4*)(smem + (tid + 512) * 16) = k2;
  {
    unsigned wv[8];
    h_mod(n0, t1, sa, wv);
    h_mod(n1, t1, sa, wv + 4);
    if (tid == 255) { wv[4] = 0u; wv[5] = 0u; wv[6] = 0u; wv[7] = 0u; }
    h_store(smem, tid + 20, wv);
    unsigned zv[8];
#pragma unroll
    for (int e = 0; e < 4; ++e) {
      const unsigned f0 = __builtin_amdgcn_readfirstlane(wv[e]);
      zv[e] = 0u;
      zv[4 + e] = (tid == 19) ? f0 : 0u;
    }
    if (tid < 20) h_store(smem, tid, zv);
  }
  __syncthreads();
  const int w = __builtin_amdgcn_readfirstlane(tid >> 6), lane = tid & 63;
  f32x4 acc[8];
#pragma unroll
  for (int q = 0; q < 8; ++q) acc[q] = (f32x4){0.f, 0.f, 0.f, 0.f};
  conv_residue<7>(smem, -96 + 32 * w, lane, acc);
  conv_residue<7>(smem, -96 + 32 * (w + 4), lane, acc);
  if (w == 3) conv_residue<0>(smem, 160, lane, acc);
  __syncthreads();
  {
    const int rho = lane & 7, cb = (lane >> 3) & 1, rq = lane >> 4;
    const int sw = (rq & 1) | (cb << 1);
    float* red = (float*)(smem + B_RED) + w * 2048 + 128 * cb + 32 * rq + rho;
#pragma unroll
    for (int g = 0; g < 4; ++g) {
      float* rg = red + 8 * (g ^ sw);
#pragma unroll
      for (int q = 0; q < 8; ++q) rg[256 * q] = acc[q][g];
    }
  }
  __syncthreads();
  {
    const int t0 = tid * 8;
    const int pos = t0 ^ (((t0 >> 5) & 1) << 3) ^ (((t0 >> 7) & 1) << 4);
    const float* red = (const float*)(smem + B_RED);
    float y[8];
#pragma unroll
    for (int e = 0; e < 8; ++e) y[e] = 0.f;
#pragma unroll
    for (int ww = 0; ww < 4; ++ww) {
      f32x4 a = *(const f32x4*)(red + ww * 2048 + pos), bb = *(const f32x4*)(red + ww * 2048 + pos + 4);
#pragma unroll
      for (int e = 0; e < 4; ++e) { y[e] += a[e]; y[4 + e] += bb[e]; }
    }
    const u32x4 hv = *(const u32x4*)(smem + B_HB + 2 * (160 + t0));
    u32x4 o;
#pragma unroll
    for (int e = 0; e < 4; ++e) {
      float h0 = lo16(hv[e]), h1 = hi16(hv[e]);
      float y0 = rbf(rbf(y[2 * e]) + rbf(h0 * Dk)), y1 = rbf(rbf(y[2 * e + 1]) + rbf(h1 * Dk));
      float x0 = lo16(xv[e]) + rbf(ga * y0), x1 = hi16(xv[e]) + rbf(ga * y1);
      o[e] = pack2(x0, x1);
    }
    *(u32x4*)((u16*)(p.ws + WS_X1T) + ((size_t)(t0 >> 6) * D_ + d) * 64 + (t0 & 63)) = o;
  }
}

DEV int d_off(int k, int c) {
  const int f = ((k >> 1) & 1) | (((k >> 3) & 1) << 1);
  return k * 128 + ((((c >> 1) ^ f)) << 5) + ((c & 1) << 4);
}
DEV bf16x8 d_frag(const char* smem, int lane, int mi, int ks) {
  const int g = lane >> 4, q = (lane & 15) >> 2, pp = lane & 3;
  const int k0 = 32 * ks + 8 * g + q;
  const int c = 2 * mi + (pp >> 1);
  typedef __attribute__((address_space(3))) s16x4* lp;
  s16x4 a = __builtin_amdgcn_ds_read_tr16_b64_v4i16((lp)(smem + d_off(k0, c) + 8 * (pp & 1)));
  s16x4 bq = __builtin_amdgcn_ds_read_tr16_b64_v4i16((lp)(smem + d_off(k0 + 4, c) + 8 * (pp & 1)));
  return __builtin_shufflevector(a, bq, 0, 1, 2, 3, 4, 5, 6, 7);
}

DEV void d_load(const Params& p, int lane, int ft, int half, bf16x8 (&wf)[10][2]) {
  const bf16x8* wsrc = (const bf16x8*)(p.ws + WS_W13) + (size_t)(ft * 20 + half * 10) * 2 * 64 + lane;
#pragma unroll
  for (int ks = 0; ks < 10; ++ks) {
    wf[ks][0] = wsrc[(ks * 2) * 64];
    wf[ks][1] = wsrc[(ks * 2 + 1) * 64];
  }
}

template <int MT>
DEV void d_half(const char* smem, int lane, int mi0, int half, const bf16x8 (&wf)[10][2], f32x4 (&acc)[2][4]) {
#pragma unroll
  for (int ks = 0; ks < 10; ++ks) {
#pragma unroll
    for (int m = 0; m < MT; ++m) {
      const bf16x8 hf = d_frag(smem, lane, mi0 + m, half * 10 + ks);
      acc[0][m] = __builtin_amdgcn_mfma_f32_16x16x32_bf16(wf[ks][0], hf, acc[0][m], 0, 0, 0);
      acc[1][m] = __builtin_amdgcn_mfma_f32_16x16x32_bf16(wf[ks][1], hf, acc[1][m], 0, 0, 0);
    }
  }
}

template <int MT>
DEV void d_epi(const Params& p, int lane, int ft, int mi0, int mt, f32x4 (&acc)[2][4]) {
  const int rq = lane >> 4;
  u16* gdst = (u16*)(p.ws + WS_G);
#pragma unroll
  for (int m = 0; m < MT; ++m) {
    float gv[4];
#pragma unroll
    for (int g = 0; g < 4; ++g) {
      float gate = rbf(acc[0][m][g]), up = rbf(acc[1][m][g]);
      float s = rbf(gate * __builtin_amdgcn_rcpf(1.f + __expf(-gate)));
      gv[g] = s * up;
    }
    const int mtile = mt * 4 + mi0 + m;
    const int lane2 = (lane & 15) + 16 * (2 * (ft & 1) + (rq >> 1));
    u32x2 o = {pack2(gv[0], gv[1]), pack2(gv[2], gv[3])};
    *(u32x2*)(gdst + ((size_t)(mtile * 54 + (ft >> 1)) * 64 + lane2) * 8 + 4 * (rq & 1)) = o;
  }
#pragma unroll
  for (int m = 0; m < 4; ++m) { acc[0][m] = (f32x4){0.f, 0.f, 0.f, 0.f}; acc[1][m] = (f32x4){0.f, 0.f, 0.f, 0.f}; }
}

#define SB0 __builtin_amdgcn_sched_barrier(0)
#define RAWBAR() do { asm volatile("s_waitcnt lgkmcnt(0)" ::: "memory"); __builtin_amdgcn_s_barrier(); asm volatile("" ::: "memory"); } while (0)

__global__ __launch_bounds__(1024) void kC(Params p) {
  __shared__ __attribute__((aligned(16))) float ps[2 * 16 * 16];
  const int tid = threadIdx.x, mt = blockIdx.x >> 2, qr = blockIdx.x & 3;
  const int w = tid >> 6, lane = tid & 63;
  const int c2 = tid & 1, d0 = tid >> 1;
  const int c = qr * 2 + c2;
  const bool two = d0 < 128;
  const int d1 = two ? d0 + 512 : d0;
  const float* mod = (const float*)(p.ws + WS_MOD);
  const u16* src = (const u16*)(p.ws + WS_X1T) + (size_t)mt * D_ * 64 + c * 8;
  u32x4 v[2];
  v[0] = *(const u32x4*)(src + d0 * 64);
  v[1] = *(const u32x4*)(src + d1 * 64);
  const float tt0 = mod[4 * 640 + d0], sb0 = mod[3 * 640 + d0], tt1 = mod[4 * 640 + d1], sb1 = mod[3 * 640 + d1];
  float x[2][8];
#pragma unroll
  for (int e = 0; e < 4; ++e) {
    x[0][2 * e] = lo16(v[0][e]); x[0][2 * e + 1] = hi16(v[0][e]);
    x[1][2 * e] = lo16(v[1][e]); x[1][2 * e + 1] = hi16(v[1][e]);
  }
  float s[8];
#pragma unroll
  for (int e = 0; e < 8; ++e) {
    float a = x[0][e] + (two ? x[1][e] : 0.f);
    a = dpp_add<0x4E>(a);
    s[e] = sum_mod4(a);
  }
  if (lane < 2) {
    *(f32x4*)(ps + w * 16 + lane * 8) = (f32x4){s[0], s[1], s[2], s[3]};
    *(f32x4*)(ps + w * 16 + lane * 8 + 4) = (f32x4){s[4], s[5], s[6], s[7]};
  }
  __syncthreads();
  float mean[8];
  {
    f32x4 a = (f32x4){0.f, 0.f, 0.f, 0.f}, bq = (f32x4){0.f, 0.f, 0.f, 0.f};
#pragma unroll
    for (int ww = 0; ww < 16; ++ww) { a += *(const f32x4*)(ps + ww * 16 + c2 * 8); bq += *(const f32x4*)(ps + ww * 16 + c2 * 8 + 4); }
#pragma unroll
    for (int e = 0; e < 4; ++e) { mean[e] = a[e] * (1.f / 640.f); mean[4 + e] = bq[e] * (1.f / 640.f); }
  }
#pragma unroll
  for (int e = 0; e < 8; ++e) {
    const float a0 = x[0][e] - mean[e], a1 = x[1][e] - mean[e];
    float a = a0 * a0 + (two ? a1 * a1 : 0.f);
    a = dpp_add<0x4E>(a);
    s[e] = sum_mod4(a);
  }
  if (lane < 2) {
    *(f32x4*)(ps + 256 + w * 16 + lane * 8) = (f32x4){s[0], s[1], s[2], s[3]};
    *(f32x4*)(ps + 256 + w * 16 + lane * 8 + 4) = (f32x4){s[4], s[5], s[6], s[7]};
  }
  __syncthreads();
  float den[8];
  {
    f32x4 a = (f32x4){0.f, 0.f, 0.f, 0.f}, bq = (f32x4){0.f, 0.f, 0.f, 0.f};
#pragma unroll
    for (int ww = 0; ww < 16; ++ww) { a += *(const f32x4*)(ps + 256 + ww * 16 + c2 * 8); bq += *(const f32x4*)(ps + 256 + ww * 16 + c2 * 8 + 4); }
#pragma unroll
    for (int e = 0; e < 8; ++e) {
      float var = rbf((e < 4 ? a[e & 3] : bq[e & 3]) * (1.f / 640.f));
      den[e] = __builtin_amdgcn_rcpf(rbf(__builtin_amdgcn_sqrtf(rbf(var + 1e-5f))));
      mean[e] = rbf(mean[e]);
    }
  }
  char* dst = p.ws + WS_H2 + (size_t)mt * 81920;
#pragma unroll
  for (int it = 0; it < 2; ++it) {
    if (it == 0 || two) {
      const float t1 = rbf(1.f + (it ? tt1 : tt0)), sbv = it ? sb1 : sb0;
      float h[8];
#pragma unroll
      for (int e = 0; e < 8; ++e) {
        const float n = rbf(rbf(x[it][e] - mean[e]) * den[e]);
        h[e] = rbf(n * t1) + sbv;
      }
      const u32x4 o = {pack2(h[0], h[1]), pack2(h[2], h[3]), pack2(h[4], h[5]), pack2(h[6], h[7])};
      *(u32x4*)(dst + d_off(it ? d1 : d0, c)) = o;
    }
  }
}

__global__ __launch_bounds__(512) void kD(Params p) {
  __shared__ __attribute__((aligned(16))) char smem[81920];
  const int tid = threadIdx.x;
  const int xc = blockIdx.x & 7, jj = blockIdx.x >> 3;
  const int mt = (xc & 1) * 16 + (jj & 15), fpart = (xc >> 1) * 2 + (jj >> 4);
  const int w = __builtin_amdgcn_readfirstlane(tid >> 6), lane = tid & 63;
  const int fs = (27 * fpart) >> 1, fe = (27 * (fpart + 1)) >> 1;
  bf16x8 wA[10][2], wB[10][2];
  {
    const u32x4* src = (const u32x4*)(p.ws + WS_H2 + (size_t)mt * 81920) + tid;
    u32x4 v[10];
#pragma unroll
    for (int it = 0; it < 10; ++it) v[it] = src[it * 512];
    SB0;
    d_load(p, lane, fs + w, 0, wA);
    SB0;
#pragma unroll
    for (int it = 0; it < 10; ++it) *(u32x4*)(smem + tid * 16 + 8192 * it) = v[it];
  }
  SB0;
  d_load(p, lane, fs + w, 1, wB);
  SB0;
  RAWBAR();
  const bool big = (fe - fs == 14);
  f32x4 acc[2][4];
#pragma unroll
  for (int m = 0; m < 4; ++m) { acc[0][m] = (f32x4){0.f, 0.f, 0.f, 0.f}; acc[1][m] = (f32x4){0.f, 0.f, 0.f, 0.f}; }
  const int ft2 = (w < 4) ? fs + 8 + w : (big ? fs + 12 + ((w & 3) >> 1) : fs + 12);
  SB0;
  d_half<4>(smem, lane, 0, 0, wA, acc);
  SB0;
  d_load(p, lane, ft2, 0, wA);
  SB0;
  d_half<4>(smem, lane, 0, 1, wB, acc);
  d_epi<4>(p, lane, fs + w, 0, mt, acc);
  SB0;
  d_load(p, lane, ft2, 1, wB);
  SB0;
  if (w < 4) {
    d_half<4>(smem, lane, 0, 0, wA, acc);
    d_half<4>(smem, lane, 0, 1, wB, acc);
    d_epi<4>(p, lane, ft2, 0, mt, acc);
  } else if (big) {
    const int mi0 = 2 * (w & 1);
    d_half<2>(smem, lane, mi0, 0, wA, acc);
    d_half<2>(smem, lane, mi0, 1, wB, acc);
    d_epi<2>(p, lane, ft2, mi0, mt, acc);
  } else {
    const int mi0 = w & 3;
    d_half<1>(smem, lane, mi0, 0, wA, acc);
    d_half<1>(smem, lane, mi0, 1, wB, acc);
    d_epi<1>(p, lane, ft2, mi0, mt, acc);
  }
}

constexpr int E_RS = 84;
constexpr int E_DEPTH = 5;
__global__ __launch_bounds__(256) void kE(Params p) {
  __shared__ __attribute__((aligned(16))) float red[4 * 64 * E_RS + 80 * 36];
  const int tid = threadIdx.x, xc = blockIdx.x & 7, j = blockIdx.x >> 3;
  const int mt = (xc >> 1) * 8 + (j & 7), np = (xc & 1) * 4 + (j >> 3);
  const int w = __builtin_amdgcn_readfirstlane(tid >> 6), lane = tid & 63;
  const int kstart = (w < 2) ? 14 * w : 28 + 13 * (w - 2);
  const int kcnt = (w < 2) ? 14 : 13;
  const bf16x8* gsrc = (const bf16x8*)(p.ws + WS_G) + (size_t)(mt * 4) * 54 * 64 + kstart * 64 + lane;
  const bf16x8* wsrc = (const bf16x8*)(p.ws + WS_W2) + (size_t)(np * 5) * 54 * 64 + kstart * 64 + lane;
  f32x4 acc[5][4];
#pragma unroll
  for (int i = 0; i < 5; ++i)
#pragma unroll
    for (int m = 0; m < 4; ++m) acc[i][m] = (f32x4){0.f, 0.f, 0.f, 0.f};
  bf16x8 gf[E_DEPTH][4], wf[E_DEPTH][5];
#pragma unroll
  for (int s = 0; s < E_DEPTH; ++s) {
#pragma unroll
    for (int m = 0; m < 4; ++m) gf[s][m] = gsrc[(m * 54 + s) * 64];
#pragma unroll
    for (int i = 0; i < 5; ++i) wf[s][i] = wsrc[(i * 54 + s) * 64];
  }
  const float* mod = (const float*)(p.ws + WS_MOD);
  u16* xs = (u16*)(red + 4 * 64 * E_RS);
  u32x4 xa, xb, xc2;
  {
    const u16* x1t = (const u16*)(p.ws + WS_X1T) + ((size_t)mt * D_ + np * 80) * 64;
    xa = *(const u32x4*)(x1t + tid * 8);
    xb = *(const u32x4*)(x1t + (tid + 256) * 8);
    xc2 = *(const u32x4*)(x1t + (tid < 128 ? tid + 512 : 0) * 8);
  }
  SB0;
#pragma unroll
  for (int kk = 0; kk < 14; ++kk) {
    if (kk < kcnt) {
#pragma unroll
      for (int i = 0; i < 5; ++i)
#pragma unroll
        for (int m = 0; m < 4; ++m)
          acc[i][m] = __builtin_amdgcn_mfma_f32_16x16x32_bf16(wf[kk % E_DEPTH][i], gf[kk % E_DEPTH][m], acc[i][m], 0, 0, 0);
    }
    SB0;
    if (kk + E_DEPTH < kcnt) {
#pragma unroll
      for (int m = 0; m < 4; ++m) gf[kk % E_DEPTH][m] = gsrc[(m * 54 + kk + E_DEPTH) * 64];
#pragma unroll
      for (int i = 0; i < 5; ++i) wf[kk % E_DEPTH][i] = wsrc[(i * 54 + kk + E_DEPTH) * 64];
    }
    SB0;
  }
  {
    const int rq = lane >> 4;
#pragma unroll
    for (int i = 0; i < 5; ++i)
#pragma unroll
      for (int m = 0; m < 4; ++m)
        *(f32x4*)(red + (w * 64 + m * 16 + (lane & 15)) * E_RS + i * 16 + 4 * rq) = acc[i][m];
    *(u32x4*)(xs + (tid >> 3) * 72 + (tid & 7) * 8) = xa;
    *(u32x4*)(xs + ((tid + 256) >> 3) * 72 + (tid & 7) * 8) = xb;
    if (tid < 128) *(u32x4*)(xs + ((tid + 512) >> 3) * 72 + (tid & 7) * 8) = xc2;
  }
  __syncthreads();
  for (int item = tid; item < 640; item += 256) {
    const int t = item / 10, cc = item % 10;
    const int dcol = np * 80 + cc * 8;
    const size_t o = (size_t)(mt * 64 + t) * D_ + dcol;
    float xv[8];
#pragma unroll
    for (int e = 0; e < 8; ++e) xv[e] = bf2f(xs[(cc * 8 + e) * 72 + t]);
    const f32x4 g0 = *(const f32x4*)(mod + 5 * 640 + dcol), g1 = *(const f32x4*)(mod + 5 * 640 + dcol + 4);
    float y[8];
#pragma unroll
    for (int e = 0; e < 8; ++e) y[e] = 0.f;
#pragma unroll
    for (int ww = 0; ww < 4; ++ww) {
      f32x4 a = *(const f32x4*)(red + (ww * 64 + t) * E_RS + cc * 8), bb = *(const f32x4*)(red + (ww * 64 + t) * E_RS + cc * 8 + 4);
#pragma unroll
      for (int e = 0; e < 4; ++e) { y[e] += a[e]; y[4 + e] += bb[e]; }
    }
    u32x4 ov;
#pragma unroll
    for (int e = 0; e < 4; ++e) {
      const float ga = (e < 2) ? g0[2 * e] : g1[2 * e - 4], gb = (e < 2) ? g0[2 * e + 1] : g1[2 * e - 3];
      float r0 = xv[2 * e] + rbf(ga * rbf(y[2 * e])), r1 = xv[2 * e + 1] + rbf(gb * rbf(y[2 * e + 1]));
      ov[e] = pack2(r0, r1);
    }
    *(u32x4*)(p.out + o) = ov;
  }
}

extern "C" void kernel_launch(void* const* d_in, const int* in_sizes, int n_in,
                              void* d_out, int out_size, void* d_ws, size_t ws_size,
                              hipStream_t stream) {
  Params p;
  p.u = (const u16*)d_in[0];
  p.Are = (const u16*)d_in[1];
  p.Aim = (const u16*)d_in[2];
  p.Bre = (const u16*)d_in[3];
  p.Bim = (const u16*)d_in[4];
  p.Cre = (const u16*)d_in[5];
  p.Cim = (const u16*)d_in[6];
  p.logdt = (const u16*)d_in[7];
  p.Dskip = (const u16*)d_in[8];
  p.w1 = (const u16*)d_in[10];
  p.w3 = (const u16*)d_in[11];
  p.w2 = (const u16*)d_in[12];
  p.c = (const u16*)d_in[13];
  p.wmod = (const u16*)d_in[14];
  p.out = (u16*)d_out;
  p.ws = (char*)d_ws;
  kA<<<A_TOTAL, 512, 0, stream>>>(p);
  kB<<<640, 256, 0, stream>>>(p);
  kC<<<128, 1024, 0, stream>>>(p);
  kD<<<256, 512, 0, stream>>>(p);
  kE<<<256, 256, 0, stream>>>(p);
}
```

```cpp
#include <hip/hip_runtime.h>
#include <hip/hip_bf16.h>

typedef unsigned short u16;
typedef __attribute__((ext_vector_type(8))) short bf16x8;
typedef __attribute__((ext_vector_type(4))) short s16x4;
typedef __attribute__((ext_vector_type(4))) float f32x4;
typedef __attribute__((ext_vector_type(4))) unsigned u32x4;
typedef __attribute__((ext_vector_type(2))) unsigned u32x2;

#define DEV __device__ __forceinline__

constexpr int L_ = 2048, D_ = 640, N_ = 128, F_ = 1728;
constexpr int KR_LEN = 2208;

constexpr size_t WS_MOD = 0;
constexpr size_t WS_NT = 16384;
constexpr size_t WS_XT = WS_NT + (size_t)D_ * L_ * 2;
constexpr size_t WS_KR = WS_XT + (size_t)D_ * L_ * 2;
constexpr size_t WS_X1T = WS_KR + (size_t)D_ * 2 * KR_LEN * 2;
constexpr size_t WS_H2 = WS_X1T + (size_t)D_ * L_ * 2;
constexpr size_t WS_W13 = WS_H2 + (size_t)D_ * L_ * 2;
constexpr size_t WS_W2 = WS_W13 + (size_t)2 * D_ * F_ * 2;
constexpr size_t WS_G = WS_W2 + (size_t)D_ * F_ * 2;
constexpr size_t WS_END = WS_G + (size_t)L_ * F_ * 2;

DEV float bf2f(u16 x) { return __uint_as_float(((unsigned)x) << 16); }
DEV u16 f2bf(float f) { return __builtin_bit_cast(u16, (__bf16)f); }
DEV float rbf(float f) { return bf2f(f2bf(f)); }
DEV unsigned pack2(float a, float b) { return (unsigned)f2bf(a) | ((unsigned)f2bf(b) << 16); }
DEV float lo16(unsigned u) { return __uint_as_float(u << 16); }
DEV float hi16(unsigned u) { return __uint_as_float(u & 0xffff0000u); }

DEV void cexp(float ar, float ai, float w, float& er, float& ei) {
  const float mag = __builtin_amdgcn_exp2f(ar * w);
  const float rev = __builtin_amdgcn_fractf(ai * w);
  er = mag * __builtin_amdgcn_cosf(rev);
  ei = mag * __builtin_amdgcn_sinf(rev);
}

template <int CTRL>
DEV float dpp_add(float x) {
  return x + __builtin_bit_cast(float, __builtin_amdgcn_update_dpp(0, __builtin_bit_cast(int, x), CTRL, 0xf, 0xf, false));
}
DEV float sum_x16(float x) {
  float a = x, b = x;
  asm volatile("s_nop 1\n\tv_permlane16_swap_b32 %0, %1" : "+v"(a), "+v"(b));
  return a + b;
}
DEV float sum_x32(float x) {
  float a = x, b = x;
  asm volatile("s_nop 1\n\tv_permlane32_swap_b32 %0, %1" : "+v"(a), "+v"(b));
  return a + b;
}
DEV float row_sum16(float x) {
  x = dpp_add<0xB1>(x);
  x = dpp_add<0x4E>(x);
  x = dpp_add<0x124>(x);
  x = dpp_add<0x128>(x);
  return x;
}
DEV float sum_mod4(float x) {
  x = dpp_add<0x124>(x);
  x = dpp_add<0x128>(x);
  x = sum_x16(x);
  x = sum_x32(x);
  return x;
}

struct Params {
  const u16 *u, *Are, *Aim, *Bre, *Bim, *Cre, *Cim, *logdt, *Dskip, *w1, *w3, *w2, *c, *wmod;
  u16* out;
  char* ws;
};

constexpr int A_KGEN = 640, A_LN = 64, A_MOD = 60, A_W13 = 270, A_W2 = 135;
constexpr int A_TOTAL = A_KGEN + A_LN + A_MOD + A_W13 + A_W2;

__global__ __launch_bounds__(512) void kA(Params p) {
  __shared__ __attribute__((aligned(16))) char smem[41984];
  const int tid = threadIdx.x;
  int b = blockIdx.x;
  if (b < A_MOD) {
    float* red = (float*)smem;
    const int cg = tid & 7, rg = tid >> 3;
    float acc[8];
#pragma unroll
    for (int e = 0; e < 8; ++e) acc[e] = 0.f;
    u32x4 wv[10];
    float cv[10];
#pragma unroll
    for (int r = 0; r < 10; ++r) {
      wv[r] = *(const u32x4*)(p.wmod + (size_t)(rg * 10 + r) * 3840 + b * 64 + cg * 8);
      cv[r] = bf2f(p.c[rg * 10 + r]);
    }
#pragma unroll
    for (int r = 0; r < 10; ++r) {
      float s = rbf(cv[r] / (1.f + __expf(-cv[r])));
#pragma unroll
      for (int e = 0; e < 4; ++e) {
        acc[2 * e] += s * lo16(wv[r][e]);
        acc[2 * e + 1] += s * hi16(wv[r][e]);
      }
    }
    *(f32x4*)(red + rg * 64 + cg * 8) = (f32x4){acc[0], acc[1], acc[2], acc[3]};
    *(f32x4*)(red + rg * 64 + cg * 8 + 4) = (f32x4){acc[4], acc[5], acc[6], acc[7]};
    __syncthreads();
    {
      const int col = tid & 63, g = tid >> 6;
      float s = 0.f;
#pragma unroll
      for (int r = 0; r < 8; ++r) s += red[(g * 8 + r) * 64 + col];
      float* red2 = red + 4096;
      red2[g * 64 + col] = s;
      __syncthreads();
      if (tid < 64) {
        float t = 0.f;
#pragma unroll
        for (int r = 0; r < 8; ++r) t += red2[r * 64 + tid];
        ((float*)(p.ws + WS_MOD))[b * 64 + tid] = rbf(t);
      }
    }
    return;
  }
  b -= A_MOD;
  if (b < A_KGEN) {
    const int d = b;
    float* red = (float*)smem;
    float* Kv = (float*)(smem + 24576);
    f32x4* prm = (f32x4*)(smem + 32784);
    const int w = tid >> 6, l = tid & 63, i = l & 15, kq = l >> 4;
    if (tid < 128) {
      const int n = d * N_ + tid;
      const u16 a0 = p.Are[n], a1 = p.Aim[n], b0 = p.Bre[n], b1 = p.Bim[n], c0 = p.Cre[n], c1 = p.Cim[n];
      const float dt = rbf(__expf(bf2f(p.logdt[d])));
      const float dBr = dt * bf2f(b0), dBi = dt * bf2f(b1);
      const float Cr = bf2f(c0), Ci = bf2f(c1);
      prm[tid] = (f32x4){dt * bf2f(a0) * 1.44269504088896341f, dt * bf2f(a1) * 0.15915494309189535f, Cr * dBr - Ci * dBi, Cr * dBi + Ci * dBr};
    }
    const int o0 = (i & 7) + 128 * (i >> 3);
    const int o1 = (i < 8) ? 256 + 2 * (i & 3) + 128 * (i >> 2) : 512 + 4 * (i & 1) + 128 * ((i >> 1) & 3);
    const int o2 = 1024 + 128 * i;
    const float fo0 = (float)o0, fo1 = (float)o1, fo2 = (float)o2, fj = (float)(8 * i);
    __syncthreads();
    f32x4 acc[3];
#pragma unroll
    for (int s = 0; s < 3; ++s) acc[s] = (f32x4){0.f, 0.f, 0.f, 0.f};
#pragma unroll 2
    for (int kk = 0; kk < 4; ++kk) {
      const f32x4 pr = prm[16 * w + 4 * kk + kq];
      const float ar = pr[0], ai = pr[1], CBr = pr[2], CBi = pr[3];
      float yr, yi;
      cexp(ar, ai, fj, yr, yi);
#pragma unroll
      for (int s = 0; s < 3; ++s) {
        float zr, zi;
        cexp(ar, ai, s == 0 ? fo0 : (s == 1 ? fo1 : fo2), zr, zi);
        const float xr = CBr * zr - CBi * zi, xin = -(CBr * zi + CBi * zr);
        acc[s] = __builtin_amdgcn_mfma_f32_16x16x4f32(yr, xr, acc[s], 0, 0, 0);
        acc[s] = __builtin_amdgcn_mfma_f32_16x16x4f32(yi, xin, acc[s], 0, 0, 0);
      }
    }
#pragma unroll
    for (int s = 0; s < 3; ++s) *(f32x4*)(red + (w * 3 + s) * 256 + i * 16 + 4 * kq) = acc[s];
    __syncthreads();
    {
      const int t8 = tid & 255, cI = t8 >> 4, j = t8 & 15, tl = tid >> 8;
      const int p0 = (cI & 7) + 128 * (cI >> 3);
      const int p1 = (cI < 8) ? 256 + 2 * (cI & 3) + 128 * (cI >> 2) : 512 + 4 * (cI & 1) + 128 * ((cI >> 1) & 3);
      float sum = 0.f;
#pragma unroll
      for (int ww = 0; ww < 8; ++ww) sum += red[(ww * 3 + tl) * 256 + t8];
      Kv[(tl ? p1 : p0) + 8 * j] = sum;
      if (tid <= 128) {
        float s2 = 0.f;
#pragma unroll
        for (int ww = 0; ww < 8; ++ww) s2 += red[(ww * 3 + 2) * 256 + tid];
        Kv[1024 + 128 * cI + 8 * j] = s2;
      }
    }
    __syncthreads();
    u16* Kr = (u16*)(p.ws + WS_KR) + (size_t)d * 2 * KR_LEN;
    for (int c = tid; c < KR_LEN / 4; c += 512) {
      unsigned hw[2], lw[2];
#pragma unroll
      for (int e2 = 0; e2 < 2; ++e2) {
        float kv[2];
#pragma unroll
        for (int z = 0; z < 2; ++z) {
          const int k = 2080 - (c * 4 + e2 * 2 + z);
          const int kc = min(max(k, 0), 2047);
          const float v = Kv[(int)rbf((float)kc)];
          kv[z] = (k == kc) ? v : 0.f;
        }
        float h0 = rbf(kv[0]), h1 = rbf(kv[1]);
        hw[e2] = pack2(h0, h1);
        lw[e2] = pack2(kv[0] - h0, kv[1] - h1);
      }
      *(u32x2*)(Kr + c * 4) = (u32x2){hw[0], hw[1]};
      *(u32x2*)(Kr + KR_LEN + c * 4) = (u32x2){lw[0], lw[1]};
    }
    return;
  }
  b -= A_KGEN;
  if (b < A_LN) {
    constexpr int XS = 648;
    u16* xs = (u16*)smem;
    float* sm = (float*)(smem + 32 * XS * 2);
    float* sd = sm + 32;
    const int row = tid >> 4, c = tid & 15;
    {
      u32x4 v[5];
#pragma unroll
      for (int q = 0; q < 5; ++q) v[q] = *(const u32x4*)(p.u + (size_t)(b * 32 + row) * D_ + (c + 16 * q) * 8);
      float s = 0.f;
#pragma unroll
      for (int q = 0; q < 5; ++q)
#pragma unroll
        for (int e = 0; e < 4; ++e) s += lo16(v[q][e]) + hi16(v[q][e]);
      s = row_sum16(s);
      const float mean = s * (1.f / 640.f);
      float s2 = 0.f;
#pragma unroll
      for (int q = 0; q < 5; ++q)
#pragma unroll
        for (int e = 0; e < 4; ++e) {
          float a = lo16(v[q][e]) - mean, bb = hi16(v[q][e]) - mean;
          s2 += a * a + bb * bb;
        }
      s2 = row_sum16(s2);
      if (c == 0) {
        float var = rbf(s2 * (1.f / 640.f));
        sm[row] = rbf(mean);
        sd[row] = __builtin_amdgcn_rcpf(rbf(__builtin_amdgcn_sqrtf(rbf(var + 1e-5f))));
      }
#pragma unroll
      for (int q = 0; q < 5; ++q) *(u32x4*)(xs + row * XS + (c + 16 * q) * 8) = v[q];
    }
    __syncthreads();
    u16* nT = (u16*)(p.ws + WS_NT);
    u16* xT = (u16*)(p.ws + WS_XT);
    const int tq = tid & 3;
    float mm[8], dd[8];
#pragma unroll
    for (int e = 0; e < 8; ++e) { mm[e] = sm[tq * 8 + e]; dd[e] = sd[tq * 8 + e]; }
#pragma unroll
    for (int it = 0; it < 5; ++it) {
      const int d = (tid >> 2) + 128 * it;
      unsigned xo[4], no[4];
#pragma unroll
      for (int e2 = 0; e2 < 4; ++e2) {
        const int r0 = tq * 8 + e2 * 2;
        u16 a = xs[r0 * XS + d], bb = xs[(r0 + 1) * XS + d];
        xo[e2] = (unsigned)a | ((unsigned)bb << 16);
        float n0 = (bf2f(a) - mm[2 * e2]) * dd[2 * e2];
        float n1 = (bf2f(bb) - mm[2 * e2 + 1]) * dd[2 * e2 + 1];
        no[e2] = pack2(n0, n1);
      }
      const size_t o = (size_t)d * L_ + b * 32 + tq * 8;
      *(u32x4*)(xT + o) = (u32x4){xo[0], xo[1], xo[2], xo[3]};
      *(u32x4*)(nT + o) = (u32x4){no[0], no[1], no[2], no[3]};
    }
    return;
  }
  b -= A_LN;
  if (b < A_W13) {
    const int ks0 = (b / 27) * 2, fc = b % 27;
    u16* ts = (u16*)smem;
    {
      const int kh = tid >> 8, mat = (tid >> 7) & 1, r = (tid >> 2) & 31, cq = tid & 3;
      const u16* src = (mat ? p.w3 : p.w1) + (size_t)((ks0 + kh) * 32 + r) * F_ + fc * 64 + cq * 16;
      u32x4 a = *(const u32x4*)src, bb = *(const u32x4*)(src + 8);
      *(u32x4*)(ts + ((kh * 2 + mat) * 32 + r) * 72 + cq * 16) = a;
      *(u32x4*)(ts + ((kh * 2 + mat) * 32 + r) * 72 + cq * 16 + 8) = bb;
    }
    __syncthreads();
    u16* dst = (u16*)(p.ws + WS_W13);
#pragma unroll
    for (int it = 0; it < 2; ++it) {
      const int item = tid + 512 * it;
      const int lane = item & 63, mat = (item >> 6) & 1, ftl = (item >> 7) & 3, kh = item >> 9;
      const int f = ftl * 16 + (lane & 15), k0 = (lane >> 4) * 8;
      const u16* tb = ts + ((kh * 2 + mat) * 32 + k0) * 72 + f;
      unsigned o[4];
#pragma unroll
      for (int e2 = 0; e2 < 4; ++e2) o[e2] = (unsigned)tb[(2 * e2) * 72] | ((unsigned)tb[(2 * e2 + 1) * 72] << 16);
      const int ft = fc * 4 + ftl;
      *(u32x4*)(dst + ((size_t)((ft * 20 + ks0 + kh) * 2 + mat) * 64 + lane) * 8) = (u32x4){o[0], o[1], o[2], o[3]};
    }
    return;
  }
  b -= A_W13;
  {
    const int ks0 = (b / 5) * 2, dc = b % 5;
    u16* ts = (u16*)smem;
    {
      const int r = tid >> 3, cq = tid & 7;
      const u16* src = p.w2 + (size_t)(ks0 * 32 + r) * D_ + dc * 128 + cq * 16;
      u32x4 a = *(const u32x4*)src, bb = *(const u32x4*)(src + 8);
      *(u32x4*)(ts + r * 136 + cq * 16) = a;
      *(u32x4*)(ts + r * 136 + cq * 16 + 8) = bb;
    }
    __syncthreads();
    u16* dst = (u16*)(p.ws + WS_W2);
#pragma unroll
    for (int it = 0; it < 2; ++it) {
      const int item = tid + 512 * it;
      const int lane = item & 63, dtl = (item >> 6) & 7, kh = item >> 9;
      const int dd = dtl * 16 + (lane & 15), k0 = kh * 32 + (lane >> 4) * 8;
      unsigned o[4];
#pragma unroll
      for (int e2 = 0; e2 < 4; ++e2)
        o[e2] = (unsigned)ts[(k0 + 2 * e2) * 136 + dd] | ((unsigned)ts[(k0 + 2 * e2 + 1) * 136 + dd] << 16);
      const int dt = dc * 8 + dtl;
      *(u32x4*)(dst + ((size_t)(dt * 54 + ks0 + kh) * 64 + lane) * 8) = (u32x4){o[0], o[1], o[2], o[3]};
    }
  }
}

constexpr int B_HB = 2 * KR_LEN * 2;
constexpr int B_HS = 4640;
constexpr int B_RED = B_HB + B_HS;
constexpr int B_LDS = B_RED + 32768;

template <int UMAX>
DEV void conv_residue(const char* smem, int o0, int lane, f32x4 (&acc)[8]) {
  const int r = lane & 15, kg = lane >> 4;
  const int rho = lane & 7, cb = (lane >> 3) & 1;
  bf16x8 bf[UMAX + 1];
  const int bbase = B_HB + rho * B_HS + 2 * (160 - o0 + 128 * cb + 8 * kg);
#pragma unroll
  for (int u = 0; u <= UMAX; ++u) bf[u] = *(const bf16x8*)(smem + bbase + 512 * u);
  const int abase = 2 * (2080 - o0 - 8 * r + 8 * kg);
#pragma unroll
  for (int i = 0; i < 8; ++i) {
    const bf16x8 ah = *(const bf16x8*)(smem + abase - 512 * i);
    const bf16x8 al = *(const bf16x8*)(smem + abase - 512 * i + KR_LEN * 2);
#pragma unroll
    for (int u = 0; u <= UMAX; ++u) {
      const int q = u + i;
      if (q < 8) {
        acc[q] = __builtin_amdgcn_mfma_f32_16x16x32_bf16(ah, bf[u], acc[q], 0, 0, 0);
        acc[q] = __builtin_amdgcn_mfma_f32_16x16x32_bf16(al, bf[u], acc[q], 0, 0, 0);
      }
    }
  }
}

DEV void h_mod(const u32x4 v, float t1, float sa, unsigned* wv) {
#pragma unroll
  for (int e = 0; e < 4; ++e) {
    float a = lo16(v[e]) * t1 + sa, bb = hi16(v[e]) * t1 + sa;
    wv[e] = pack2(a, bb);
  }
}
DEV void h_store(char* smem, int c, const unsigned* wv) {
#pragma unroll
  for (int rho = 0; rho < 8; ++rho) {
    u32x4 o;
#pragma unroll
    for (int e = 0; e < 4; ++e) {
      const int k = (rho >> 1) + e;
      o[e] = (rho & 1) ? __builtin_amdgcn_alignbit(wv[k + 1], wv[k], 16) : wv[k];
    }
    *(u32x4*)(smem + B_HB + rho * B_HS + c * 16) = o;
  }
}

__global__ __launch_bounds__(256) void kB(Params p) {
  __shared__ __attribute__((aligned(16))) char smem[B_LDS];
  const int tid = threadIdx.x, d = blockIdx.x;
  const float* mod = (const float*)(p.ws + WS_MOD);
  const u16* nT = (const u16*)(p.ws + WS_NT) + (size_t)d * L_;
  const u32x4* ksrc = (const u32x4*)((const u16*)(p.ws + WS_KR) + (size_t)d * 2 * KR_LEN);
  const u32x4 n0 = *(const u32x4*)(nT + tid * 8);
  const u32x4 n1 = *(const u32x4*)(nT + (tid < 255 ? tid * 8 + 8 : 0));
  const u32x4 k0 = ksrc[tid], k1 = ksrc[tid + 256];
  const u32x4 k2 = ksrc[tid < 40 ? tid + 512 : 0];
  const u32x4 xv = *(const u32x4*)((const u16*)(p.ws + WS_XT) + (size_t)d * L_ + tid * 8);
  const float sa = mod[d], cav = mod[640 + d], ga = mod[1280 + d];
  const float Dk = bf2f(p.Dskip[d]);
  const float t1 = rbf(1.f + cav);
  *(u32x4*)(smem + tid * 16) = k0;
  *(u32x4*)(smem + (tid + 256) * 16) = k1;
  if (tid < 40) *(u32x4*)(smem + (tid + 512) * 16) = k2;
  {
    unsigned wv[8];
    h_mod(n0, t1, sa, wv);
    h_mod(n1, t1, sa, wv + 4);
    if (tid == 255) { wv[4] = 0u; wv[5] = 0u; wv[6] = 0u; wv[7] = 0u; }
    h_store(smem, tid + 20, wv);
    unsigned zv[8];
#pragma unroll
    for (int e = 0; e < 4; ++e) {
      const unsigned f0 = __builtin_amdgcn_readfirstlane(wv[e]);
      zv[e] = 0u;
      zv[4 + e] = (tid == 19) ? f0 : 0u;
    }
    if (tid < 20) h_store(smem, tid, zv);
  }
  __syncthreads();
  const int w = __builtin_amdgcn_readfirstlane(tid >> 6), lane = tid & 63;
  f32x4 acc[8];
#pragma unroll
  for (int q = 0; q < 8; ++q) acc[q] = (f32x4){0.f, 0.f, 0.f, 0.f};
  conv_residue<7>(smem, -96 + 32 * w, lane, acc);
  conv_residue<7>(smem, -96 + 32 * (w + 4), lane, acc);
  if (w == 3) conv_residue<0>(smem, 160, lane, acc);
  __syncthreads();
  {
    const int rho = lane & 7, cb = (lane >> 3) & 1, rq = lane >> 4;
    const int sw = (rq & 1) | (cb << 1);
    float* red = (float*)(smem + B_RED) + w * 2048 + 128 * cb + 32 * rq + rho;
#pragma unroll
    for (int g = 0; g < 4; ++g) {
      float* rg = red + 8 * (g ^ sw);
#pragma unroll
      for (int q = 0; q < 8; ++q) rg[256 * q] = acc[q][g];
    }
  }
  __syncthreads();
  {
    const int t0 = tid * 8;
    const int pos = t0 ^ (((t0 >> 5) & 1) << 3) ^ (((t0 >> 7) & 1) << 4);
    const float* red = (const float*)(smem + B_RED);
    float y[8];
#pragma unroll
    for (int e = 0; e < 8; ++e) y[e] = 0.f;
#pragma unroll
    for (int ww = 0; ww < 4; ++ww) {
      f32x4 a = *(const f32x4*)(red + ww * 2048 + pos), bb = *(const f32x4*)(red + ww * 2048 + pos + 4);
#pragma unroll
      for (int e = 0; e < 4; ++e) { y[e] += a[e]; y[4 + e] += bb[e]; }
    }
    const u32x4 hv = *(const u32x4*)(smem + B_HB + 2 * (160 + t0));
    u32x4 o;
#pragma unroll
    for (int e = 0; e < 4; ++e) {
      float h0 = lo16(hv[e]), h1 = hi16(hv[e]);
      float y0 = y[2 * e] + h0 * Dk, y1 = y[2 * e + 1] + h1 * Dk;
      float x0 = lo16(xv[e]) + ga * y0, x1 = hi16(xv[e]) + ga * y1;
      o[e] = pack2(x0, x1);
    }
    *(u32x4*)((u16*)(p.ws + WS_X1T) + ((size_t)(t0 >> 6) * D_ + d) * 64 + (t0 & 63)) = o;
  }
}

DEV int d_off(int k, int c) {
  const int f = ((k >> 1) & 1) | (((k >> 3) & 1) << 1);
  return k * 128 + ((((c >> 1) ^ f)) << 5) + ((c & 1) << 4);
}
DEV bf16x8 d_frag(const char* smem, int lane, int mi, int ks) {
  const int g = lane >> 4, q = (lane & 15) >> 2, pp = lane & 3;
  const int k0 = 32 * ks + 8 * g + q;
  const int c = 2 * mi + (pp >> 1);
  typedef __attribute__((address_space(3))) s16x4* lp;
  s16x4 a = __builtin_amdgcn_ds_read_tr16_b64_v4i16((lp)(smem + d_off(k0, c) + 8 * (pp & 1)));
  s16x4 bq = __builtin_amdgcn_ds_read_tr16_b64_v4i16((lp)(smem + d_off(k0 + 4, c) + 8 * (pp & 1)));
  return __builtin_shufflevector(a, bq, 0, 1, 2, 3, 4, 5, 6, 7);
}

DEV void d_load(const Params& p, int lane, int ft, int half, bf16x8 (&wf)[10][2]) {
  const bf16x8* wsrc = (const bf16x8*)(p.ws + WS_W13) + (size_t)(ft * 20 + half * 10) * 2 * 64 + lane;
#pragma unroll
  for (int ks = 0; ks < 10; ++ks) {
    wf[ks][0] = wsrc[(ks * 2) * 64];
    wf[ks][1] = wsrc[(ks * 2 + 1) * 64];
  }
}

template <int MT>
DEV void d_half(const char* smem, int lane, int mi0, int half, const bf16x8 (&wf)[10][2], f32x4 (&acc)[2][4]) {
#pragma unroll
  for (int ks = 0; ks < 10; ++ks) {
#pragma unroll
    for (int m = 0; m < MT; ++m) {
      const bf16x8 hf = d_frag(smem, lane, mi0 + m, half * 10 + ks);
      acc[0][m] = __builtin_amdgcn_mfma_f32_16x16x32_bf16(wf[ks][0], hf, acc[0][m], 0, 0, 0);
      acc[1][m] = __builtin_amdgcn_mfma_f32_16x16x32_bf16(wf[ks][1], hf, acc[1][m], 0, 0, 0);
    }
  }
}

template <int MT>
DEV void d_epi(const Params& p, int lane, int ft, int mi0, int mt, f32x4 (&acc)[2][4]) {
  const int rq = lane >> 4;
  u16* gdst = (u16*)(p.ws + WS_G);
#pragma unroll
  for (int m = 0; m < MT; ++m) {
    float gv[4];
#pragma unroll
    for (int g = 0; g < 4; ++g) {
      const float gate = acc[0][m][g], up = acc[1][m][g];
      gv[g] = gate * up * __builtin_amdgcn_rcpf(1.f + __builtin_amdgcn_exp2f(gate * -1.44269504088896341f));
    }
    const int mtile = mt * 4 + mi0 + m;
    const int lane2 = (lane & 15) + 16 * (2 * (ft & 1) + (rq >> 1));
    u32x2 o = {pack2(gv[0], gv[1]), pack2(gv[2], gv[3])};
    *(u32x2*)(gdst + ((size_t)(mtile * 54 + (ft >> 1)) * 64 + lane2) * 8 + 4 * (rq & 1)) = o;
  }
#pragma unroll
  for (int m = 0; m < 4; ++m) { acc[0][m] = (f32x4){0.f, 0.f, 0.f, 0.f}; acc[1][m] = (f32x4){0.f, 0.f, 0.f, 0.f}; }
}

#define SB0 __builtin_amdgcn_sched_barrier(0)
#define RAWBAR() do { asm volatile("s_waitcnt lgkmcnt(0)" ::: "memory"); __builtin_amdgcn_s_barrier(); asm volatile("" ::: "memory"); } while (0)

DEV float sum_mod8(float x) {
  x = dpp_add<0x128>(x);
  x = sum_x16(x);
  x = sum_x32(x);
  return x;
}
typedef __attribute__((ext_vector_type(2))) float f32x2;
DEV f32x2 unpk(unsigned u) { return (f32x2){lo16(u), hi16(u)}; }

__global__ __launch_bounds__(512) void kD(Params p) {
  __shared__ __attribute__((aligned(16))) char smem[81920 + 4096];
  const int tid = threadIdx.x;
  const int xc = blockIdx.x & 7, jj = blockIdx.x >> 3;
  const int mt = (xc & 1) * 16 + (jj & 15), fpart = (xc >> 1) * 2 + (jj >> 4);
  const int w = __builtin_amdgcn_readfirstlane(tid >> 6), lane = tid & 63;
  float* ps = (float*)(smem + 81920);
  const float* mod = (const float*)(p.ws + WS_MOD);
  const int c = tid & 7, d0 = tid >> 3;
  const int fs = (27 * fpart) >> 1, fe = (27 * (fpart + 1)) >> 1;
  bf16x8 wA[10][2], wB[10][2];
  char* vdst = smem + d_off(d0, c);
  u32x4 v[10];
  float cbv[10], sbv[10];
  {
    const u16* src = (const u16*)(p.ws + WS_X1T) + (size_t)mt * D_ * 64 + d0 * 64 + c * 8;
#pragma unroll
    for (int it = 0; it < 10; ++it) v[it] = *(const u32x4*)(src + it * 4096);
#pragma unroll
    for (int it = 0; it < 10; ++it) { cbv[it] = mod[4 * 640 + d0 + 64 * it]; sbv[it] = mod[3 * 640 + d0 + 64 * it]; }
  }
  SB0;
  d_load(p, lane, fs + w, 0, wA);
  SB0;
  f32x2 s2[4];
#pragma unroll
  for (int e = 0; e < 4; ++e) s2[e] = (f32x2){0.f, 0.f};
#pragma unroll
  for (int it = 0; it < 10; ++it)
#pragma unroll
    for (int e = 0; e < 4; ++e) s2[e] += unpk(v[it][e]);
  float s[8];
#pragma unroll
  for (int e = 0; e < 8; ++e) s[e] = sum_mod8(s2[e >> 1][e & 1]);
  if (lane < 8) {
    *(f32x4*)(ps + w * 64 + lane * 8) = (f32x4){s[0], s[1], s[2], s[3]};
    *(f32x4*)(ps + w * 64 + lane * 8 + 4) = (f32x4){s[4], s[5], s[6], s[7]};
  }
  RAWBAR();
  f32x2 mean[4];
  {
    f32x4 a = (f32x4){0.f, 0.f, 0.f, 0.f}, bq = (f32x4){0.f, 0.f, 0.f, 0.f};
#pragma unroll
    for (int ww = 0; ww < 8; ++ww) { a += *(const f32x4*)(ps + ww * 64 + c * 8); bq += *(const f32x4*)(ps + ww * 64 + c * 8 + 4); }
    mean[0] = (f32x2){a[0], a[1]} * (1.f / 640.f); mean[1] = (f32x2){a[2], a[3]} * (1.f / 640.f);
    mean[2] = (f32x2){bq[0], bq[1]} * (1.f / 640.f); mean[3] = (f32x2){bq[2], bq[3]} * (1.f / 640.f);
  }
#pragma unroll
  for (int e = 0; e < 4; ++e) s2[e] = (f32x2){0.f, 0.f};
#pragma unroll
  for (int it = 0; it < 10; ++it)
#pragma unroll
    for (int e = 0; e < 4; ++e) { const f32x2 a = unpk(v[it][e]) - mean[e]; s2[e] += a * a; }
#pragma unroll
  for (int e = 0; e < 8; ++e) s[e] = sum_mod8(s2[e >> 1][e & 1]);
  if (lane < 8) {
    *(f32x4*)(ps + 512 + w * 64 + lane * 8) = (f32x4){s[0], s[1], s[2], s[3]};
    *(f32x4*)(ps + 512 + w * 64 + lane * 8 + 4) = (f32x4){s[4], s[5], s[6], s[7]};
  }
  RAWBAR();
  f32x2 den[4];
  {
    f32x4 a = (f32x4){0.f, 0.f, 0.f, 0.f}, bq = (f32x4){0.f, 0.f, 0.f, 0.f};
#pragma unroll
    for (int ww = 0; ww < 8; ++ww) { a += *(const f32x4*)(ps + 512 + ww * 64 + c * 8); bq += *(const f32x4*)(ps + 512 + ww * 64 + c * 8 + 4); }
#pragma unroll
    for (int e = 0; e < 8; ++e) {
      const float var = rbf((e < 4 ? a[e & 3] : bq[e & 3]) * (1.f / 640.f));
      den[e >> 1][e & 1] = __builtin_amdgcn_rcpf(rbf(__builtin_amdgcn_sqrtf(rbf(var + 1e-5f))));
      mean[e >> 1][e & 1] = rbf(mean[e >> 1][e & 1]);
    }
  }
#pragma unroll
  for (int it = 0; it < 10; ++it) {
    const float tt = rbf(1.f + cbv[it]);
    u32x4 o;
#pragma unroll
    for (int e = 0; e < 4; ++e) {
      const f32x2 h = ((unpk(v[it][e]) - mean[e]) * den[e]) * tt + sbv[it];
      o[e] = pack2(h[0], h[1]);
    }
    *(u32x4*)(vdst + 8192 * it) = o;
  }
  SB0;
  RAWBAR();
  d_load(p, lane, fs + w, 1, wB);
  SB0;
  const bool big = (fe - fs == 14);
  f32x4 acc[2][4];
#pragma unroll
  for (int m = 0; m < 4; ++m) { acc[0][m] = (f32x4){0.f, 0.f, 0.f, 0.f}; acc[1][m] = (f32x4){0.f, 0.f, 0.f, 0.f}; }
  const int ft2 = (w < 4) ? fs + 8 + w : (big ? fs + 12 + ((w & 3) >> 1) : fs + 12);
  SB0;
  d_half<4>(smem, lane, 0, 0, wA, acc);
  SB0;
  d_load(p, lane, ft2, 0, wA);
  SB0;
  d_half<4>(smem, lane, 0, 1, wB, acc);
  d_epi<4>(p, lane, fs + w, 0, mt, acc);
  SB0;
  d_load(p, lane, ft2, 1, wB);
  SB0;
  if (w < 4) {
    d_half<4>(smem, lane, 0, 0, wA, acc);
    d_half<4>(smem, lane, 0, 1, wB, acc);
    d_epi<4>(p, lane, ft2, 0, mt, acc);
  } else if (big) {
    const int mi0 = 2 * (w & 1);
    d_half<2>(smem, lane, mi0, 0, wA, acc);
    d_half<2>(smem, lane, mi0, 1, wB, acc);
    d_epi<2>(p, lane, ft2, mi0, mt, acc);
  } else {
    const int mi0 = w & 3;
    d_half<1>(smem, lane, mi0, 0, wA, acc);
    d_half<1>(smem, lane, mi0, 1, wB, acc);
    d_epi<1>(p, lane, ft2, mi0, mt, acc);
  }
}

constexpr int E_RS = 84;
constexpr int E_DEPTH = 5;
__global__ __launch_bounds__(256) void kE(Params p) {
  __shared__ __attribute__((aligned(16))) float red[4 * 64 * E_RS + 80 * 36];
  const int tid = threadIdx.x, xc = blockIdx.x & 7, j = blockIdx.x >> 3;
  const int mt = (xc >> 1) * 8 + (j & 7), np = (xc & 1) * 4 + (j >> 3);
  const int w = __builtin_amdgcn_readfirstlane(tid >> 6), lane = tid & 63;
  const int kstart = (w < 2) ? 14 * w : 28 + 13 * (w - 2);
  const int kcnt = (w < 2) ? 14 : 13;
  const bf16x8* gsrc = (const bf16x8*)(p.ws + WS_G) + (size_t)(mt * 4) * 54 * 64 + kstart * 64 + lane;
  const bf16x8* wsrc = (const bf16x8*)(p.ws + WS_W2) + (size_t)(np * 5) * 54 * 64 + kstart * 64 + lane;
  f32x4 acc[5][4];
#pragma unroll
  for (int i = 0; i < 5; ++i)
#pragma unroll
    for (int m = 0; m < 4; ++m) acc[i][m] = (f32x4){0.f, 0.f, 0.f, 0.f};
  bf16x8 gf[E_DEPTH][4], wf[E_DEPTH][5];
#pragma unroll
  for (int s = 0; s < E_DEPTH; ++s) {
#pragma unroll
    for (int m = 0; m < 4; ++m) gf[s][m] = gsrc[(m * 54 + s) * 64];
#pragma unroll
    for (int i = 0; i < 5; ++i) wf[s][i] = wsrc[(i * 54 + s) * 64];
  }
  const float* mod = (const float*)(p.ws + WS_MOD);
  u16* xs = (u16*)(red + 4 * 64 * E_RS);
  u32x4 xa, xb, xc2;
  {
    const u16* x1t = (const u16*)(p.ws + WS_X1T) + ((size_t)mt * D_ + np * 80) * 64;
    xa = *(const u32x4*)(x1t + tid * 8);
    xb = *(const u32x4*)(x1t + (tid + 256) * 8);
    xc2 = *(const u32x4*)(x1t + (tid < 128 ? tid + 512 : 0) * 8);
  }
  SB0;
#pragma unroll
  for (int kk = 0; kk < 14; ++kk) {
    if (kk < kcnt) {
#pragma unroll
      for (int i = 0; i < 5; ++i)
#pragma unroll
        for (int m = 0; m < 4; ++m)
          acc[i][m] = __builtin_amdgcn_mfma_f32_16x16x32_bf16(wf[kk % E_DEPTH][i], gf[kk % E_DEPTH][m], acc[i][m], 0, 0, 0);
    }
    SB0;
    if (kk + E_DEPTH < kcnt) {
#pragma unroll
      for (int m = 0; m < 4; ++m) gf[kk % E_DEPTH][m] = gsrc[(m * 54 + kk + E_DEPTH) * 64];
#pragma unroll
      for (int i = 0; i < 5; ++i) wf[kk % E_DEPTH][i] = wsrc[(i * 54 + kk + E_DEPTH) * 64];
    }
    SB0;
  }
  {
    const int rq = lane >> 4;
#pragma unroll
    for (int i = 0; i < 5; ++i)
#pragma unroll
      for (int m = 0; m < 4; ++m)
        *(f32x4*)(red + (w * 64 + m * 16 + (lane & 15)) * E_RS + i * 16 + 4 * rq) = acc[i][m];
    *(u32x4*)(xs + (tid >> 3) * 72 + (tid & 7) * 8) = xa;
    *(u32x4*)(xs + ((tid + 256) >> 3) * 72 + (tid & 7) * 8) = xb;
    if (tid < 128) *(u32x4*)(xs + ((tid + 512) >> 3) * 72 + (tid & 7) * 8) = xc2;
  }
  __syncthreads();
  for (int item = tid; item < 640; item += 256) {
    const int t = item / 10, cc = item % 10;
    const int dcol = np * 80 + cc * 8;
    const size_t o = (size_t)(mt * 64 + t) * D_ + dcol;
    float xv[8];
#pragma unroll
    for (int e = 0; e < 8; ++e) xv[e] = bf2f(xs[(cc * 8 + e) * 72 + t]);
    const f32x4 g0 = *(const f32x4*)(mod + 5 * 640 + dcol), g1 = *(const f32x4*)(mod + 5 * 640 + dcol + 4);
    float y[8];
#pragma unroll
    for (int e = 0; e < 8; ++e) y[e] = 0.f;
#pragma unroll
    for (int ww = 0; ww < 4; ++ww) {
      f32x4 a = *(const f32x4*)(red + (ww * 64 + t) * E_RS + cc * 8), bb = *(const f32x4*)(red + (ww * 64 + t) * E_RS + cc * 8 + 4);
#pragma unroll
      for (int e = 0; e < 4; ++e) { y[e] += a[e]; y[4 + e] += bb[e]; }
    }
    u32x4 ov;
#pragma unroll
    for (int e = 0; e < 4; ++e) {
      const float ga = (e < 2) ? g0[2 * e] : g1[2 * e - 4], gb = (e < 2) ? g0[2 * e + 1] : g1[2 * e - 3];
      float r0 = xv[2 * e] + ga * y[2 * e], r1 = xv[2 * e + 1] + gb * y[2 * e + 1];
      ov[e] = pack2(r0, r1);
    }
    *(u32x4*)(p.out + o) = ov;
  }
}

extern "C" void kernel_launch(void* const* d_in, const int* in_sizes, int n_in,
                              void* d_out, int out_size, void* d_ws, size_t ws_size,
                              hipStream_t stream) {
  Params p;
  p.u = (const u16*)d_in[0];
  p.Are = (const u16*)d_in[1];
  p.Aim = (const u16*)d_in[2];
  p.Bre = (const u16*)d_in[3];
  p.Bim = (const u16*)d_in[4];
  p.Cre = (const u16*)d_in[5];
  p.Cim = (const u16*)d_in[6];
  p.logdt = (const u16*)d_in[7];
  p.Dskip = (const u16*)d_in[8];
  p.w1 = (const u16*)d_in[10];
  p.w3 = (const u16*)d_in[11];
  p.w2 = (const u16*)d_in[12];
  p.c = (const u16*)d_in[13];
  p.wmod = (const u16*)d_in[14];
  p.out = (u16*)d_out;
  p.ws = (char*)d_ws;
  kA<<<A_TOTAL, 512, 0, stream>>>(p);
  kB<<<640, 256, 0, stream>>>(p);
  kD<<<256, 512, 0, stream>>>(p);
  kE<<<256, 256, 0, stream>>>(p);
}
```

```cpp
#include <hip/hip_runtime.h>
#include <hip/hip_bf16.h>

typedef unsigned short u16;
typedef __attribute__((ext_vector_type(8))) short bf16x8;
typedef __attribute__((ext_vector_type(4))) short s16x4;
typedef __attribute__((ext_vector_type(4))) float f32x4;
typedef __attribute__((ext_vector_type(4))) unsigned u32x4;
typedef __attribute__((ext_vector_type(2))) unsigned u32x2;

#define DEV __device__ __forceinline__

constexpr int L_ = 2048, D_ = 640, N_ = 128, F_ = 1728;
constexpr int KR_LEN = 2208;

constexpr size_t WS_MOD = 0;
constexpr size_t WS_NT = 16384;
constexpr size_t WS_XT = WS_NT + (size_t)D_ * L_ * 2;
constexpr size_t WS_KR = WS_XT + (size_t)D_ * L_ * 2;
constexpr size_t WS_X1T = WS_KR + (size_t)D_ * 2 * KR_LEN * 2;
constexpr size_t WS_H2 = WS_X1T + (size_t)D_ * L_ * 2;
constexpr size_t WS_W13 = WS_H2 + (size_t)D_ * L_ * 2;
constexpr size_t WS_W2 = WS_W13 + (size_t)2 * D_ * F_ * 2;
constexpr size_t WS_G = WS_W2 + (size_t)D_ * F_ * 2;
constexpr size_t WS_END = WS_G + (size_t)L_ * F_ * 2;

DEV float bf2f(u16 x) { return __uint_as_float(((unsigned)x) << 16); }
DEV u16 f2bf(float f) { return __builtin_bit_cast(u16, (__bf16)f); }
DEV float rbf(float f) { return bf2f(f2bf(f)); }
DEV unsigned pack2(float a, float b) { return (unsigned)f2bf(a) | ((unsigned)f2bf(b) << 16); }
DEV float lo16(unsigned u) { return __uint_as_float(u << 16); }
DEV float hi16(unsigned u) { return __uint_as_float(u & 0xffff0000u); }

DEV void cexp(float ar, float ai, float w, float& er, float& ei) {
  const float mag = __builtin_amdgcn_exp2f(ar * w);
  const float rev = __builtin_amdgcn_fractf(ai * w);
  er = mag * __builtin_amdgcn_cosf(rev);
  ei = mag * __builtin_amdgcn_sinf(rev);
}

template <int CTRL>
DEV float dpp_add(float x) {
  return x + __builtin_bit_cast(float, __builtin_amdgcn_update_dpp(0, __builtin_bit_cast(int, x), CTRL, 0xf, 0xf, false));
}
DEV float sum_x16(float x) {
  float a = x, b = x;
  asm volatile("s_nop 1\n\tv_permlane16_swap_b32 %0, %1" : "+v"(a), "+v"(b));
  return a + b;
}
DEV float sum_x32(float x) {
  float a = x, b = x;
  asm volatile("s_nop 1\n\tv_permlane32_swap_b32 %0, %1" : "+v"(a), "+v"(b));
  return a + b;
}
DEV float row_sum16(float x) {
  x = dpp_add<0xB1>(x);
  x = dpp_add<0x4E>(x);
  x = dpp_add<0x124>(x);
  x = dpp_add<0x128>(x);
  return x;
}
DEV float sum_mod4(float x) {
  x = dpp_add<0x124>(x);
  x = dpp_add<0x128>(x);
  x = sum_x16(x);
  x = sum_x32(x);
  return x;
}

struct Params {
  const u16 *u, *Are, *Aim, *Bre, *Bim, *Cre, *Cim, *logdt, *Dskip, *w1, *w3, *w2, *c, *wmod;
  u16* out;
  char* ws;
};

constexpr int A_KGEN = 640, A_LN = 64, A_MOD = 60, A_W13 = 270, A_W2 = 135;
constexpr int A_TOTAL = A_KGEN + A_LN + A_MOD + A_W13 + A_W2;

__global__ __launch_bounds__(512) void kA(Params p) {
  __shared__ __attribute__((aligned(16))) char smem[41984];
  const int tid = threadIdx.x;
  int b = blockIdx.x;
  if (b < A_MOD) {
    float* red = (float*)smem;
    const int cg = tid & 7, rg = tid >> 3;
    float acc[8];
#pragma unroll
    for (int e = 0; e < 8; ++e) acc[e] = 0.f;
    u32x4 wv[10];
    float cv[10];
#pragma unroll
    for (int r = 0; r < 10; ++r) {
      wv[r] = *(const u32x4*)(p.wmod + (size_t)(rg * 10 + r) * 3840 + b * 64 + cg * 8);
      cv[r] = bf2f(p.c[rg * 10 + r]);
    }
#pragma unroll
    for (int r = 0; r < 10; ++r) {
      float s = rbf(cv[r] / (1.f + __expf(-cv[r])));
#pragma unroll
      for (int e = 0; e < 4; ++e) {
        acc[2 * e] += s * lo16(wv[r][e]);
        acc[2 * e + 1] += s * hi16(wv[r][e]);
      }
    }
    *(f32x4*)(red + rg * 64 + cg * 8) = (f32x4){acc[0], acc[1], acc[2], acc[3]};
    *(f32x4*)(red + rg * 64 + cg * 8 + 4) = (f32x4){acc[4], acc[5], acc[6], acc[7]};
    __syncthreads();
    {
      const int col = tid & 63, g = tid >> 6;
      float s = 0.f;
#pragma unroll
      for (int r = 0; r < 8; ++r) s += red[(g * 8 + r) * 64 + col];
      float* red2 = red + 4096;
      red2[g * 64 + col] = s;
      __syncthreads();
      if (tid < 64) {
        float t = 0.f;
#pragma unroll
        for (int r = 0; r < 8; ++r) t += red2[r * 64 + tid];
        ((float*)(p.ws + WS_MOD))[b * 64 + tid] = rbf(t);
      }
    }
    return;
  }
  b -= A_MOD;
  if (b < A_KGEN) {
    const int d = b;
    float* red = (float*)smem;
    float* Kv = (float*)(smem + 24576);
    f32x4* prm = (f32x4*)(smem + 32784);
    const int w = tid >> 6, l = tid & 63, i = l & 15, kq = l >> 4;
    if (tid < 128) {
      const int n = d * N_ + tid;
      const u16 a0 = p.Are[n], a1 = p.Aim[n], b0 = p.Bre[n], b1 = p.Bim[n], c0 = p.Cre[n], c1 = p.Cim[n];
      const float dt = rbf(__expf(bf2f(p.logdt[d])));
      const float dBr = dt * bf2f(b0), dBi = dt * bf2f(b1);
      const float Cr = bf2f(c0), Ci = bf2f(c1);
      prm[tid] = (f32x4){dt * bf2f(a0) * 1.44269504088896341f, dt * bf2f(a1) * 0.15915494309189535f, Cr * dBr - Ci * dBi, Cr * dBi + Ci * dBr};
    }
    const int o0 = (i & 7) + 128 * (i >> 3);
    const int o1 = (i < 8) ? 256 + 2 * (i & 3) + 128 * (i >> 2) : 512 + 4 * (i & 1) + 128 * ((i >> 1) & 3);
    const int o2 = 1024 + 128 * i;
    const float fo0 = (float)o0, fo1 = (float)o1, fo2 = (float)o2, fj = (float)(8 * i);
    __syncthreads();
    f32x4 acc[3];
#pragma unroll
    for (int s = 0; s < 3; ++s) acc[s] = (f32x4){0.f, 0.f, 0.f, 0.f};
#pragma unroll 2
    for (int kk = 0; kk < 4; ++kk) {
      const f32x4 pr = prm[16 * w + 4 * kk + kq];
      const float ar = pr[0], ai = pr[1], CBr = pr[2], CBi = pr[3];
      float yr, yi;
      cexp(ar, ai, fj, yr, yi);
#pragma unroll
      for (int s = 0; s < 3; ++s) {
        float zr, zi;
        cexp(ar, ai, s == 0 ? fo0 : (s == 1 ? fo1 : fo2), zr, zi);
        const float xr = CBr * zr - CBi * zi, xin = -(CBr * zi + CBi * zr);
        acc[s] = __builtin_amdgcn_mfma_f32_16x16x4f32(yr, xr, acc[s], 0, 0, 0);
        acc[s] = __builtin_amdgcn_mfma_f32_16x16x4f32(yi, xin, acc[s], 0, 0, 0);
      }
    }
#pragma unroll
    for (int s = 0; s < 3; ++s) *(f32x4*)(red + (w * 3 + s) * 256 + i * 16 + 4 * kq) = acc[s];
    __syncthreads();
    {
      const int t8 = tid & 255, cI = t8 >> 4, j = t8 & 15, tl = tid >> 8;
      const int p0 = (cI & 7) + 128 * (cI >> 3);
      const int p1 = (cI < 8) ? 256 + 2 * (cI & 3) + 128 * (cI >> 2) : 512 + 4 * (cI & 1) + 128 * ((cI >> 1) & 3);
      float sum = 0.f;
#pragma unroll
      for (int ww = 0; ww < 8; ++ww) sum += red[(ww * 3 + tl) * 256 + t8];
      Kv[(tl ? p1 : p0) + 8 * j] = sum;
      if (tid <= 128) {
        float s2 = 0.f;
#pragma unroll
        for (int ww = 0; ww < 8; ++ww) s2 += red[(ww * 3 + 2) * 256 + tid];
        Kv[1024 + 128 * cI + 8 * j] = s2;
      }
    }
    __syncthreads();
    u16* Kr = (u16*)(p.ws + WS_KR) + (size_t)d * 2 * KR_LEN;
    for (int c = tid; c < KR_LEN / 4; c += 512) {
      unsigned hw[2], lw[2];
#pragma unroll
      for (int e2 = 0; e2 < 2; ++e2) {
        float kv[2];
#pragma unroll
        for (int z = 0; z < 2; ++z) {
          const int k = 2080 - (c * 4 + e2 * 2 + z);
          const int kc = min(max(k, 0), 2047);
          const float v = Kv[(int)rbf((float)kc)];
          kv[z] = (k == kc) ? v : 0.f;
        }
        float h0 = rbf(kv[0]), h1 = rbf(kv[1]);
        hw[e2] = pack2(h0, h1);
        lw[e2] = pack2(kv[0] - h0, kv[1] - h1);
      }
      *(u32x2*)(Kr + c * 4) = (u32x2){hw[0], hw[1]};
      *(u32x2*)(Kr + KR_LEN + c * 4) = (u32x2){lw[0], lw[1]};
    }
    return;
  }
  b -= A_KGEN;
  if (b < A_LN) {
    constexpr int XS = 648;
    u16* xs = (u16*)smem;
    float* sm = (float*)(smem + 32 * XS * 2);
    float* sd = sm + 32;
    const int row = tid >> 4, c = tid & 15;
    {
      u32x4 v[5];
#pragma unroll
      for (int q = 0; q < 5; ++q) v[q] = *(const u32x4*)(p.u + (size_t)(b * 32 + row) * D_ + (c + 16 * q) * 8);
      float s = 0.f;
#pragma unroll
      for (int q = 0; q < 5; ++q)
#pragma unroll
        for (int e = 0; e < 4; ++e) s += lo16(v[q][e]) + hi16(v[q][e]);
      s = row_sum16(s);
      const float mean = s * (1.f / 640.f);
      float s2 = 0.f;
#pragma unroll
      for (int q = 0; q < 5; ++q)
#pragma unroll
        for (int e = 0; e < 4; ++e) {
          float a = lo16(v[q][e]) - mean, bb = hi16(v[q][e]) - mean;
          s2 += a * a + bb * bb;
        }
      s2 = row_sum16(s2);
      if (c == 0) {
        float var = rbf(s2 * (1.f / 640.f));
        sm[row] = rbf(mean);
        sd[row] = __builtin_amdgcn_rcpf(rbf(__builtin_amdgcn_sqrtf(rbf(var + 1e-5f))));
      }
#pragma unroll
      for (int q = 0; q < 5; ++q) *(u32x4*)(xs + row * XS + (c + 16 * q) * 8) = v[q];
    }
    __syncthreads();
    u16* nT = (u16*)(p.ws + WS_NT);
    u16* xT = (u16*)(p.ws + WS_XT);
    const int tq = tid & 3;
    float mm[8], dd[8];
#pragma unroll
    for (int e = 0; e < 8; ++e) { mm[e] = sm[tq * 8 + e]; dd[e] = sd[tq * 8 + e]; }
#pragma unroll
    for (int it = 0; it < 5; ++it) {
      const int d = (tid >> 2) + 128 * it;
      unsigned xo[4], no[4];
#pragma unroll
      for (int e2 = 0; e2 < 4; ++e2) {
        const int r0 = tq * 8 + e2 * 2;
        u16 a = xs[r0 * XS + d], bb = xs[(r0 + 1) * XS + d];
        xo[e2] = (unsigned)a | ((unsigned)bb << 16);
        float n0 = (bf2f(a) - mm[2 * e2]) * dd[2 * e2];
        float n1 = (bf2f(bb) - mm[2 * e2 + 1]) * dd[2 * e2 + 1];
        no[e2] = pack2(n0, n1);
      }
      const size_t o = (size_t)d * L_ + b * 32 + tq * 8;
      *(u32x4*)(xT + o) = (u32x4){xo[0], xo[1], xo[2], xo[3]};
      *(u32x4*)(nT + o) = (u32x4){no[0], no[1], no[2], no[3]};
    }
    return;
  }
  b -= A_LN;
  if (b < A_W13) {
    const int ks0 = (b / 27) * 2, fc = b % 27;
    u16* ts = (u16*)smem;
    {
      const int kh = tid >> 8, mat = (tid >> 7) & 1, r = (tid >> 2) & 31, cq = tid & 3;
      const u16* src = (mat ? p.w3 : p.w1) + (size_t)((ks0 + kh) * 32 + r) * F_ + fc * 64 + cq * 16;
      u32x4 a = *(const u32x4*)src, bb = *(const u32x4*)(src + 8);
      *(u32x4*)(ts + ((kh * 2 + mat) * 32 + r) * 72 + cq * 16) = a;
      *(u32x4*)(ts + ((kh * 2 + mat) * 32 + r) * 72 + cq * 16 + 8) = bb;
    }
    __syncthreads();
    u16* dst = (u16*)(p.ws + WS_W13);
#pragma unroll
    for (int it = 0; it < 2; ++it) {
      const int item = tid + 512 * it;
      const int lane = item & 63, mat = (item >> 6) & 1, ftl = (item >> 7) & 3, kh = item >> 9;
      const int f = ftl * 16 + (lane & 15), k0 = (lane >> 4) * 8;
      const u16* tb = ts + ((kh * 2 + mat) * 32 + k0) * 72 + f;
      unsigned o[4];
#pragma unroll
      for (int e2 = 0; e2 < 4; ++e2) o[e2] = (unsigned)tb[(2 * e2) * 72] | ((unsigned)tb[(2 * e2 + 1) * 72] << 16);
      const int ft = fc * 4 + ftl;
      *(u32x4*)(dst + ((size_t)((ft * 20 + ks0 + kh) * 2 + mat) * 64 + lane) * 8) = (u32x4){o[0], o[1], o[2], o[3]};
    }
    return;
  }
  b -= A_W13;
  {
    const int ks0 = (b / 5) * 2, dc = b % 5;
    u16* ts = (u16*)smem;
    {
      const int r = tid >> 3, cq = tid & 7;
      const u16* src = p.w2 + (size_t)(ks0 * 32 + r) * D_ + dc * 128 + cq * 16;
      u32x4 a = *(const u32x4*)src, bb = *(const u32x4*)(src + 8);
      *(u32x4*)(ts + r * 136 + cq * 16) = a;
      *(u32x4*)(ts + r * 136 + cq * 16 + 8) = bb;
    }
    __syncthreads();
    u16* dst = (u16*)(p.ws + WS_W2);
#pragma unroll
    for (int it = 0; it < 2; ++it) {
      const int item = tid + 512 * it;
      const int lane = item & 63, dtl = (item >> 6) & 7, kh = item >> 9;
      const int dd = dtl * 16 + (lane & 15), k0 = kh * 32 + (lane >> 4) * 8;
      unsigned o[4];
#pragma unroll
      for (int e2 = 0; e2 < 4; ++e2)
        o[e2] = (unsigned)ts[(k0 + 2 * e2) * 136 + dd] | ((unsigned)ts[(k0 + 2 * e2 + 1) * 136 + dd] << 16);
      const int dt = dc * 8 + dtl;
      *(u32x4*)(dst + ((size_t)(dt * 54 + ks0 + kh) * 64 + lane) * 8) = (u32x4){o[0], o[1], o[2], o[3]};
    }
  }
}

constexpr int B_HB = 2 * KR_LEN * 2;
constexpr int B_HS = 4640;
constexpr int B_RED = B_HB + B_HS;
constexpr int B_LDS = B_RED + 32768;

template <int UMAX>
DEV void conv_residue(const char* smem, int o0, int lane, f32x4 (&acc)[8]) {
  const int r = lane & 15, kg = lane >> 4;
  const int rho = lane & 7, cb = (lane >> 3) & 1;
  bf16x8 bf[UMAX + 1];
  const int bbase = B_HB + rho * B_HS + 2 * (160 - o0 + 128 * cb + 8 * kg);
#pragma unroll
  for (int u = 0; u <= UMAX; ++u) bf[u] = *(const bf16x8*)(smem + bbase + 512 * u);
  const int abase = 2 * (2080 - o0 - 8 * r + 8 * kg);
#pragma unroll
  for (int i = 0; i < 8; ++i) {
    const bf16x8 ah = *(const bf16x8*)(smem + abase - 512 * i);
    const bf16x8 al = *(const bf16x8*)(smem + abase - 512 * i + KR_LEN * 2);
#pragma unroll
    for (int u = 0; u <= UMAX; ++u) {
      const int q = u + i;
      if (q < 8) {
        acc[q] = __builtin_amdgcn_mfma_f32_16x16x32_bf16(ah, bf[u], acc[q], 0, 0, 0);
        acc[q] = __builtin_amdgcn_mfma_f32_16x16x32_bf16(al, bf[u], acc[q], 0, 0, 0);
      }
    }
  }
}

DEV void h_mod(const u32x4 v, float t1, float sa, unsigned* wv) {
#pragma unroll
  for (int e = 0; e < 4; ++e) {
    float a = lo16(v[e]) * t1 + sa, bb = hi16(v[e]) * t1 + sa;
    wv[e] = pack2(a, bb);
  }
}
DEV void h_store(char* smem, int c, const unsigned* wv) {
#pragma unroll
  for (int rho = 0; rho < 8; ++rho) {
    u32x4 o;
#pragma unroll
    for (int e = 0; e < 4; ++e) {
      const int k = (rho >> 1) + e;
      o[e] = (rho & 1) ? __builtin_amdgcn_alignbit(wv[k + 1], wv[k], 16) : wv[k];
    }
    *(u32x4*)(smem + B_HB + rho * B_HS + c * 16) = o;
  }
}

__global__ __launch_bounds__(256) void kB(Params p) {
  __shared__ __attribute__((aligned(16))) char smem[B_LDS];
  const int tid = threadIdx.x, d = blockIdx.x;
  const float* mod = (const float*)(p.ws + WS_MOD);
  const u16* nT = (const u16*)(p.ws + WS_NT) + (size_t)d * L_;
  const u32x4* ksrc = (const u32x4*)((const u16*)(p.ws + WS_KR) + (size_t)d * 2 * KR_LEN);
  const u32x4 n0 = *(const u32x4*)(nT + tid * 8);
  const u32x4 n1 = *(const u32x4*)(nT + (tid < 255 ? tid * 8 + 8 : 0));
  const u32x4 k0 = ksrc[tid], k1 = ksrc[tid + 256];
  const u32x4 k2 = ksrc[tid < 40 ? tid + 512 : 0];
  const u32x4 xv = *(const u32x4*)((const u16*)(p.ws + WS_XT) + (size_t)d * L_ + tid * 8);
  const float sa = mod[d], cav = mod[640 + d], ga = mod[1280 + d];
  const float Dk = bf2f(p.Dskip[d]);
  const float t1 = rbf(1.f + cav);
  *(u32x4*)(smem + tid * 16) = k0;
  *(u32x4*)(smem + (tid + 256) * 16) = k1;
  if (tid < 40) *(u32x4*)(smem + (tid + 512) * 16) = k2;
  {
    unsigned wv[8];
    h_mod(n0, t1, sa, wv);
    h_mod(n1, t1, sa, wv + 4);
    if (tid == 255) { wv[4] = 0u; wv[5] = 0u; wv[6] = 0u; wv[7] = 0u; }
    h_store(smem, tid + 20, wv);
    unsigned zv[8];
#pragma unroll
    for (int e = 0; e < 4; ++e) {
      const unsigned f0 = __builtin_amdgcn_readfirstlane(wv[e]);
      zv[e] = 0u;
      zv[4 + e] = (tid == 19) ? f0 : 0u;
    }
    if (tid < 20) h_store(smem, tid, zv);
  }
  __syncthreads();
  const int w = __builtin_amdgcn_readfirstlane(tid >> 6), lane = tid & 63;
  f32x4 acc[8];
#pragma unroll
  for (int q = 0; q < 8; ++q) acc[q] = (f32x4){0.f, 0.f, 0.f, 0.f};
  conv_residue<7>(smem, -96 + 32 * w, lane, acc);
  conv_residue<7>(smem, -96 + 32 * (w + 4), lane, acc);
  if (w == 3) conv_residue<0>(smem, 160, lane, acc);
  __syncthreads();
  {
    const int rho = lane & 7, cb = (lane >> 3) & 1, rq = lane >> 4;
    const int sw = (rq & 1) | (cb << 1);
    float* red = (float*)(smem + B_RED) + w * 2048 + 128 * cb + 32 * rq + rho;
#pragma unroll
    for (int g = 0; g < 4; ++g) {
      float* rg = red + 8 * (g ^ sw);
#pragma unroll
      for (int q = 0; q < 8; ++q) rg[256 * q] = acc[q][g];
    }
  }
  __syncthreads();
  {
    const int t0 = tid * 8;
    const int pos = t0 ^ (((t0 >> 5) & 1) << 3) ^ (((t0 >> 7) & 1) << 4);
    const float* red = (const float*)(smem + B_RED);
    float y[8];
#pragma unroll
    for (int e = 0; e < 8; ++e) y[e] = 0.f;
#pragma unroll
    for (int ww = 0; ww < 4; ++ww) {
      f32x4 a = *(const f32x4*)(red + ww * 2048 + pos), bb = *(const f32x4*)(red + ww * 2048 + pos + 4);
#pragma unroll
      for (int e = 0; e < 4; ++e) { y[e] += a[e]; y[4 + e] += bb[e]; }
    }
    const u32x4 hv = *(const u32x4*)(smem + B_HB + 2 * (160 + t0));
    u32x4 o;
#pragma unroll
    for (int e = 0; e < 4; ++e) {
      float h0 = lo16(hv[e]), h1 = hi16(hv[e]);
      float y0 = rbf(rbf(y[2 * e]) + rbf(h0 * Dk)), y1 = rbf(rbf(y[2 * e + 1]) + rbf(h1 * Dk));
      float x0 = lo16(xv[e]) + rbf(ga * y0), x1 = hi16(xv[e]) + rbf(ga * y1);
      o[e] = pack2(x0, x1);
    }
    *(u32x4*)((u16*)(p.ws + WS_X1T) + ((size_t)(t0 >> 6) * D_ + d) * 64 + (t0 & 63)) = o;
  }
}

DEV int d_off(int k, int c) {
  const int f = ((k >> 1) & 1) | (((k >> 3) & 1) << 1);
  return k * 128 + ((((c >> 1) ^ f)) << 5) + ((c & 1) << 4);
}
DEV bf16x8 d_frag(const char* smem, int lane, int mi, int ks) {
  const int g = lane >> 4, q = (lane & 15) >> 2, pp = lane & 3;
  const int k0 = 32 * ks + 8 * g + q;
  const int c = 2 * mi + (pp >> 1);
  typedef __attribute__((address_space(3))) s16x4* lp;
  s16x4 a = __builtin_amdgcn_ds_read_tr16_b64_v4i16((lp)(smem + d_off(k0, c) + 8 * (pp & 1)));
  s16x4 bq = __builtin_amdgcn_ds_read_tr16_b64_v4i16((lp)(smem + d_off(k0 + 4, c) + 8 * (pp & 1)));
  return __builtin_shufflevector(a, bq, 0, 1, 2, 3, 4, 5, 6, 7);
}

DEV void d_load(const Params& p, int lane, int ft, int half, bf16x8 (&wf)[10][2]) {
  const bf16x8* wsrc = (const bf16x8*)(p.ws + WS_W13) + (size_t)(ft * 20 + half * 10) * 2 * 64 + lane;
#pragma unroll
  for (int ks = 0; ks < 10; ++ks) {
    wf[ks][0] = wsrc[(ks * 2) * 64];
    wf[ks][1] = wsrc[(ks * 2 + 1) * 64];
  }
}

template <int MT>
DEV void d_half(const char* smem, int lane, int mi0, int half, const bf16x8 (&wf)[10][2], f32x4 (&acc)[2][4]) {
#pragma unroll
  for (int ks = 0; ks < 10; ++ks) {
#pragma unroll
    for (int m = 0; m < MT; ++m) {
      const bf16x8 hf = d_frag(smem, lane, mi0 + m, half * 10 + ks);
      acc[0][m] = __builtin_amdgcn_mfma_f32_16x16x32_bf16(wf[ks][0], hf, acc[0][m], 0, 0, 0);
      acc[1][m] = __builtin_amdgcn_mfma_f32_16x16x32_bf16(wf[ks][1], hf, acc[1][m], 0, 0, 0);
    }
  }
}

template <int MT>
DEV void d_epi(const Params& p, int lane, int ft, int mi0, int mt, f32x4 (&acc)[2][4]) {
  const int rq = lane >> 4;
  u16* gdst = (u16*)(p.ws + WS_G);
#pragma unroll
  for (int m = 0; m < MT; ++m) {
    float gv[4];
#pragma unroll
    for (int g = 0; g < 4; ++g) {
      const float gate = acc[0][m][g], up = acc[1][m][g];
      gv[g] = gate * up * __builtin_amdgcn_rcpf(1.f + __builtin_amdgcn_exp2f(gate * -1.44269504088896341f));
    }
    const int mtile = mt * 4 + mi0 + m;
    const int lane2 = (lane & 15) + 16 * (2 * (ft & 1) + (rq >> 1));
    u32x2 o = {pack2(gv[0], gv[1]), pack2(gv[2], gv[3])};
    *(u32x2*)(gdst + ((size_t)(mtile * 54 + (ft >> 1)) * 64 + lane2) * 8 + 4 * (rq & 1)) = o;
  }
#pragma unroll
  for (int m = 0; m < 4; ++m) { acc[0][m] = (f32x4){0.f, 0.f, 0.f, 0.f}; acc[1][m] = (f32x4){0.f, 0.f, 0.f, 0.f}; }
}

#define SB0 __builtin_amdgcn_sched_barrier(0)
#define RAWBAR() do { asm volatile("s_waitcnt lgkmcnt(0)" ::: "memory"); __builtin_amdgcn_s_barrier(); asm volatile("" ::: "memory"); } while (0)

DEV float sum_mod8(float x) {
  x = dpp_add<0x128>(x);
  x = sum_x16(x);
  x = sum_x32(x);
  return x;
}
typedef __attribute__((ext_vector_type(2))) float f32x2;
DEV f32x2 unpk(unsigned u) { return (f32x2){lo16(u), hi16(u)}; }

__global__ __launch_bounds__(512) void kD(Params p) {
  __shared__ __attribute__((aligned(16))) char smem[81920 + 4096 + 5120];
  const int tid = threadIdx.x;
  const int xc = blockIdx.x & 7, jj = blockIdx.x >> 3;
#define D_MAP 0
  const int mt = (xc & 1) * 16 + (jj & 15), fpart = (xc >> 1) * 2 + (jj >> 4);
  const int w = __builtin_amdgcn_readfirstlane(tid >> 6), lane = tid & 63;
  float* ps = (float*)(smem + 81920);
  const float* mod = (const float*)(p.ws + WS_MOD);
  const int c = tid & 7, d0 = tid >> 3;
  const int fs = (27 * fpart) >> 1, fe = (27 * (fpart + 1)) >> 1;
  bf16x8 wA[10][2], wB[10][2];
  char* vdst = smem + d_off(d0, c);
  float* ms = (float*)(smem + 81920 + 4096);
  u32x4 v[10], pv;
  {
    const u16* src = (const u16*)(p.ws + WS_X1T) + (size_t)mt * D_ * 64 + d0 * 64 + c * 8;
#pragma unroll
    for (int it = 0; it < 10; ++it) v[it] = *(const u32x4*)(src + it * 4096);
    pv = *(const u32x4*)(src - d0 * 64);
    const f32x4 mv = *(const f32x4*)(mod + 3 * 640 + (tid < 320 ? tid : 0) * 4);
    if (tid < 320) *(f32x4*)(ms + tid * 4) = mv;
  }
  SB0;
  f32x2 piv[4], s1[4], s2[4];
#pragma unroll
  for (int e = 0; e < 4; ++e) { piv[e] = unpk(pv[e]); s1[e] = (f32x2){0.f, 0.f}; s2[e] = (f32x2){0.f, 0.f}; }
#pragma unroll
  for (int it = 0; it < 10; ++it)
#pragma unroll
    for (int e = 0; e < 4; ++e) { const f32x2 a = unpk(v[it][e]) - piv[e]; s1[e] += a; s2[e] += a * a; }
  {
    float s[8], q[8];
#pragma unroll
    for (int e = 0; e < 8; ++e) { s[e] = sum_mod8(s1[e >> 1][e & 1]); q[e] = sum_mod8(s2[e >> 1][e & 1]); }
    if (lane < 8) {
      *(f32x4*)(ps + w * 64 + lane * 8) = (f32x4){s[0], s[1], s[2], s[3]};
      *(f32x4*)(ps + w * 64 + lane * 8 + 4) = (f32x4){s[4], s[5], s[6], s[7]};
      *(f32x4*)(ps + 512 + w * 64 + lane * 8) = (f32x4){q[0], q[1], q[2], q[3]};
      *(f32x4*)(ps + 512 + w * 64 + lane * 8 + 4) = (f32x4){q[4], q[5], q[6], q[7]};
    }
  }
  SB0;
  d_load(p, lane, fs + w, 0, wA);
  SB0;
  RAWBAR();
  f32x2 mean[4], den[4];
  {
    f32x4 a = (f32x4){0.f, 0.f, 0.f, 0.f}, bq = (f32x4){0.f, 0.f, 0.f, 0.f};
    f32x4 a2 = (f32x4){0.f, 0.f, 0.f, 0.f}, b2 = (f32x4){0.f, 0.f, 0.f, 0.f};
#pragma unroll
    for (int ww = 0; ww < 8; ++ww) {
      a += *(const f32x4*)(ps + ww * 64 + c * 8); bq += *(const f32x4*)(ps + ww * 64 + c * 8 + 4);
      a2 += *(const f32x4*)(ps + 512 + ww * 64 + c * 8); b2 += *(const f32x4*)(ps + 512 + ww * 64 + c * 8 + 4);
    }
#pragma unroll
    for (int e = 0; e < 8; ++e) {
      const float m1 = (e < 4 ? a[e & 3] : bq[e & 3]) * (1.f / 640.f);
      const float m2 = (e < 4 ? a2[e & 3] : b2[e & 3]) * (1.f / 640.f);
      const float var = rbf(fmaxf(m2 - m1 * m1, 0.f));
      den[e >> 1][e & 1] = __builtin_amdgcn_rcpf(rbf(__builtin_amdgcn_sqrtf(rbf(var + 1e-5f))));
      mean[e >> 1][e & 1] = rbf(piv[e >> 1][e & 1] + m1);
    }
  }
#pragma unroll
  for (int it = 0; it < 10; ++it) {
    const float tt = rbf(1.f + ms[640 + d0 + 64 * it]), sbv = ms[d0 + 64 * it];
    u32x4 o;
#pragma unroll
    for (int e = 0; e < 4; ++e) {
      const f32x2 h = ((unpk(v[it][e]) - mean[e]) * den[e]) * tt + sbv;
      o[e] = pack2(h[0], h[1]);
    }
    *(u32x4*)(vdst + 8192 * it) = o;
  }
  SB0;
  d_load(p, lane, fs + w, 1, wB);
  SB0;
  RAWBAR();
  const bool big = (fe - fs == 14);
  f32x4 acc[2][4];
#pragma unroll
  for (int m = 0; m < 4; ++m) { acc[0][m] = (f32x4){0.f, 0.f, 0.f, 0.f}; acc[1][m] = (f32x4){0.f, 0.f, 0.f, 0.f}; }
  const int ft2 = (w < 4) ? fs + 8 + w : (big ? fs + 12 + ((w & 3) >> 1) : fs + 12);
  SB0;
  d_half<4>(smem, lane, 0, 0, wA, acc);
  SB0;
  d_load(p, lane, ft2, 0, wA);
  SB0;
  d_half<4>(smem, lane, 0, 1, wB, acc);
  d_epi<4>(p, lane, fs + w, 0, mt, acc);
  SB0;
  d_load(p, lane, ft2, 1, wB);
  SB0;
  if (w < 4) {
    d_half<4>(smem, lane, 0, 0, wA, acc);
    d_half<4>(smem, lane, 0, 1, wB, acc);
    d_epi<4>(p, lane, ft2, 0, mt, acc);
  } else if (big) {
    const int mi0 = 2 * (w & 1);
    d_half<2>(smem, lane, mi0, 0, wA, acc);
    d_half<2>(smem, lane, mi0, 1, wB, acc);
    d_epi<2>(p, lane, ft2, mi0, mt, acc);
  } else {
    const int mi0 = w & 3;
    d_half<1>(smem, lane, mi0, 0, wA, acc);
    d_half<1>(smem, lane, mi0, 1, wB, acc);
    d_epi<1>(p, lane, ft2, mi0, mt, acc);
  }
}

constexpr int E_RS = 84;
#define E_DEPTH_ 5
constexpr int E_DEPTH = E_DEPTH_;
__global__ __launch_bounds__(256) void kE(Params p) {
  __shared__ __attribute__((aligned(16))) float red[4 * 64 * E_RS + 80 * 36];
  const int tid = threadIdx.x, xc = blockIdx.x & 7, j = blockIdx.x >> 3;
#define E_MAP 0
  const int mt = (xc >> 1) * 8 + (j & 7), np = (xc & 1) * 4 + (j >> 3);
  const int w = __builtin_amdgcn_readfirstlane(tid >> 6), lane = tid & 63;
  const int kstart = (w < 2) ? 14 * w : 28 + 13 * (w - 2);
  const int kcnt = (w < 2) ? 14 : 13;
  const bf16x8* gsrc = (const bf16x8*)(p.ws + WS_G) + (size_t)(mt * 4) * 54 * 64 + kstart * 64 + lane;
  const bf16x8* wsrc = (const bf16x8*)(p.ws + WS_W2) + (size_t)(np * 5) * 54 * 64 + kstart * 64 + lane;
  f32x4 acc[5][4];
#pragma unroll
  for (int i = 0; i < 5; ++i)
#pragma unroll
    for (int m = 0; m < 4; ++m) acc[i][m] = (f32x4){0.f, 0.f, 0.f, 0.f};
  bf16x8 gf[E_DEPTH][4], wf[E_DEPTH][5];
#pragma unroll
  for (int s = 0; s < E_DEPTH; ++s) {
#pragma unroll
    for (int m = 0; m < 4; ++m) gf[s][m] = gsrc[(m * 54 + s) * 64];
#pragma unroll
    for (int i = 0; i < 5; ++i) wf[s][i] = wsrc[(i * 54 + s) * 64];
  }
  const float* mod = (const float*)(p.ws + WS_MOD);
  u16* xs = (u16*)(red + 4 * 64 * E_RS);
  u32x4 xa, xb, xc2;
  {
    const u16* x1t = (const u16*)(p.ws + WS_X1T) + ((size_t)mt * D_ + np * 80) * 64;
    xa = *(const u32x4*)(x1t + tid * 8);
    xb = *(const u32x4*)(x1t + (tid + 256) * 8);
    xc2 = *(const u32x4*)(x1t + (tid < 128 ? tid + 512 : 0) * 8);
  }
  SB0;
#pragma unroll
  for (int kk = 0; kk < 14; ++kk) {
    if (kk < kcnt) {
#pragma unroll
      for (int i = 0; i < 5; ++i)
#pragma unroll
        for (int m = 0; m < 4; ++m)
          acc[i][m] = __builtin_amdgcn_mfma_f32_16x16x32_bf16(wf[kk % E_DEPTH][i], gf[kk % E_DEPTH][m], acc[i][m], 0, 0, 0);
    }
    SB0;
    if (kk + E_DEPTH < kcnt) {
#pragma unroll
      for (int m = 0; m < 4; ++m) gf[kk % E_DEPTH][m] = gsrc[(m * 54 + kk + E_DEPTH) * 64];
#pragma unroll
      for (int i = 0; i < 5; ++i) wf[kk % E_DEPTH][i] = wsrc[(i * 54 + kk + E_DEPTH) * 64];
    }
    SB0;
  }
  {
    const int rq = lane >> 4;
#pragma unroll
    for (int i = 0; i < 5; ++i)
#pragma unroll
      for (int m = 0; m < 4; ++m)
        *(f32x4*)(red + (w * 64 + m * 16 + (lane & 15)) * E_RS + i * 16 + 4 * rq) = acc[i][m];
    *(u32x4*)(xs + (tid >> 3) * 72 + (tid & 7) * 8) = xa;
    *(u32x4*)(xs + ((tid + 256) >> 3) * 72 + (tid & 7) * 8) = xb;
    if (tid < 128) *(u32x4*)(xs + ((tid + 512) >> 3) * 72 + (tid & 7) * 8) = xc2;
  }
  __syncthreads();
  for (int item = tid; item < 640; item += 256) {
    const int t = item / 10, cc = item % 10;
    const int dcol = np * 80 + cc * 8;
    const size_t o = (size_t)(mt * 64 + t) * D_ + dcol;
    float xv[8];
#pragma unroll
    for (int e = 0; e < 8; ++e) xv[e] = bf2f(xs[(cc * 8 + e) * 72 + t]);
    const f32x4 g0 = *(const f32x4*)(mod + 5 * 640 + dcol), g1 = *(const f32x4*)(mod + 5 * 640 + dcol + 4);
    float y[8];
#pragma unroll
    for (int e = 0; e < 8; ++e) y[e] = 0.f;
#pragma unroll
    for (int ww = 0; ww < 4; ++ww) {
      f32x4 a = *(const f32x4*)(red + (ww * 64 + t) * E_RS + cc * 8), bb = *(const f32x4*)(red + (ww * 64 + t) * E_RS + cc * 8 + 4);
#pragma unroll
      for (int e = 0; e < 4; ++e) { y[e] += a[e]; y[4 + e] += bb[e]; }
    }
    u32x4 ov;
#pragma unroll
    for (int e = 0; e < 4; ++e) {
      const float ga = (e < 2) ? g0[2 * e] : g1[2 * e - 4], gb = (e < 2) ? g0[2 * e + 1] : g1[2 * e - 3];
      float r0 = xv[2 * e] + rbf(ga * rbf(y[2 * e])), r1 = xv[2 * e + 1] + rbf(gb * rbf(y[2 * e + 1]));
      ov[e] = pack2(r0, r1);
    }
    *(u32x4*)(p.out + o) = ov;
  }
}

extern "C" void kernel_launch(void* const* d_in, const int* in_sizes, int n_in,
                              void* d_out, int out_size, void* d_ws, size_t ws_size,
                              hipStream_t stream) {
  Params p;
  p.u = (const u16*)d_in[0];
  p.Are = (const u16*)d_in[1];
  p.Aim = (const u16*)d_in[2];
  p.Bre = (const u16*)d_in[3];
  p.Bim = (const u16*)d_in[4];
  p.Cre = (const u16*)d_in[5];
  p.Cim = (const u16*)d_in[6];
  p.logdt = (const u16*)d_in[7];
  p.Dskip = (const u16*)d_in[8];
  p.w1 = (const u16*)d_in[10];
  p.w3 = (const u16*)d_in[11];
  p.w2 = (const u16*)d_in[12];
  p.c = (const u16*)d_in[13];
  p.wmod = (const u16*)d_in[14];
  p.out = (u16*)d_out;
  p.ws = (char*)d_ws;
  kA<<<A_TOTAL, 512, 0, stream>>>(p);
  kB<<<640, 256, 0, stream>>>(p);
  kD<<<256, 512, 0, stream>>>(p);
  kE<<<256, 256, 0, stream>>>(p);
}
```

```cpp
#include <hip/hip_runtime.h>
#include <hip/hip_bf16.h>

typedef unsigned short u16;
typedef __attribute__((ext_vector_type(8))) short bf16x8;
typedef __attribute__((ext_vector_type(4))) short s16x4;
typedef __attribute__((ext_vector_type(4))) float f32x4;
typedef __attribute__((ext_vector_type(4))) unsigned u32x4;
typedef __attribute__((ext_vector_type(2))) unsigned u32x2;

#define DEV __device__ __forceinline__

constexpr int L_ = 2048, D_ = 640, N_ = 128, F_ = 1728;
constexpr int KR_LEN = 2208;

constexpr size_t WS_MOD = 0;
constexpr size_t WS_NT = 16384;
constexpr size_t WS_XT = WS_NT + (size_t)D_ * L_ * 2;
constexpr size_t WS_KR = WS_XT + (size_t)D_ * L_ * 2;
constexpr size_t WS_X1T = WS_KR + (size_t)D_ * 2 * KR_LEN * 2;
constexpr size_t WS_H2 = WS_X1T + (size_t)D_ * L_ * 2;
constexpr size_t WS_W13 = WS_H2 + (size_t)D_ * L_ * 2;
constexpr size_t WS_W2 = WS_W13 + (size_t)2 * D_ * F_ * 2;
constexpr size_t WS_G = WS_W2 + (size_t)D_ * F_ * 2;
constexpr size_t WS_END = WS_G + (size_t)L_ * F_ * 2;

DEV float bf2f(u16 x) { return __uint_as_float(((unsigned)x) << 16); }
DEV u16 f2bf(float f) { return __builtin_bit_cast(u16, (__bf16)f); }
DEV float rbf(float f) { return bf2f(f2bf(f)); }
DEV unsigned pack2(float a, float b) { return (unsigned)f2bf(a) | ((unsigned)f2bf(b) << 16); }
DEV float lo16(unsigned u) { return __uint_as_float(u << 16); }
DEV float hi16(unsigned u) { return __uint_as_float(u & 0xffff0000u); }

DEV void cexp(float ar, float ai, float w, float& er, float& ei) {
  const float mag = __builtin_amdgcn_exp2f(ar * w);
  const float rev = __builtin_amdgcn_fractf(ai * w);
  er = mag * __builtin_amdgcn_cosf(rev);
  ei = mag * __builtin_amdgcn_sinf(rev);
}

template <int CTRL>
DEV float dpp_add(float x) {
  return x + __builtin_bit_cast(float, __builtin_amdgcn_update_dpp(0, __builtin_bit_cast(int, x), CTRL, 0xf, 0xf, false));
}
DEV float sum_x16(float x) {
  float a = x, b = x;
  asm volatile("s_nop 1\n\tv_permlane16_swap_b32 %0, %1" : "+v"(a), "+v"(b));
  return a + b;
}
DEV float sum_x32(float x) {
  float a = x, b = x;
  asm volatile("s_nop 1\n\tv_permlane32_swap_b32 %0, %1" : "+v"(a), "+v"(b));
  return a + b;
}
DEV float row_sum16(float x) {
  x = dpp_add<0xB1>(x);
  x = dpp_add<0x4E>(x);
  x = dpp_add<0x124>(x);
  x = dpp_add<0x128>(x);
  return x;
}
DEV float sum_mod4(float x) {
  x = dpp_add<0x124>(x);
  x = dpp_add<0x128>(x);
  x = sum_x16(x);
  x = sum_x32(x);
  return x;
}

struct Params {
  const u16 *u, *Are, *Aim, *Bre, *Bim, *Cre, *Cim, *logdt, *Dskip, *w1, *w3, *w2, *c, *wmod;
  u16* out;
  char* ws;
};

constexpr int A_KGEN = 640, A_LN = 64, A_MOD = 60, A_W2 = 135;
constexpr int A_TOTAL = A_KGEN + A_LN + A_MOD + A_W2;

__global__ __launch_bounds__(512) void kA(Params p) {
  __shared__ __attribute__((aligned(16))) char smem[41984];
  const int tid = threadIdx.x;
  int b = blockIdx.x;
  if (b < A_MOD) {
    float* red = (float*)smem;
    const int cg = tid & 7, rg = tid >> 3;
    float acc[8];
#pragma unroll
    for (int e = 0; e < 8; ++e) acc[e] = 0.f;
    u32x4 wv[10];
    float cv[10];
#pragma unroll
    for (int r = 0; r < 10; ++r) {
      wv[r] = *(const u32x4*)(p.wmod + (size_t)(rg * 10 + r) * 3840 + b * 64 + cg * 8);
      cv[r] = bf2f(p.c[rg * 10 + r]);
    }
#pragma unroll
    for (int r = 0; r < 10; ++r) {
      float s = rbf(cv[r] / (1.f + __expf(-cv[r])));
#pragma unroll
      for (int e = 0; e < 4; ++e) {
        acc[2 * e] += s * lo16(wv[r][e]);
        acc[2 * e + 1] += s * hi16(wv[r][e]);
      }
    }
    *(f32x4*)(red + rg * 64 + cg * 8) = (f32x4){acc[0], acc[1], acc[2], acc[3]};
    *(f32x4*)(red + rg * 64 + cg * 8 + 4) = (f32x4){acc[4], acc[5], acc[6], acc[7]};
    __syncthreads();
    {
      const int col = tid & 63, g = tid >> 6;
      float s = 0.f;
#pragma unroll
      for (int r = 0; r < 8; ++r) s += red[(g * 8 + r) * 64 + col];
      float* red2 = red + 4096;
      red2[g * 64 + col] = s;
      __syncthreads();
      if (tid < 64) {
        float t = 0.f;
#pragma unroll
        for (int r = 0; r < 8; ++r) t += red2[r * 64 + tid];
        ((float*)(p.ws + WS_MOD))[b * 64 + tid] = rbf(t);
      }
    }
    return;
  }
  b -= A_MOD;
  if (b < A_KGEN) {
    const int d = b;
    float* red = (float*)smem;
    float* Kv = (float*)(smem + 24576);
    f32x4* prm = (f32x4*)(smem + 32784);
    const int w = tid >> 6, l = tid & 63, i = l & 15, kq = l >> 4;
    if (tid < 128) {
      const int n = d * N_ + tid;
      const u16 a0 = p.Are[n], a1 = p.Aim[n], b0 = p.Bre[n], b1 = p.Bim[n], c0 = p.Cre[n], c1 = p.Cim[n];
      const float dt = rbf(__expf(bf2f(p.logdt[d])));
      const float dBr = dt * bf2f(b0), dBi = dt * bf2f(b1);
      const float Cr = bf2f(c0), Ci = bf2f(c1);
      prm[tid] = (f32x4){dt * bf2f(a0) * 1.44269504088896341f, dt * bf2f(a1) * 0.15915494309189535f, Cr * dBr - Ci * dBi, Cr * dBi + Ci * dBr};
    }
    const int o0 = (i & 7) + 128 * (i >> 3);
    const int o1 = (i < 8) ? 256 + 2 * (i & 3) + 128 * (i >> 2) : 512 + 4 * (i & 1) + 128 * ((i >> 1) & 3);
    const int o2 = 1024 + 128 * i;
    const float fo0 = (float)o0, fo1 = (float)o1, fo2 = (float)o2, fj = (float)(8 * i);
    __syncthreads();
    f32x4 acc[3];
#pragma unroll
    for (int s = 0; s < 3; ++s) acc[s] = (f32x4){0.f, 0.f, 0.f, 0.f};
#pragma unroll 2
    for (int kk = 0; kk < 4; ++kk) {
      const f32x4 pr = prm[16 * w + 4 * kk + kq];
      const float ar = pr[0], ai = pr[1], CBr = pr[2], CBi = pr[3];
      float yr, yi;
      cexp(ar, ai, fj, yr, yi);
#pragma unroll
      for (int s = 0; s < 3; ++s) {
        float zr, zi;
        cexp(ar, ai, s == 0 ? fo0 : (s == 1 ? fo1 : fo2), zr, zi);
        const float xr = CBr * zr - CBi * zi, xin = -(CBr * zi + CBi * zr);
        acc[s] = __builtin_amdgcn_mfma_f32_16x16x4f32(yr, xr, acc[s], 0, 0, 0);
        acc[s] = __builtin_amdgcn_mfma_f32_16x16x4f32(yi, xin, acc[s], 0, 0, 0);
      }
    }
#pragma unroll
    for (int s = 0; s < 3; ++s) *(f32x4*)(red + (w * 3 + s) * 256 + i * 16 + 4 * kq) = acc[s];
    __syncthreads();
    {
      const int t8 = tid & 255, cI = t8 >> 4, j = t8 & 15, tl = tid >> 8;
      const int p0 = (cI & 7) + 128 * (cI >> 3);
      const int p1 = (cI < 8) ? 256 + 2 * (cI & 3) + 128 * (cI >> 2) : 512 + 4 * (cI & 1) + 128 * ((cI >> 1) & 3);
      float sum = 0.f;
#pragma unroll
      for (int ww = 0; ww < 8; ++ww) sum += red[(ww * 3 + tl) * 256 + t8];
      Kv[(tl ? p1 : p0) + 8 * j] = sum;
      if (tid <= 128) {
        float s2 = 0.f;
#pragma unroll
        for (int ww = 0; ww < 8; ++ww) s2 += red[(ww * 3 + 2) * 256 + tid];
        Kv[1024 + 128 * cI + 8 * j] = s2;
      }
    }
    __syncthreads();
    u16* Kr = (u16*)(p.ws + WS_KR) + (size_t)d * 2 * KR_LEN;
    for (int c = tid; c < KR_LEN / 4; c += 512) {
      unsigned hw[2], lw[2];
#pragma unroll
      for (int e2 = 0; e2 < 2; ++e2) {
        float kv[2];
#pragma unroll
        for (int z = 0; z < 2; ++z) {
          const int k = 2080 - (c * 4 + e2 * 2 + z);
          const int kc = min(max(k, 0), 2047);
          const float v = Kv[(int)rbf((float)kc)];
          kv[z] = (k == kc) ? v : 0.f;
        }
        float h0 = rbf(kv[0]), h1 = rbf(kv[1]);
        hw[e2] = pack2(h0, h1);
        lw[e2] = pack2(kv[0] - h0, kv[1] - h1);
      }
      *(u32x2*)(Kr + c * 4) = (u32x2){hw[0], hw[1]};
      *(u32x2*)(Kr + KR_LEN + c * 4) = (u32x2){lw[0], lw[1]};
    }
    return;
  }
  b -= A_KGEN;
  if (b < A_LN) {
    constexpr int XS = 648;
    u16* xs = (u16*)smem;
    float* sm = (float*)(smem + 32 * XS * 2);
    float* sd = sm + 32;
    const int row = tid >> 4, c = tid & 15;
    {
      u32x4 v[5];
#pragma unroll
      for (int q = 0; q < 5; ++q) v[q] = *(const u32x4*)(p.u + (size_t)(b * 32 + row) * D_ + (c + 16 * q) * 8);
      float s = 0.f;
#pragma unroll
      for (int q = 0; q < 5; ++q)
#pragma unroll
        for (int e = 0; e < 4; ++e) s += lo16(v[q][e]) + hi16(v[q][e]);
      s = row_sum16(s);
      const float mean = s * (1.f / 640.f);
      float s2 = 0.f;
#pragma unroll
      for (int q = 0; q < 5; ++q)
#pragma unroll
        for (int e = 0; e < 4; ++e) {
          float a = lo16(v[q][e]) - mean, bb = hi16(v[q][e]) - mean;
          s2 += a * a + bb * bb;
        }
      s2 = row_sum16(s2);
      if (c == 0) {
        float var = rbf(s2 * (1.f / 640.f));
        sm[row] = rbf(mean);
        sd[row] = __builtin_amdgcn_rcpf(rbf(__builtin_amdgcn_sqrtf(rbf(var + 1e-5f))));
      }
#pragma unroll
      for (int q = 0; q < 5; ++q) *(u32x4*)(xs + row * XS + (c + 16 * q) * 8) = v[q];
    }
    __syncthreads();
    u16* nT = (u16*)(p.ws + WS_NT);
    u16* xT = (u16*)(p.ws + WS_XT);
    const int tq = tid & 3;
    float mm[8], dd[8];
#pragma unroll
    for (int e = 0; e < 8; ++e) { mm[e] = sm[tq * 8 + e]; dd[e] = sd[tq * 8 + e]; }
#pragma unroll
    for (int it = 0; it < 5; ++it) {
      const int d = (tid >> 2) + 128 * it;
      unsigned xo[4], no[4];
#pragma unroll
      for (int e2 = 0; e2 < 4; ++e2) {
        const int r0 = tq * 8 + e2 * 2;
        u16 a = xs[r0 * XS + d], bb = xs[(r0 + 1) * XS + d];
        xo[e2] = (unsigned)a | ((unsigned)bb << 16);
        float n0 = (bf2f(a) - mm[2 * e2]) * dd[2 * e2];
        float n1 = (bf2f(bb) - mm[2 * e2 + 1]) * dd[2 * e2 + 1];
        no[e2] = pack2(n0, n1);
      }
      const size_t o = (size_t)d * L_ + b * 32 + tq * 8;
      *(u32x4*)(xT + o) = (u32x4){xo[0], xo[1], xo[2], xo[3]};
      *(u32x4*)(nT + o) = (u32x4){no[0], no[1], no[2], no[3]};
    }
    return;
  }
  b -= A_LN;
  {
    const int ks0 = (b / 5) * 2, dc = b % 5;
    u16* ts = (u16*)smem;
    {
      const int r = tid >> 3, cq = tid & 7;
      const u16* src = p.w2 + (size_t)(ks0 * 32 + r) * D_ + dc * 128 + cq * 16;
      u32x4 a = *(const u32x4*)src, bb = *(const u32x4*)(src + 8);
      *(u32x4*)(ts + r * 136 + cq * 16) = a;
      *(u32x4*)(ts + r * 136 + cq * 16 + 8) = bb;
    }
    __syncthreads();
    u16* dst = (u16*)(p.ws + WS_W2);
#pragma unroll
    for (int it = 0; it < 2; ++it) {
      const int item = tid + 512 * it;
      const int lane = item & 63, dtl = (item >> 6) & 7, kh = item >> 9;
      const int dd = dtl * 16 + (lane & 15), k0 = kh * 32 + (lane >> 4) * 8;
      unsigned o[4];
#pragma unroll
      for (int e2 = 0; e2 < 4; ++e2)
        o[e2] = (unsigned)ts[(k0 + 2 * e2) * 136 + dd] | ((unsigned)ts[(k0 + 2 * e2 + 1) * 136 + dd] << 16);
      const int dt = dc * 8 + dtl;
      *(u32x4*)(dst + ((size_t)(dt * 54 + ks0 + kh) * 64 + lane) * 8) = (u32x4){o[0], o[1], o[2], o[3]};
    }
  }
}

constexpr int B_HB = 2 * KR_LEN * 2;
constexpr int B_HS = 4640;
constexpr int B_RED = B_HB + B_HS;
constexpr int B_LDS = B_RED + 32768;

template <int UMAX>
DEV void conv_residue(const char* smem, int o0, int lane, f32x4 (&acc)[8]) {
  const int r = lane & 15, kg = lane >> 4;
  const int rho = lane & 7, cb = (lane >> 3) & 1;
  bf16x8 bf[UMAX + 1];
  const int bbase = B_HB + rho * B_HS + 2 * (160 - o0 + 128 * cb + 8 * kg);
#pragma unroll
  for (int u = 0; u <= UMAX; ++u) bf[u] = *(const bf16x8*)(smem + bbase + 512 * u);
  const int abase = 2 * (2080 - o0 - 8 * r + 8 * kg);
#pragma unroll
  for (int i = 0; i < 8; ++i) {
    const bf16x8 ah = *(const bf16x8*)(smem + abase - 512 * i);
    const bf16x8 al = *(const bf16x8*)(smem + abase - 512 * i + KR_LEN * 2);
#pragma unroll
    for (int u = 0; u <= UMAX; ++u) {
      const int q = u + i;
      if (q < 8) {
        acc[q] = __builtin_amdgcn_mfma_f32_16x16x32_bf16(ah, bf[u], acc[q], 0, 0, 0);
        acc[q] = __builtin_amdgcn_mfma_f32_16x16x32_bf16(al, bf[u], acc[q], 0, 0, 0);
      }
    }
  }
}

DEV void h_mod(const u32x4 v, float t1, float sa, unsigned* wv) {
#pragma unroll
  for (int e = 0; e < 4; ++e) {
    float a = lo16(v[e]) * t1 + sa, bb = hi16(v[e]) * t1 + sa;
    wv[e] = pack2(a, bb);
  }
}
DEV void h_store(char* smem, int c, const unsigned* wv) {
#pragma unroll
  for (int rho = 0; rho < 8; ++rho) {
    u32x4 o;
#pragma unroll
    for (int e = 0; e < 4; ++e) {
      const int k = (rho >> 1) + e;
      o[e] = (rho & 1) ? __builtin_amdgcn_alignbit(wv[k + 1], wv[k], 16) : wv[k];
    }
    *(u32x4*)(smem + B_HB + rho * B_HS + c * 16) = o;
  }
}

__global__ __launch_bounds__(256, 3) void kB(Params p) {
  __shared__ __attribute__((aligned(16))) char smem[B_LDS];
  const int tid = threadIdx.x;
  const float* mod = (const float*)(p.ws + WS_MOD);
  if (blockIdx.x < 108) {
    const int fc = blockIdx.x >> 1, kh = blockIdx.x & 1;
    const int mat = tid >> 7, r = (tid >> 1) & 63, hc = tid & 1;
    const u16* src = (mat ? p.w3 : p.w1) + (size_t)(kh * 320 + r) * F_ + fc * 32 + hc * 16;
    u32x4 va[5], vb[5];
    float t1[5], sb[5];
#pragma unroll
    for (int j = 0; j < 5; ++j) {
      va[j] = *(const u32x4*)(src + (size_t)j * 64 * F_);
      vb[j] = *(const u32x4*)(src + (size_t)j * 64 * F_ + 8);
      t1[j] = mod[4 * 640 + kh * 320 + j * 64 + r];
      sb[j] = mod[3 * 640 + kh * 320 + j * 64 + r];
    }
    u16* ts = (u16*)smem;
    float* cs = (float*)(smem + 10240);
    float S[16], Bc[16];
#pragma unroll
    for (int e = 0; e < 16; ++e) { S[e] = 0.f; Bc[e] = 0.f; }
    u16* dst = (u16*)(p.ws + WS_W13);
#pragma unroll
    for (int j = 0; j < 5; ++j) {
      const float tt = rbf(1.f + t1[j]);
      u32x4 oa, ob;
#pragma unroll
      for (int e = 0; e < 4; ++e) {
        const float a0 = lo16(va[j][e]), a1 = hi16(va[j][e]), b0 = lo16(vb[j][e]), b1 = hi16(vb[j][e]);
        oa[e] = pack2(a0 * tt, a1 * tt);
        ob[e] = pack2(b0 * tt, b1 * tt);
        S[2 * e] += lo16(oa[e]); S[2 * e + 1] += hi16(oa[e]); S[8 + 2 * e] += lo16(ob[e]); S[8 + 2 * e + 1] += hi16(ob[e]);
        Bc[2 * e] += sb[j] * a0; Bc[2 * e + 1] += sb[j] * a1; Bc[8 + 2 * e] += sb[j] * b0; Bc[8 + 2 * e + 1] += sb[j] * b1;
      }
      if (j) __syncthreads();
      *(u32x4*)(ts + (mat * 64 + r) * 40 + hc * 16) = oa;
      *(u32x4*)(ts + (mat * 64 + r) * 40 + hc * 16 + 8) = ob;
      __syncthreads();
#pragma unroll
      for (int it = 0; it < 2; ++it) {
        const int item = tid + 256 * it;
        const int lane = item & 63, m2 = (item >> 6) & 1, ftl = (item >> 7) & 1, ksl = item >> 8;
        const u16* tb = ts + (m2 * 64 + ksl * 32 + (lane >> 4) * 8) * 40 + ftl * 16 + (lane & 15);
        unsigned o[4];
#pragma unroll
        for (int e2 = 0; e2 < 4; ++e2) o[e2] = (unsigned)tb[(2 * e2) * 40] | ((unsigned)tb[(2 * e2 + 1) * 40] << 16);
        const int ft = fc * 2 + ftl, ks = kh * 10 + j * 2 + ksl;
        *(u32x4*)(dst + ((size_t)((ft * 20 + ks) * 2 + m2) * 64 + lane) * 8) = (u32x4){o[0], o[1], o[2], o[3]};
      }
    }
#pragma unroll
    for (int e = 0; e < 4; ++e) {
      *(f32x4*)(cs + ((0 * 2 + mat) * 64 + r) * 32 + hc * 16 + 4 * e) = (f32x4){S[4 * e], S[4 * e + 1], S[4 * e + 2], S[4 * e + 3]};
      *(f32x4*)(cs + ((1 * 2 + mat) * 64 + r) * 32 + hc * 16 + 4 * e) = (f32x4){Bc[4 * e], Bc[4 * e + 1], Bc[4 * e + 2], Bc[4 * e + 3]};
    }
    __syncthreads();
    if (tid < 128) {
      const int col = tid & 31, sm2 = tid >> 5;
      float a = 0.f;
#pragma unroll 8
      for (int rr = 0; rr < 64; ++rr) a += cs[(sm2 * 64 + rr) * 32 + col];
      ((float*)(p.ws + WS_H2))[(kh * 4 + sm2) * F_ + fc * 32 + col] = a;
    }
    return;
  }
  const int d = blockIdx.x - 108;
  const u16* nT = (const u16*)(p.ws + WS_NT) + (size_t)d * L_;
  const u32x4* ksrc = (const u32x4*)((const u16*)(p.ws + WS_KR) + (size_t)d * 2 * KR_LEN);
  const u32x4 n0 = *(const u32x4*)(nT + tid * 8);
  const u32x4 n1 = *(const u32x4*)(nT + (tid < 255 ? tid * 8 + 8 : 0));
  const u32x4 k0 = ksrc[tid], k1 = ksrc[tid + 256];
  const u32x4 k2 = ksrc[tid < 40 ? tid + 512 : 0];
  const u32x4 xv = *(const u32x4*)((const u16*)(p.ws + WS_XT) + (size_t)d * L_ + tid * 8);
  const float sa = mod[d], cav = mod[640 + d], ga = mod[1280 + d];
  const float Dk = bf2f(p.Dskip[d]);
  const float t1 = rbf(1.f + cav);
  *(u32x4*)(smem + tid * 16) = k0;
  *(u32x4*)(smem + (tid + 256) * 16) = k1;
  if (tid < 40) *(u32x4*)(smem + (tid + 512) * 16) = k2;
  {
    unsigned wv[8];
    h_mod(n0, t1, sa, wv);
    h_mod(n1, t1, sa, wv + 4);
    if (tid == 255) { wv[4] = 0u; wv[5] = 0u; wv[6] = 0u; wv[7] = 0u; }
    h_store(smem, tid + 20, wv);
    unsigned zv[8];
#pragma unroll
    for (int e = 0; e < 4; ++e) {
      const unsigned f0 = __builtin_amdgcn_readfirstlane(wv[e]);
      zv[e] = 0u;
      zv[4 + e] = (tid == 19) ? f0 : 0u;
    }
    if (tid < 20) h_store(smem, tid, zv);
  }
  __syncthreads();
  const int w = __builtin_amdgcn_readfirstlane(tid >> 6), lane = tid & 63;
  f32x4 acc[8];
#pragma unroll
  for (int q = 0; q < 8; ++q) acc[q] = (f32x4){0.f, 0.f, 0.f, 0.f};
  conv_residue<7>(smem, -96 + 32 * w, lane, acc);
  conv_residue<7>(smem, -96 + 32 * (w + 4), lane, acc);
  if (w == 3) conv_residue<0>(smem, 160, lane, acc);
  __syncthreads();
  {
    const int rho = lane & 7, cb = (lane >> 3) & 1, rq = lane >> 4;
    const int sw = (rq & 1) | (cb << 1);
    float* red = (float*)(smem + B_RED) + w * 2048 + 128 * cb + 32 * rq + rho;
#pragma unroll
    for (int g = 0; g < 4; ++g) {
      float* rg = red + 8 * (g ^ sw);
#pragma unroll
      for (int q = 0; q < 8; ++q) rg[256 * q] = acc[q][g];
    }
  }
  __syncthreads();
  {
    const int t0 = tid * 8;
    const int pos = t0 ^ (((t0 >> 5) & 1) << 3) ^ (((t0 >> 7) & 1) << 4);
    const float* red = (const float*)(smem + B_RED);
    float y[8];
#pragma unroll
    for (int e = 0; e < 8; ++e) y[e] = 0.f;
#pragma unroll
    for (int ww = 0; ww < 4; ++ww) {
      f32x4 a = *(const f32x4*)(red + ww * 2048 + pos), bb = *(const f32x4*)(red + ww * 2048 + pos + 4);
#pragma unroll
      for (int e = 0; e < 4; ++e) { y[e] += a[e]; y[4 + e] += bb[e]; }
    }
    const u32x4 hv = *(const u32x4*)(smem + B_HB + 2 * (160 + t0));
    u32x4 o;
#pragma unroll
    for (int e = 0; e < 4; ++e) {
      float h0 = lo16(hv[e]), h1 = hi16(hv[e]);
      float y0 = rbf(rbf(y[2 * e]) + rbf(h0 * Dk)), y1 = rbf(rbf(y[2 * e + 1]) + rbf(h1 * Dk));
      float x0 = lo16(xv[e]) + rbf(ga * y0), x1 = hi16(xv[e]) + rbf(ga * y1);
      o[e] = pack2(x0, x1);
    }
    *(u32x4*)((u16*)(p.ws + WS_X1T) + ((size_t)(t0 >> 6) * D_ + d) * 64 + (t0 & 63)) = o;
  }
}

DEV int d_off(int k, int c) {
  const int f = ((k >> 1) & 1) | (((k >> 3) & 1) << 1);
  return k * 128 + ((((c >> 1) ^ f)) << 5) + ((c & 1) << 4);
}
DEV bf16x8 d_frag(const char* smem, int lane, int mi, int ks) {
  const int g = lane >> 4, q = (lane & 15) >> 2, pp = lane & 3;
  const int k0 = 32 * ks + 8 * g + q;
  const int c = 2 * mi + (pp >> 1);
  typedef __attribute__((address_space(3))) s16x4* lp;
  s16x4 a = __builtin_amdgcn_ds_read_tr16_b64_v4i16((lp)(smem + d_off(k0, c) + 8 * (pp & 1)));
  s16x4 bq = __builtin_amdgcn_ds_read_tr16_b64_v4i16((lp)(smem + d_off(k0 + 4, c) + 8 * (pp & 1)));
  return __builtin_shufflevector(a, bq, 0, 1, 2, 3, 4, 5, 6, 7);
}

DEV void d_load(const Params& p, int lane, int ft, int half, bf16x8 (&wf)[10][2]) {
  const bf16x8* wsrc = (const bf16x8*)(p.ws + WS_W13) + (size_t)(ft * 20 + half * 10) * 2 * 64 + lane;
#pragma unroll
  for (int ks = 0; ks < 10; ++ks) {
    wf[ks][0] = wsrc[(ks * 2) * 64];
    wf[ks][1] = wsrc[(ks * 2 + 1) * 64];
  }
}

DEV const bf16x8* d_wptr(const Params& p, int lane, int ft, int half) {
  return (const bf16x8*)(p.ws + WS_W13) + (size_t)(ft * 20 + half * 10) * 2 * 64 + lane;
}
template <int MT, bool RELOAD>
DEV void d_half(const char* smem, int lane, int mi0, int half, bf16x8 (&wf)[10][2], f32x4 (&acc)[2][4], const bf16x8* nsrc) {
#pragma unroll
  for (int ks = 0; ks < 10; ++ks) {
#pragma unroll
    for (int m = 0; m < MT; ++m) {
      const bf16x8 hf = d_frag(smem, lane, mi0 + m, half * 10 + ks);
      acc[0][m] = __builtin_amdgcn_mfma_f32_16x16x32_bf16(wf[ks][0], hf, acc[0][m], 0, 0, 0);
      acc[1][m] = __builtin_amdgcn_mfma_f32_16x16x32_bf16(wf[ks][1], hf, acc[1][m], 0, 0, 0);
    }
    if (RELOAD) {
      __builtin_amdgcn_sched_barrier(0);
      wf[ks][0] = nsrc[(ks * 2) * 64];
      wf[ks][1] = nsrc[(ks * 2 + 1) * 64];
      __builtin_amdgcn_sched_barrier(0);
    }
  }
}

template <int MT>
DEV void d_epi(const Params& p, const float* st, int lane, int ft, int mi0, int mt, f32x4 (&acc)[2][4]) {
  const int rq = lane >> 4;
  u16* gdst = (u16*)(p.ws + WS_G);
  const float* cs = (const float*)(p.ws + WS_H2) + ft * 16 + 4 * rq;
  f32x4 S[2], Bc[2];
#pragma unroll
  for (int mat = 0; mat < 2; ++mat) {
    S[mat] = *(const f32x4*)(cs + (0 + mat) * F_) + *(const f32x4*)(cs + (4 + mat) * F_);
    Bc[mat] = *(const f32x4*)(cs + (2 + mat) * F_) + *(const f32x4*)(cs + (6 + mat) * F_);
  }
#pragma unroll
  for (int m = 0; m < MT; ++m) {
    __builtin_amdgcn_sched_barrier(0);
    const float mu = st[(mi0 + m) * 16 + (lane & 15)], rs = st[64 + (mi0 + m) * 16 + (lane & 15)];
    float gv[4];
#pragma unroll
    for (int g = 0; g < 4; ++g) {
      const float gate = (acc[0][m][g] - mu * S[0][g]) * rs + Bc[0][g];
      const float up = (acc[1][m][g] - mu * S[1][g]) * rs + Bc[1][g];
      gv[g] = gate * up * __builtin_amdgcn_rcpf(1.f + __builtin_amdgcn_exp2f(gate * -1.44269504088896341f));
    }
    const int mtile = mt * 4 + mi0 + m;
    const int lane2 = (lane & 15) + 16 * (2 * (ft & 1) + (rq >> 1));
    u32x2 o = {pack2(gv[0], gv[1]), pack2(gv[2], gv[3])};
    *(u32x2*)(gdst + ((size_t)(mtile * 54 + (ft >> 1)) * 64 + lane2) * 8 + 4 * (rq & 1)) = o;
  }
#pragma unroll
  for (int m = 0; m < 4; ++m) { acc[0][m] = (f32x4){0.f, 0.f, 0.f, 0.f}; acc[1][m] = (f32x4){0.f, 0.f, 0.f, 0.f}; }
}

#define SB0 __builtin_amdgcn_sched_barrier(0)
#define RAWBAR() do { asm volatile("s_waitcnt lgkmcnt(0)" ::: "memory"); __builtin_amdgcn_s_barrier(); asm volatile("" ::: "memory"); } while (0)

DEV float sum_mod8(float x) {
  x = dpp_add<0x128>(x);
  x = sum_x16(x);
  x = sum_x32(x);
  return x;
}
typedef __attribute__((ext_vector_type(2))) float f32x2;
DEV f32x2 unpk(unsigned u) { return (f32x2){lo16(u), hi16(u)}; }

__global__ __launch_bounds__(512) void kD(Params p) {
  __shared__ __attribute__((aligned(16))) char smem[81920 + 4096 + 512];
  const int tid = threadIdx.x;
  const int xc = blockIdx.x & 7, jj = blockIdx.x >> 3;
#define D_MAP 0
  const int mt = (xc & 1) * 16 + (jj & 15), fpart = (xc >> 1) * 2 + (jj >> 4);
  const int w = __builtin_amdgcn_readfirstlane(tid >> 6), lane = tid & 63;
  float* ps = (float*)(smem + 81920);
  const float* mod = (const float*)(p.ws + WS_MOD);
  const int c = tid & 7, d0 = tid >> 3;
  const int fs = (27 * fpart) >> 1, fe = (27 * (fpart + 1)) >> 1;
  bf16x8 wA[10][2], wB[10][2];
  char* vdst = smem + d_off(d0, c);
  float* st = (float*)(smem + 81920 + 4096);
  u32x4 v[10], pv;
  {
    const u16* src = (const u16*)(p.ws + WS_X1T) + (size_t)mt * D_ * 64 + d0 * 64 + c * 8;
#pragma unroll
    for (int it = 0; it < 10; ++it) v[it] = *(const u32x4*)(src + it * 4096);
    pv = *(const u32x4*)(src - d0 * 64);
  }
  SB0;
  d_load(p, lane, fs + w, 0, wA);
  SB0;
  f32x2 piv[4], s1[4], s2[4];
#pragma unroll
  for (int e = 0; e < 4; ++e) { piv[e] = unpk(pv[e]); s1[e] = (f32x2){0.f, 0.f}; s2[e] = (f32x2){0.f, 0.f}; }
#pragma unroll
  for (int it = 0; it < 10; ++it) {
    *(u32x4*)(vdst + 8192 * it) = v[it];
#pragma unroll
    for (int e = 0; e < 4; ++e) { const f32x2 a = unpk(v[it][e]) - piv[e]; s1[e] += a; s2[e] += a * a; }
  }
  SB0;
  {
    float s[8], q[8];
#pragma unroll
    for (int e = 0; e < 8; ++e) { s[e] = sum_mod8(s1[e >> 1][e & 1]); q[e] = sum_mod8(s2[e >> 1][e & 1]); }
    if (lane < 8) {
      *(f32x4*)(ps + w * 64 + lane * 8) = (f32x4){s[0], s[1], s[2], s[3]};
      *(f32x4*)(ps + w * 64 + lane * 8 + 4) = (f32x4){s[4], s[5], s[6], s[7]};
      *(f32x4*)(ps + 512 + w * 64 + lane * 8) = (f32x4){q[0], q[1], q[2], q[3]};
      *(f32x4*)(ps + 512 + w * 64 + lane * 8 + 4) = (f32x4){q[4], q[5], q[6], q[7]};
    }
  }
  SB0;
  d_load(p, lane, fs + w, 1, wB);
  SB0;
  RAWBAR();
  if (w == 7) {
    float a = 0.f, a2 = 0.f;
#pragma unroll
    for (int ww = 0; ww < 8; ++ww) { a += ps[ww * 64 + lane]; a2 += ps[512 + ww * 64 + lane]; }
    const float m1 = a * (1.f / 640.f), m2 = a2 * (1.f / 640.f);
    const float var = rbf(fmaxf(m2 - m1 * m1, 0.f));
    const float pvt = bf2f(*(const u16*)(smem + d_off(0, lane >> 3) + (lane & 7) * 2));
    st[lane] = rbf(pvt + m1);
    st[64 + lane] = __builtin_amdgcn_rcpf(rbf(__builtin_amdgcn_sqrtf(rbf(var + 1e-5f))));
  }
  const bool big = (fe - fs == 14);
  f32x4 acc[2][4];
#pragma unroll
  for (int m = 0; m < 4; ++m) { acc[0][m] = (f32x4){0.f, 0.f, 0.f, 0.f}; acc[1][m] = (f32x4){0.f, 0.f, 0.f, 0.f}; }
  const int ft2 = (w < 4) ? fs + 8 + w : (big ? fs + 12 + ((w & 3) >> 1) : fs + 12);
  SB0;
  d_half<4, true>(smem, lane, 0, 0, wA, acc, d_wptr(p, lane, ft2, 0));
  d_half<4, true>(smem, lane, 0, 1, wB, acc, d_wptr(p, lane, ft2, 1));
  RAWBAR();
  d_epi<4>(p, st, lane, fs + w, 0, mt, acc);
  SB0;
  int ln = lane;
  asm volatile("" : "+v"(ln));
  if (w < 4) {
    d_half<4, false>(smem, ln, 0, 0, wA, acc, nullptr);
    d_half<4, false>(smem, ln, 0, 1, wB, acc, nullptr);
    d_epi<4>(p, st, ln, ft2, 0, mt, acc);
  } else if (big) {
    const int mi0 = 2 * (w & 1);
    d_half<2, false>(smem, ln, mi0, 0, wA, acc, nullptr);
    d_half<2, false>(smem, ln, mi0, 1, wB, acc, nullptr);
    d_epi<2>(p, st, ln, ft2, mi0, mt, acc);
  } else {
    const int mi0 = w & 3;
    d_half<1, false>(smem, ln, mi0, 0, wA, acc, nullptr);
    d_half<1, false>(smem, ln, mi0, 1, wB, acc, nullptr);
    d_epi<1>(p, st, ln, ft2, mi0, mt, acc);
  }
}

constexpr int E_RS = 84;
#define E_DEPTH_ 5
constexpr int E_DEPTH = E_DEPTH_;
__global__ __launch_bounds__(256) void kE(Params p) {
  __shared__ __attribute__((aligned(16))) float red[4 * 64 * E_RS + 80 * 36];
  const int tid = threadIdx.x, xc = blockIdx.x & 7, j = blockIdx.x >> 3;
#define E_MAP 0
  const int mt = (xc >> 1) * 8 + (j & 7), np = (xc & 1) * 4 + (j >> 3);
  const int w = __builtin_amdgcn_readfirstlane(tid >> 6), lane = tid & 63;
  const int kstart = (w < 2) ? 14 * w : 28 + 13 * (w - 2);
  const int kcnt = (w < 2) ? 14 : 13;
  const bf16x8* gsrc = (const bf16x8*)(p.ws + WS_G) + (size_t)(mt * 4) * 54 * 64 + kstart * 64 + lane;
  const bf16x8* wsrc = (const bf16x8*)(p.ws + WS_W2) + (size_t)(np * 5) * 54 * 64 + kstart * 64 + lane;
  f32x4 acc[5][4];
#pragma unroll
  for (int i = 0; i < 5; ++i)
#pragma unroll
    for (int m = 0; m < 4; ++m) acc[i][m] = (f32x4){0.f, 0.f, 0.f, 0.f};
  bf16x8 gf[E_DEPTH][4], wf[E_DEPTH][5];
#pragma unroll
  for (int s = 0; s < E_DEPTH; ++s) {
#pragma unroll
    for (int m = 0; m < 4; ++m) gf[s][m] = gsrc[(m * 54 + s) * 64];
#pragma unroll
    for (int i = 0; i < 5; ++i) wf[s][i] = wsrc[(i * 54 + s) * 64];
  }
  const float* mod = (const float*)(p.ws + WS_MOD);
  u16* xs = (u16*)(red + 4 * 64 * E_RS);
  u32x4 xa, xb, xc2;
  {
    const u16* x1t = (const u16*)(p.ws + WS_X1T) + ((size_t)mt * D_ + np * 80) * 64;
    xa = *(const u32x4*)(x1t + tid * 8);
    xb = *(const u32x4*)(x1t + (tid + 256) * 8);
    xc2 = *(const u32x4*)(x1t + (tid < 128 ? tid + 512 : 0) * 8);
  }
  SB0;
#pragma unroll
  for (int kk = 0; kk < 14; ++kk) {
    if (kk < kcnt) {
#pragma unroll
      for (int i = 0; i < 5; ++i)
#pragma unroll
        for (int m = 0; m < 4; ++m)
          acc[i][m] = __builtin_amdgcn_mfma_f32_16x16x32_bf16(wf[kk % E_DEPTH][i], gf[kk % E_DEPTH][m], acc[i][m], 0, 0, 0);
    }
    SB0;
    if (kk + E_DEPTH < kcnt) {
#pragma unroll
      for (int m = 0; m < 4; ++m) gf[kk % E_DEPTH][m] = gsrc[(m * 54 + kk + E_DEPTH) * 64];
#pragma unroll
      for (int i = 0; i < 5; ++i) wf[kk % E_DEPTH][i] = wsrc[(i * 54 + kk + E_DEPTH) * 64];
    }
    SB0;
  }
  {
    const int rq = lane >> 4;
#pragma unroll
    for (int i = 0; i < 5; ++i)
#pragma unroll
      for (int m = 0; m < 4; ++m)
        *(f32x4*)(red + (w * 64 + m * 16 + (lane & 15)) * E_RS + i * 16 + 4 * rq) = acc[i][m];
    *(u32x4*)(xs + (tid >> 3) * 72 + (tid & 7) * 8) = xa;
    *(u32x4*)(xs + ((tid + 256) >> 3) * 72 + (tid & 7) * 8) = xb;
    if (tid < 128) *(u32x4*)(xs + ((tid + 512) >> 3) * 72 + (tid & 7) * 8) = xc2;
  }
  __syncthreads();
  for (int item = tid; item < 640; item += 256) {
    const int t = item / 10, cc = item % 10;
    const int dcol = np * 80 + cc * 8;
    const size_t o = (size_t)(mt * 64 + t) * D_ + dcol;
    float xv[8];
#pragma unroll
    for (int e = 0; e < 8; ++e) xv[e] = bf2f(xs[(cc * 8 + e) * 72 + t]);
    const f32x4 g0 = *(const f32x4*)(mod + 5 * 640 + dcol), g1 = *(const f32x4*)(mod + 5 * 640 + dcol + 4);
    float y[8];
#pragma unroll
    for (int e = 0; e < 8; ++e) y[e] = 0.f;
#pragma unroll
    for (int ww = 0; ww < 4; ++ww) {
      f32x4 a = *(const f32x4*)(red + (ww * 64 + t) * E_RS + cc * 8), bb = *(const f32x4*)(red + (ww * 64 + t) * E_RS + cc * 8 + 4);
#pragma unroll
      for (int e = 0; e < 4; ++e) { y[e] += a[e]; y[4 + e] += bb[e]; }
    }
    u32x4 ov;
#pragma unroll
    for (int e = 0; e < 4; ++e) {
      const float ga = (e < 2) ? g0[2 * e] : g1[2 * e - 4], gb = (e < 2) ? g0[2 * e + 1] : g1[2 * e - 3];
      float r0 = xv[2 * e] + rbf(ga * rbf(y[2 * e])), r1 = xv[2 * e + 1] + rbf(gb * rbf(y[2 * e + 1]));
      ov[e] = pack2(r0, r1);
    }
    *(u32x4*)(p.out + o) = ov;
  }
}

extern "C" void kernel_launch(void* const* d_in, const int* in_sizes, int n_in,
                              void* d_out, int out_size, void* d_ws, size_t ws_size,
                              hipStream_t stream) {
  Params p;
  p.u = (const u16*)d_in[0];
  p.Are = (const u16*)d_in[1];
  p.Aim = (const u16*)d_in[2];
  p.Bre = (const u16*)d_in[3];
  p.Bim = (const u16*)d_in[4];
  p.Cre = (const u16*)d_in[5];
  p.Cim = (const u16*)d_in[6];
  p.logdt = (const u16*)d_in[7];
  p.Dskip = (const u16*)d_in[8];
  p.w1 = (const u16*)d_in[10];
  p.w3 = (const u16*)d_in[11];
  p.w2 = (const u16*)d_in[12];
  p.c = (const u16*)d_in[13];
  p.wmod = (const u16*)d_in[14];
  p.out = (u16*)d_out;
  p.ws = (char*)d_ws;
  kA<<<A_TOTAL, 512, 0, stream>>>(p);
  kB<<<748, 256, 0, stream>>>(p);
  kD<<<256, 512, 0, stream>>>(p);
  kE<<<256, 256, 0, stream>>>(p);
}
```

```cpp
#include <hip/hip_runtime.h>
#include <hip/hip_bf16.h>

typedef unsigned short u16;
typedef __attribute__((ext_vector_type(8))) short bf16x8;
typedef __attribute__((ext_vector_type(4))) short s16x4;
typedef __attribute__((ext_vector_type(4))) float f32x4;
typedef __attribute__((ext_vector_type(4))) unsigned u32x4;
typedef __attribute__((ext_vector_type(2))) unsigned u32x2;

#define DEV __device__ __forceinline__

constexpr int L_ = 2048, D_ = 640, N_ = 128, F_ = 1728;
constexpr int KR_LEN = 2208;

constexpr size_t WS_MOD = 0;
constexpr size_t WS_NT = 16384;
constexpr size_t WS_XT = WS_NT + (size_t)D_ * L_ * 2;
constexpr size_t WS_KR = WS_XT + (size_t)D_ * L_ * 2;
constexpr size_t WS_X1T = WS_KR + (size_t)D_ * 2 * KR_LEN * 2;
constexpr size_t WS_H2 = WS_X1T + (size_t)D_ * L_ * 2;
constexpr size_t WS_W13 = WS_H2 + (size_t)D_ * L_ * 2;
constexpr size_t WS_W2 = WS_W13 + (size_t)2 * D_ * F_ * 2;
constexpr size_t WS_G = WS_W2 + (size_t)D_ * F_ * 2;
constexpr size_t WS_END = WS_G + (size_t)L_ * F_ * 2;

DEV float bf2f(u16 x) { return __uint_as_float(((unsigned)x) << 16); }
DEV u16 f2bf(float f) { return __builtin_bit_cast(u16, (__bf16)f); }
DEV float rbf(float f) { return bf2f(f2bf(f)); }
DEV unsigned pack2(float a, float b) { return (unsigned)f2bf(a) | ((unsigned)f2bf(b) << 16); }
DEV float lo16(unsigned u) { return __uint_as_float(u << 16); }
DEV float hi16(unsigned u) { return __uint_as_float(u & 0xffff0000u); }

DEV void cexp(float ar, float ai, float w, float& er, float& ei) {
  const float mag = __builtin_amdgcn_exp2f(ar * w);
  const float rev = __builtin_amdgcn_fractf(ai * w);
  er = mag * __builtin_amdgcn_cosf(rev);
  ei = mag * __builtin_amdgcn_sinf(rev);
}

template <int CTRL>
DEV float dpp_add(float x) {
  return x + __builtin_bit_cast(float, __builtin_amdgcn_update_dpp(0, __builtin_bit_cast(int, x), CTRL, 0xf, 0xf, false));
}
DEV float sum_x16(float x) {
  float a = x, b = x;
  asm volatile("s_nop 1\n\tv_permlane16_swap_b32 %0, %1" : "+v"(a), "+v"(b));
  return a + b;
}
DEV float sum_x32(float x) {
  float a = x, b = x;
  asm volatile("s_nop 1\n\tv_permlane32_swap_b32 %0, %1" : "+v"(a), "+v"(b));
  return a + b;
}
DEV float row_sum16(float x) {
  x = dpp_add<0xB1>(x);
  x = dpp_add<0x4E>(x);
  x = dpp_add<0x124>(x);
  x = dpp_add<0x128>(x);
  return x;
}
DEV float sum_mod4(float x) {
  x = dpp_add<0x124>(x);
  x = dpp_add<0x128>(x);
  x = sum_x16(x);
  x = sum_x32(x);
  return x;
}

struct Params {
  const u16 *u, *Are, *Aim, *Bre, *Bim, *Cre, *Cim, *logdt, *Dskip, *w1, *w3, *w2, *c, *wmod;
  u16* out;
  char* ws;
};

constexpr int A_KGEN = 640, A_LN = 64, A_MOD = 60, A_W2 = 135;
constexpr int A_TOTAL = A_KGEN + A_LN + A_MOD + A_W2;

__global__ __launch_bounds__(512) void kA(Params p) {
  __shared__ __attribute__((aligned(16))) char smem[41984];
  const int tid = threadIdx.x;
  int b = blockIdx.x;
  if (b < A_MOD) {
    float* red = (float*)smem;
    const int cg = tid & 7, rg = tid >> 3;
    float acc[8];
#pragma unroll
    for (int e = 0; e < 8; ++e) acc[e] = 0.f;
    u32x4 wv[10];
    float cv[10];
#pragma unroll
    for (int r = 0; r < 10; ++r) {
      wv[r] = *(const u32x4*)(p.wmod + (size_t)(rg * 10 + r) * 3840 + b * 64 + cg * 8);
      cv[r] = bf2f(p.c[rg * 10 + r]);
    }
#pragma unroll
    for (int r = 0; r < 10; ++r) {
      float s = rbf(cv[r] / (1.f + __expf(-cv[r])));
#pragma unroll
      for (int e = 0; e < 4; ++e) {
        acc[2 * e] += s * lo16(wv[r][e]);
        acc[2 * e + 1] += s * hi16(wv[r][e]);
      }
    }
    *(f32x4*)(red + rg * 64 + cg * 8) = (f32x4){acc[0], acc[1], acc[2], acc[3]};
    *(f32x4*)(red + rg * 64 + cg * 8 + 4) = (f32x4){acc[4], acc[5], acc[6], acc[7]};
    __syncthreads();
    {
      const int col = tid & 63, g = tid >> 6;
      float s = 0.f;
#pragma unroll
      for (int r = 0; r < 8; ++r) s += red[(g * 8 + r) * 64 + col];
      float* red2 = red + 4096;
      red2[g * 64 + col] = s;
      __syncthreads();
      if (tid < 64) {
        float t = 0.f;
#pragma unroll
        for (int r = 0; r < 8; ++r) t += red2[r * 64 + tid];
        ((float*)(p.ws + WS_MOD))[b * 64 + tid] = rbf(t);
      }
    }
    return;
  }
  b -= A_MOD;
  if (b < A_KGEN) {
    const int d = b;
    float* red = (float*)smem;
    float* Kv = (float*)(smem + 24576);
    f32x4* prm = (f32x4*)(smem + 32784);
    const int w = tid >> 6, l = tid & 63, i = l & 15, kq = l >> 4;
    if (tid < 128) {
      const int n = d * N_ + tid;
      const u16 a0 = p.Are[n], a1 = p.Aim[n], b0 = p.Bre[n], b1 = p.Bim[n], c0 = p.Cre[n], c1 = p.Cim[n];
      const float dt = rbf(__expf(bf2f(p.logdt[d])));
      const float dBr = dt * bf2f(b0), dBi = dt * bf2f(b1);
      const float Cr = bf2f(c0), Ci = bf2f(c1);
      prm[tid] = (f32x4){dt * bf2f(a0) * 1.44269504088896341f, dt * bf2f(a1) * 0.15915494309189535f, Cr * dBr - Ci * dBi, Cr * dBi + Ci * dBr};
    }
    const int o0 = (i & 7) + 128 * (i >> 3);
    const int o1 = (i < 8) ? 256 + 2 * (i & 3) + 128 * (i >> 2) : 512 + 4 * (i & 1) + 128 * ((i >> 1) & 3);
    const int o2 = 1024 + 128 * i;
    const float fo0 = (float)o0, fo1 = (float)o1, fo2 = (float)o2, fj = (float)(8 * i);
    __syncthreads();
    f32x4 acc[3];
#pragma unroll
    for (int s = 0; s < 3; ++s) acc[s] = (f32x4){0.f, 0.f, 0.f, 0.f};
#pragma unroll 2
    for (int kk = 0; kk < 4; ++kk) {
      const f32x4 pr = prm[16 * w + 4 * kk + kq];
      const float ar = pr[0], ai = pr[1], CBr = pr[2], CBi = pr[3];
      float yr, yi;
      cexp(ar, ai, fj, yr, yi);
#pragma unroll
      for (int s = 0; s < 3; ++s) {
        float zr, zi;
        cexp(ar, ai, s == 0 ? fo0 : (s == 1 ? fo1 : fo2), zr, zi);
        const float xr = CBr * zr - CBi * zi, xin = -(CBr * zi + CBi * zr);
        acc[s] = __builtin_amdgcn_mfma_f32_16x16x4f32(yr, xr, acc[s], 0, 0, 0);
        acc[s] = __builtin_amdgcn_mfma_f32_16x16x4f32(yi, xin, acc[s], 0, 0, 0);
      }
    }
#pragma unroll
    for (int s = 0; s < 3; ++s) *(f32x4*)(red + (w * 3 + s) * 256 + i * 16 + 4 * kq) = acc[s];
    __syncthreads();
    {
      const int t8 = tid & 255, cI = t8 >> 4, j = t8 & 15, tl = tid >> 8;
      const int p0 = (cI & 7) + 128 * (cI >> 3);
      const int p1 = (cI < 8) ? 256 + 2 * (cI & 3) + 128 * (cI >> 2) : 512 + 4 * (cI & 1) + 128 * ((cI >> 1) & 3);
      float sum = 0.f;
#pragma unroll
      for (int ww = 0; ww < 8; ++ww) sum += red[(ww * 3 + tl) * 256 + t8];
      Kv[(tl ? p1 : p0) + 8 * j] = sum;
      if (tid <= 128) {
        float s2 = 0.f;
#pragma unroll
        for (int ww = 0; ww < 8; ++ww) s2 += red[(ww * 3 + 2) * 256 + tid];
        Kv[1024 + 128 * cI + 8 * j] = s2;
      }
    }
    __syncthreads();
    u16* Kr = (u16*)(p.ws + WS_KR) + (size_t)d * 2 * KR_LEN;
    for (int c = tid; c < KR_LEN / 4; c += 512) {
      unsigned hw[2], lw[2];
#pragma unroll
      for (int e2 = 0; e2 < 2; ++e2) {
        float kv[2];
#pragma unroll
        for (int z = 0; z < 2; ++z) {
          const int k = 2080 - (c * 4 + e2 * 2 + z);
          const int kc = min(max(k, 0), 2047);
          const float v = Kv[(int)rbf((float)kc)];
          kv[z] = (k == kc) ? v : 0.f;
        }
        float h0 = rbf(kv[0]), h1 = rbf(kv[1]);
        hw[e2] = pack2(h0, h1);
        lw[e2] = pack2(kv[0] - h0, kv[1] - h1);
      }
      *(u32x2*)(Kr + c * 4) = (u32x2){hw[0], hw[1]};
      *(u32x2*)(Kr + KR_LEN + c * 4) = (u32x2){lw[0], lw[1]};
    }
    return;
  }
  b -= A_KGEN;
  if (b < A_LN) {
    constexpr int XS = 648;
    u16* xs = (u16*)smem;
    float* sm = (float*)(smem + 32 * XS * 2);
    float* sd = sm + 32;
    const int row = tid >> 4, c = tid & 15;
    {
      u32x4 v[5];
#pragma unroll
      for (int q = 0; q < 5; ++q) v[q] = *(const u32x4*)(p.u + (size_t)(b * 32 + row) * D_ + (c + 16 * q) * 8);
      float s = 0.f;
#pragma unroll
      for (int q = 0; q < 5; ++q)
#pragma unroll
        for (int e = 0; e < 4; ++e) s += lo16(v[q][e]) + hi16(v[q][e]);
      s = row_sum16(s);
      const float mean = s * (1.f / 640.f);
      float s2 = 0.f;
#pragma unroll
      for (int q = 0; q < 5; ++q)
#pragma unroll
        for (int e = 0; e < 4; ++e) {
          float a = lo16(v[q][e]) - mean, bb = hi16(v[q][e]) - mean;
          s2 += a * a + bb * bb;
        }
      s2 = row_sum16(s2);
      if (c == 0) {
        float var = rbf(s2 * (1.f / 640.f));
        sm[row] = rbf(mean);
        sd[row] = __builtin_amdgcn_rcpf(rbf(__builtin_amdgcn_sqrtf(rbf(var + 1e-5f))));
      }
#pragma unroll
      for (int q = 0; q < 5; ++q) *(u32x4*)(xs + row * XS + (c + 16 * q) * 8) = v[q];
    }
    __syncthreads();
    u16* nT = (u16*)(p.ws + WS_NT);
    u16* xT = (u16*)(p.ws + WS_XT);
    const int tq = tid & 3;
    float mm[8], dd[8];
#pragma unroll
    for (int e = 0; e < 8; ++e) { mm[e] = sm[tq * 8 + e]; dd[e] = sd[tq * 8 + e]; }
#pragma unroll
    for (int it = 0; it < 5; ++it) {
      const int d = (tid >> 2) + 128 * it;
      unsigned xo[4], no[4];
#pragma unroll
      for (int e2 = 0; e2 < 4; ++e2) {
        const int r0 = tq * 8 + e2 * 2;
        u16 a = xs[r0 * XS + d], bb = xs[(r0 + 1) * XS + d];
        xo[e2] = (unsigned)a | ((unsigned)bb << 16);
        float n0 = (bf2f(a) - mm[2 * e2]) * dd[2 * e2];
        float n1 = (bf2f(bb) - mm[2 * e2 + 1]) * dd[2 * e2 + 1];
        no[e2] = pack2(n0, n1);
      }
      const size_t o = (size_t)d * L_ + b * 32 + tq * 8;
      *(u32x4*)(xT + o) = (u32x4){xo[0], xo[1], xo[2], xo[3]};
      *(u32x4*)(nT + o) = (u32x4){no[0], no[1], no[2], no[3]};
    }
    return;
  }
  b -= A_LN;
  {
    const int ks0 = (b / 5) * 2, dc = b % 5;
    u16* ts = (u16*)smem;
    {
      const int r = tid >> 3, cq = tid & 7;
      const u16* src = p.w2 + (size_t)(ks0 * 32 + r) * D_ + dc * 128 + cq * 16;
      u32x4 a = *(const u32x4*)src, bb = *(const u32x4*)(src + 8);
      *(u32x4*)(ts + r * 136 + cq * 16) = a;
      *(u32x4*)(ts + r * 136 + cq * 16 + 8) = bb;
    }
    __syncthreads();
    u16* dst = (u16*)(p.ws + WS_W2);
#pragma unroll
    for (int it = 0; it < 2; ++it) {
      const int item = tid + 512 * it;
      const int lane = item & 63, dtl = (item >> 6) & 7, kh = item >> 9;
      const int dd = dtl * 16 + (lane & 15), k0 = kh * 32 + (lane >> 4) * 8;
      unsigned o[4];
#pragma unroll
      for (int e2 = 0; e2 < 4; ++e2)
        o[e2] = (unsigned)ts[(k0 + 2 * e2) * 136 + dd] | ((unsigned)ts[(k0 + 2 * e2 + 1) * 136 + dd] << 16);
      const int dt = dc * 8 + dtl;
      *(u32x4*)(dst + ((size_t)(dt * 54 + ks0 + kh) * 64 + lane) * 8) = (u32x4){o[0], o[1], o[2], o[3]};
    }
  }
}

constexpr int B_HB = 2 * KR_LEN * 2;
constexpr int B_HS = 4640;
constexpr int B_RED = B_HB + B_HS;
constexpr int B_LDS = B_RED + 32768;

template <int UMAX>
DEV void conv_residue(const char* smem, int o0, int lane, f32x4 (&acc)[8]) {
  const int r = lane & 15, kg = lane >> 4;
  const int rho = lane & 7, cb = (lane >> 3) & 1;
  bf16x8 bf[UMAX + 1];
  const int bbase = B_HB + rho * B_HS + 2 * (160 - o0 + 128 * cb + 8 * kg);
#pragma unroll
  for (int u = 0; u <= UMAX; ++u) bf[u] = *(const bf16x8*)(smem + bbase + 512 * u);
  const int abase = 2 * (2080 - o0 - 8 * r + 8 * kg);
#pragma unroll
  for (int i = 0; i < 8; ++i) {
    const bf16x8 ah = *(const bf16x8*)(smem + abase - 512 * i);
    const bf16x8 al = *(const bf16x8*)(smem + abase - 512 * i + KR_LEN * 2);
#pragma unroll
    for (int u = 0; u <= UMAX; ++u) {
      const int q = u + i;
      if (q < 8) {
        acc[q] = __builtin_amdgcn_mfma_f32_16x16x32_bf16(ah, bf[u], acc[q], 0, 0, 0);
        acc[q] = __builtin_amdgcn_mfma_f32_16x16x32_bf16(al, bf[u], acc[q], 0, 0, 0);
      }
    }
  }
}

DEV void h_mod(const u32x4 v, float t1, float sa, unsigned* wv) {
#pragma unroll
  for (int e = 0; e < 4; ++e) {
    float a = lo16(v[e]) * t1 + sa, bb = hi16(v[e]) * t1 + sa;
    wv[e] = pack2(a, bb);
  }
}
DEV void h_store(char* smem, int c, const unsigned* wv) {
#pragma unroll
  for (int rho = 0; rho < 8; ++rho) {
    u32x4 o;
#pragma unroll
    for (int e = 0; e < 4; ++e) {
      const int k = (rho >> 1) + e;
      o[e] = (rho & 1) ? __builtin_amdgcn_alignbit(wv[k + 1], wv[k], 16) : wv[k];
    }
    *(u32x4*)(smem + B_HB + rho * B_HS + c * 16) = o;
  }
}

__global__ __launch_bounds__(256, 3) void kB(Params p) {
  __shared__ __attribute__((aligned(16))) char smem[B_LDS];
  const int tid = threadIdx.x;
  const float* mod = (const float*)(p.ws + WS_MOD);
  if (blockIdx.x < 108) {
    const int fc = blockIdx.x >> 1, kh = blockIdx.x & 1;
    const int mat = tid >> 7, r = (tid >> 1) & 63, hc = tid & 1;
    const u16* src = (mat ? p.w3 : p.w1) + (size_t)(kh * 320 + r) * F_ + fc * 32 + hc * 16;
    u32x4 va[5], vb[5];
    float t1[5], sb[5];
#pragma unroll
    for (int j = 0; j < 5; ++j) {
      va[j] = *(const u32x4*)(src + (size_t)j * 64 * F_);
      vb[j] = *(const u32x4*)(src + (size_t)j * 64 * F_ + 8);
      t1[j] = mod[4 * 640 + kh * 320 + j * 64 + r];
      sb[j] = mod[3 * 640 + kh * 320 + j * 64 + r];
    }
    u16* ts = (u16*)smem;
    float* cs = (float*)(smem + 10240);
    float S[16], Bc[16];
#pragma unroll
    for (int e = 0; e < 16; ++e) { S[e] = 0.f; Bc[e] = 0.f; }
    u16* dst = (u16*)(p.ws + WS_W13);
#pragma unroll
    for (int j = 0; j < 5; ++j) {
      const float tt = rbf(1.f + t1[j]);
      u32x4 oa, ob;
#pragma unroll
      for (int e = 0; e < 4; ++e) {
        const float a0 = lo16(va[j][e]), a1 = hi16(va[j][e]), b0 = lo16(vb[j][e]), b1 = hi16(vb[j][e]);
        oa[e] = pack2(a0 * tt, a1 * tt);
        ob[e] = pack2(b0 * tt, b1 * tt);
        S[2 * e] += lo16(oa[e]); S[2 * e + 1] += hi16(oa[e]); S[8 + 2 * e] += lo16(ob[e]); S[8 + 2 * e + 1] += hi16(ob[e]);
        Bc[2 * e] += sb[j] * a0; Bc[2 * e + 1] += sb[j] * a1; Bc[8 + 2 * e] += sb[j] * b0; Bc[8 + 2 * e + 1] += sb[j] * b1;
      }
      if (j) __syncthreads();
      *(u32x4*)(ts + (mat * 64 + r) * 40 + hc * 16) = oa;
      *(u32x4*)(ts + (mat * 64 + r) * 40 + hc * 16 + 8) = ob;
      __syncthreads();
#pragma unroll
      for (int it = 0; it < 2; ++it) {
        const int item = tid + 256 * it;
        const int lane = item & 63, m2 = (item >> 6) & 1, ftl = (item >> 7) & 1, ksl = item >> 8;
        const u16* tb = ts + (m2 * 64 + ksl * 32 + (lane >> 4) * 8) * 40 + ftl * 16 + (lane & 15);
        unsigned o[4];
#pragma unroll
        for (int e2 = 0; e2 < 4; ++e2) o[e2] = (unsigned)tb[(2 * e2) * 40] | ((unsigned)tb[(2 * e2 + 1) * 40] << 16);
        const int ft = fc * 2 + ftl, ks = kh * 10 + j * 2 + ksl;
        *(u32x4*)(dst + ((size_t)((ft * 20 + ks) * 2 + m2) * 64 + lane) * 8) = (u32x4){o[0], o[1], o[2], o[3]};
      }
    }
#pragma unroll
    for (int e = 0; e < 4; ++e) {
      *(f32x4*)(cs + ((0 * 2 + mat) * 64 + r) * 32 + hc * 16 + 4 * e) = (f32x4){S[4 * e], S[4 * e + 1], S[4 * e + 2], S[4 * e + 3]};
      *(f32x4*)(cs + ((1 * 2 + mat) * 64 + r) * 32 + hc * 16 + 4 * e) = (f32x4){Bc[4 * e], Bc[4 * e + 1], Bc[4 * e + 2], Bc[4 * e + 3]};
    }
    __syncthreads();
    if (tid < 128) {
      const int col = tid & 31, sm2 = tid >> 5;
      float a = 0.f;
#pragma unroll 8
      for (int rr = 0; rr < 64; ++rr) a += cs[(sm2 * 64 + rr) * 32 + col];
      ((float*)(p.ws + WS_H2))[(kh * 4 + sm2) * F_ + fc * 32 + col] = a;
    }
    return;
  }
  const int d = blockIdx.x - 108;
  const u16* nT = (const u16*)(p.ws + WS_NT) + (size_t)d * L_;
  const u32x4* ksrc = (const u32x4*)((const u16*)(p.ws + WS_KR) + (size_t)d * 2 * KR_LEN);
  const u32x4 n0 = *(const u32x4*)(nT + tid * 8);
  const u32x4 n1 = *(const u32x4*)(nT + (tid < 255 ? tid * 8 + 8 : 0));
  const u32x4 k0 = ksrc[tid], k1 = ksrc[tid + 256];
  const u32x4 k2 = ksrc[tid < 40 ? tid + 512 : 0];
  const u32x4 xv = *(const u32x4*)((const u16*)(p.ws + WS_XT) + (size_t)d * L_ + tid * 8);
  const float sa = mod[d], cav = mod[640 + d], ga = mod[1280 + d];
  const float Dk = bf2f(p.Dskip[d]);
  const float t1 = rbf(1.f + cav);
  *(u32x4*)(smem + tid * 16) = k0;
  *(u32x4*)(smem + (tid + 256) * 16) = k1;
  if (tid < 40) *(u32x4*)(smem + (tid + 512) * 16) = k2;
  {
    unsigned wv[8];
    h_mod(n0, t1, sa, wv);
    h_mod(n1, t1, sa, wv + 4);
    if (tid == 255) { wv[4] = 0u; wv[5] = 0u; wv[6] = 0u; wv[7] = 0u; }
    h_store(smem, tid + 20, wv);
    unsigned zv[8];
#pragma unroll
    for (int e = 0; e < 4; ++e) {
      const unsigned f0 = __builtin_amdgcn_readfirstlane(wv[e]);
      zv[e] = 0u;
      zv[4 + e] = (tid == 19) ? f0 : 0u;
    }
    if (tid < 20) h_store(smem, tid, zv);
  }
  __syncthreads();
  const int w = __builtin_amdgcn_readfirstlane(tid >> 6), lane = tid & 63;
  f32x4 acc[8];
#pragma unroll
  for (int q = 0; q < 8; ++q) acc[q] = (f32x4){0.f, 0.f, 0.f, 0.f};
  conv_residue<7>(smem, -96 + 32 * w, lane, acc);
  conv_residue<7>(smem, -96 + 32 * (w + 4), lane, acc);
  if (w == 3) conv_residue<0>(smem, 160, lane, acc);
  __syncthreads();
  {
    const int rho = lane & 7, cb = (lane >> 3) & 1, rq = lane >> 4;
    const int sw = (rq & 1) | (cb << 1);
    float* red = (float*)(smem + B_RED) + w * 2048 + 128 * cb + 32 * rq + rho;
#pragma unroll
    for (int g = 0; g < 4; ++g) {
      float* rg = red + 8 * (g ^ sw);
#pragma unroll
      for (int q = 0; q < 8; ++q) rg[256 * q] = acc[q][g];
    }
  }
  __syncthreads();
  {
    const int t0 = tid * 8;
    const int pos = t0 ^ (((t0 >> 5) & 1) << 3) ^ (((t0 >> 7) & 1) << 4);
    const float* red = (const float*)(smem + B_RED);
    float y[8];
#pragma unroll
    for (int e = 0; e < 8; ++e) y[e] = 0.f;
#pragma unroll
    for (int ww = 0; ww < 4; ++ww) {
      f32x4 a = *(const f32x4*)(red + ww * 2048 + pos), bb = *(const f32x4*)(red + ww * 2048 + pos + 4);
#pragma unroll
      for (int e = 0; e < 4; ++e) { y[e] += a[e]; y[4 + e] += bb[e]; }
    }
    const u32x4 hv = *(const u32x4*)(smem + B_HB + 2 * (160 + t0));
    u32x4 o;
#pragma unroll
    for (int e = 0; e < 4; ++e) {
      float h0 = lo16(hv[e]), h1 = hi16(hv[e]);
      float y0 = rbf(rbf(y[2 * e]) + rbf(h0 * Dk)), y1 = rbf(rbf(y[2 * e + 1]) + rbf(h1 * Dk));
      float x0 = lo16(xv[e]) + rbf(ga * y0), x1 = hi16(xv[e]) + rbf(ga * y1);
      o[e] = pack2(x0, x1);
    }
    *(u32x4*)((u16*)(p.ws + WS_X1T) + ((size_t)(t0 >> 6) * D_ + d) * 64 + (t0 & 63)) = o;
  }
}

#define SB0 __builtin_amdgcn_sched_barrier(0)
#define RAWBAR() do { asm volatile("s_waitcnt lgkmcnt(0)" ::: "memory"); __builtin_amdgcn_s_barrier(); asm volatile("" ::: "memory"); } while (0)
typedef __attribute__((ext_vector_type(2))) float f32x2;
DEV f32x2 unpk(unsigned u) { return (f32x2){lo16(u), hi16(u)}; }

DEV int d_off(int k, int c) {
  const int f = (k & 3) | (((k >> 3) & 1) << 2);
  return k * 256 + (((c >> 1) ^ f) << 5) + ((c & 1) << 4);
}

__global__ __launch_bounds__(512) void kD(Params p) {
  __shared__ __attribute__((aligned(16))) char smem[2 * 32768 + 8192 + 1024];
  const int tid = threadIdx.x;
  const int xc = blockIdx.x & 7, jj = blockIdx.x >> 3;
  const int mt = (xc & 1) * 8 + (jj & 7), fpart = (xc >> 1) * 4 + (jj >> 3);
  const int w = __builtin_amdgcn_readfirstlane(tid >> 6), lane = tid & 63;
  float* ps = (float*)(smem + 65536);
  float* st = (float*)(smem + 65536 + 8192);
  const int fs = (27 * fpart) >> 2, fe = (27 * (fpart + 1)) >> 2;
  const bool active = (fs + w < fe);
  const int ft = active ? fs + w : fs;
  const int c = tid & 15, r0 = tid >> 4;
  const u16* xsrc = (const u16*)(p.ws + WS_X1T) + ((size_t)(mt * 2 + (c >> 3)) * D_ + r0) * 64 + (c & 7) * 8;
  const int sdst = d_off(r0, c);
  u32x4 v[4];
#pragma unroll
  for (int j = 0; j < 4; ++j) v[j] = *(const u32x4*)(xsrc + j * 32 * 64);
  const u32x4 pv = *(const u32x4*)(xsrc - r0 * 64);
  const bf16x8* wsrc = (const bf16x8*)(p.ws + WS_W13) + (size_t)ft * 20 * 2 * 64 + lane;
  bf16x8 wf[8][2];
  SB0;
#pragma unroll
  for (int ks = 0; ks < 8; ++ks) { wf[ks][0] = wsrc[(ks * 2) * 64]; wf[ks][1] = wsrc[(ks * 2 + 1) * 64]; }
  SB0;
  f32x2 piv[4], s1[4], s2[4];
#pragma unroll
  for (int e = 0; e < 4; ++e) { piv[e] = unpk(pv[e]); s1[e] = (f32x2){0.f, 0.f}; s2[e] = (f32x2){0.f, 0.f}; }
  auto stage = [&](int buf) {
#pragma unroll
    for (int j = 0; j < 4; ++j) {
      *(u32x4*)(smem + buf * 32768 + sdst + 8192 * j) = v[j];
#pragma unroll
      for (int e = 0; e < 4; ++e) { const f32x2 a = unpk(v[j][e]) - piv[e]; s1[e] += a; s2[e] += a * a; }
    }
  };
  stage(0);
  SB0;
#pragma unroll
  for (int j = 0; j < 4; ++j) v[j] = *(const u32x4*)(xsrc + (128 + j * 32) * 64);
  SB0;
  RAWBAR();
  f32x4 acc[2][8];
#pragma unroll
  for (int m = 0; m < 8; ++m) { acc[0][m] = (f32x4){0.f, 0.f, 0.f, 0.f}; acc[1][m] = (f32x4){0.f, 0.f, 0.f, 0.f}; }
  const int g = lane >> 4, q = (lane & 15) >> 2, pp = lane & 3;
  int fo[8];
#pragma unroll
  for (int m = 0; m < 8; ++m) fo[m] = d_off(8 * g + q, 2 * m + (pp >> 1)) + 8 * (pp & 1);
  typedef __attribute__((address_space(3))) s16x4* lp;
#pragma unroll
  for (int ch = 0; ch < 5; ++ch) {
    if (ch < 4) {
      stage((ch + 1) & 1);
      SB0;
      if (ch < 3) {
#pragma unroll
        for (int j = 0; j < 4; ++j) v[j] = *(const u32x4*)(xsrc + ((ch + 2) * 128 + j * 32) * 64);
      }
      SB0;
    }
    if (active) {
      const char* bufp = smem + (ch & 1) * 32768;
#pragma unroll
      for (int k4 = 0; k4 < 4; ++k4) {
        const int ks = ch * 4 + k4;
#pragma unroll
        for (int m = 0; m < 8; ++m) {
          s16x4 a = __builtin_amdgcn_ds_read_tr16_b64_v4i16((lp)(bufp + fo[m] + 8192 * k4));
          s16x4 bq = __builtin_amdgcn_ds_read_tr16_b64_v4i16((lp)(bufp + fo[m] + 8192 * k4 + 1024));
          const bf16x8 hf = __builtin_shufflevector(a, bq, 0, 1, 2, 3, 4, 5, 6, 7);
          acc[0][m] = __builtin_amdgcn_mfma_f32_16x16x32_bf16(wf[ks & 7][0], hf, acc[0][m], 0, 0, 0);
          acc[1][m] = __builtin_amdgcn_mfma_f32_16x16x32_bf16(wf[ks & 7][1], hf, acc[1][m], 0, 0, 0);
        }
        if (ks + 8 < 20) {
          SB0;
          wf[ks & 7][0] = wsrc[((ks + 8) * 2) * 64];
          wf[ks & 7][1] = wsrc[((ks + 8) * 2 + 1) * 64];
          SB0;
        }
      }
    }
    if (ch == 3) {
      float s[8], qq[8];
#pragma unroll
      for (int e = 0; e < 8; ++e) { s[e] = sum_x32(sum_x16(s1[e >> 1][e & 1])); qq[e] = sum_x32(sum_x16(s2[e >> 1][e & 1])); }
      if (lane < 16) {
        *(f32x4*)(ps + w * 128 + lane * 8) = (f32x4){s[0], s[1], s[2], s[3]};
        *(f32x4*)(ps + w * 128 + lane * 8 + 4) = (f32x4){s[4], s[5], s[6], s[7]};
        *(f32x4*)(ps + 1024 + w * 128 + lane * 8) = (f32x4){qq[0], qq[1], qq[2], qq[3]};
        *(f32x4*)(ps + 1024 + w * 128 + lane * 8 + 4) = (f32x4){qq[4], qq[5], qq[6], qq[7]};
      }
    }
    if (ch < 4) RAWBAR();
    if (ch == 3 && w == 7) {
#pragma unroll
      for (int hh = 0; hh < 2; ++hh) {
        const int t = lane + 64 * hh;
        float a = 0.f, a2 = 0.f;
#pragma unroll
        for (int ww = 0; ww < 8; ++ww) { a += ps[ww * 128 + t]; a2 += ps[1024 + ww * 128 + t]; }
        const float m1 = a * (1.f / 640.f), m2 = a2 * (1.f / 640.f);
        const float var = rbf(fmaxf(m2 - m1 * m1, 0.f));
        const float pvt = bf2f(((const u16*)(p.ws + WS_X1T))[(size_t)(mt * 2 + hh) * D_ * 64 + lane]);
        st[t] = rbf(pvt + m1);
        st[128 + t] = __builtin_amdgcn_rcpf(rbf(__builtin_amdgcn_sqrtf(rbf(var + 1e-5f))));
      }
    }
  }
  RAWBAR();
  if (active) {
    const int rq = lane >> 4;
    u16* gdst = (u16*)(p.ws + WS_G);
    const float* cs = (const float*)(p.ws + WS_H2) + ft * 16 + 4 * rq;
    f32x4 S[2], Bc[2];
#pragma unroll
    for (int mat = 0; mat < 2; ++mat) {
      S[mat] = *(const f32x4*)(cs + (0 + mat) * F_) + *(const f32x4*)(cs + (4 + mat) * F_);
      Bc[mat] = *(const f32x4*)(cs + (2 + mat) * F_) + *(const f32x4*)(cs + (6 + mat) * F_);
    }
#pragma unroll
    for (int m = 0; m < 8; ++m) {
      const float mu = st[m * 16 + (lane & 15)], rs = st[128 + m * 16 + (lane & 15)];
      float gv[4];
#pragma unroll
      for (int gg = 0; gg < 4; ++gg) {
        const float gate = (acc[0][m][gg] - mu * S[0][gg]) * rs + Bc[0][gg];
        const float up = (acc[1][m][gg] - mu * S[1][gg]) * rs + Bc[1][gg];
        gv[gg] = gate * up * __builtin_amdgcn_rcpf(1.f + __builtin_amdgcn_exp2f(gate * -1.44269504088896341f));
      }
      const int mtile = mt * 8 + m;
      const int lane2 = (lane & 15) + 16 * (2 * (ft & 1) + (rq >> 1));
      u32x2 o = {pack2(gv[0], gv[1]), pack2(gv[2], gv[3])};
      *(u32x2*)(gdst + ((size_t)(mtile * 54 + (ft >> 1)) * 64 + lane2) * 8 + 4 * (rq & 1)) = o;
    }
  }
}

constexpr int E_RS = 84;
#define E_DEPTH_ 5
constexpr int E_DEPTH = E_DEPTH_;
__global__ __launch_bounds__(256) void kE(Params p) {
  __shared__ __attribute__((aligned(16))) float red[4 * 64 * E_RS + 80 * 36];
  const int tid = threadIdx.x, xc = blockIdx.x & 7, j = blockIdx.x >> 3;
#define E_MAP 0
  const int mt = (xc >> 1) * 8 + (j & 7), np = (xc & 1) * 4 + (j >> 3);
  const int w = __builtin_amdgcn_readfirstlane(tid >> 6), lane = tid & 63;
  const int kstart = (w < 2) ? 14 * w : 28 + 13 * (w - 2);
  const int kcnt = (w < 2) ? 14 : 13;
  const bf16x8* gsrc = (const bf16x8*)(p.ws + WS_G) + (size_t)(mt * 4) * 54 * 64 + kstart * 64 + lane;
  const bf16x8* wsrc = (const bf16x8*)(p.ws + WS_W2) + (size_t)(np * 5) * 54 * 64 + kstart * 64 + lane;
  f32x4 acc[5][4];
#pragma unroll
  for (int i = 0; i < 5; ++i)
#pragma unroll
    for (int m = 0; m < 4; ++m) acc[i][m] = (f32x4){0.f, 0.f, 0.f, 0.f};
  bf16x8 gf[E_DEPTH][4], wf[E_DEPTH][5];
#pragma unroll
  for (int s = 0; s < E_DEPTH; ++s) {
#pragma unroll
    for (int m = 0; m < 4; ++m) gf[s][m] = gsrc[(m * 54 + s) * 64];
#pragma unroll
    for (int i = 0; i < 5; ++i) wf[s][i] = wsrc[(i * 54 + s) * 64];
  }
  const float* mod = (const float*)(p.ws + WS_MOD);
  u16* xs = (u16*)(red + 4 * 64 * E_RS);
  u32x4 xa, xb, xc2;
  {
    const u16* x1t = (const u16*)(p.ws + WS_X1T) + ((size_t)mt * D_ + np * 80) * 64;
    xa = *(const u32x4*)(x1t + tid * 8);
    xb = *(const u32x4*)(x1t + (tid + 256) * 8);
    xc2 = *(const u32x4*)(x1t + (tid < 128 ? tid + 512 : 0) * 8);
  }
  SB0;
#pragma unroll
  for (int kk = 0; kk < 14; ++kk) {
    if (kk < kcnt) {
#pragma unroll
      for (int i = 0; i < 5; ++i)
#pragma unroll
        for (int m = 0; m < 4; ++m)
          acc[i][m] = __builtin_amdgcn_mfma_f32_16x16x32_bf16(wf[kk % E_DEPTH][i], gf[kk % E_DEPTH][m], acc[i][m], 0, 0, 0);
    }
    SB0;
    if (kk + E_DEPTH < kcnt) {
#pragma unroll
      for (int m = 0; m < 4; ++m) gf[kk % E_DEPTH][m] = gsrc[(m * 54 + kk + E_DEPTH) * 64];
#pragma unroll
      for (int i = 0; i < 5; ++i) wf[kk % E_DEPTH][i] = wsrc[(i * 54 + kk + E_DEPTH) * 64];
    }
    SB0;
  }
  {
    const int rq = lane >> 4;
#pragma unroll
    for (int i = 0; i < 5; ++i)
#pragma unroll
      for (int m = 0; m < 4; ++m)
        *(f32x4*)(red + (w * 64 + m * 16 + (lane & 15)) * E_RS + i * 16 + 4 * rq) = acc[i][m];
    *(u32x4*)(xs + (tid >> 3) * 72 + (tid & 7) * 8) = xa;
    *(u32x4*)(xs + ((tid + 256) >> 3) * 72 + (tid & 7) * 8) = xb;
    if (tid < 128) *(u32x4*)(xs + ((tid + 512) >> 3) * 72 + (tid & 7) * 8) = xc2;
  }
  __syncthreads();
  for (int item = tid; item < 640; item += 256) {
    const int t = item / 10, cc = item % 10;
    const int dcol = np * 80 + cc * 8;
    const size_t o = (size_t)(mt * 64 + t) * D_ + dcol;
    float xv[8];
#pragma unroll
    for (int e = 0; e < 8; ++e) xv[e] = bf2f(xs[(cc * 8 + e) * 72 + t]);
    const f32x4 g0 = *(const f32x4*)(mod + 5 * 640 + dcol), g1 = *(const f32x4*)(mod + 5 * 640 + dcol + 4);
    float y[8];
#pragma unroll
    for (int e = 0; e < 8; ++e) y[e] = 0.f;
#pragma unroll
    for (int ww = 0; ww < 4; ++ww) {
      f32x4 a = *(const f32x4*)(red + (ww * 64 + t) * E_RS + cc * 8), bb = *(const f32x4*)(red + (ww * 64 + t) * E_RS + cc * 8 + 4);
#pragma unroll
      for (int e = 0; e < 4; ++e) { y[e] += a[e]; y[4 + e] += bb[e]; }
    }
    u32x4 ov;
#pragma unroll
    for (int e = 0; e < 4; ++e) {
      const float ga = (e < 2) ? g0[2 * e] : g1[2 * e - 4], gb = (e < 2) ? g0[2 * e + 1] : g1[2 * e - 3];
      float r0 = xv[2 * e] + rbf(ga * rbf(y[2 * e])), r1 = xv[2 * e + 1] + rbf(gb * rbf(y[2 * e + 1]));
      ov[e] = pack2(r0, r1);
    }
    *(u32x4*)(p.out + o) = ov;
  }
}

extern "C" void kernel_launch(void* const* d_in, const int* in_sizes, int n_in,
                              void* d_out, int out_size, void* d_ws, size_t ws_size,
                              hipStream_t stream) {
  Params p;
  p.u = (const u16*)d_in[0];
  p.Are = (const u16*)d_in[1];
  p.Aim = (const u16*)d_in[2];
  p.Bre = (const u16*)d_in[3];
  p.Bim = (const u16*)d_in[4];
  p.Cre = (const u16*)d_in[5];
  p.Cim = (const u16*)d_in[6];
  p.logdt = (const u16*)d_in[7];
  p.Dskip = (const u16*)d_in[8];
  p.w1 = (const u16*)d_in[10];
  p.w3 = (const u16*)d_in[11];
  p.w2 = (const u16*)d_in[12];
  p.c = (const u16*)d_in[13];
  p.wmod = (const u16*)d_in[14];
  p.out = (u16*)d_out;
  p.ws = (char*)d_ws;
  kA<<<A_TOTAL, 512, 0, stream>>>(p);
  kB<<<748, 256, 0, stream>>>(p);
  kD<<<256, 512, 0, stream>>>(p);
  kE<<<256, 256, 0, stream>>>(p);
}
```

```cpp
#include <hip/hip_runtime.h>
#include <hip/hip_bf16.h>

typedef unsigned short u16;
typedef __attribute__((ext_vector_type(8))) short bf16x8;
typedef __attribute__((ext_vector_type(4))) short s16x4;
typedef __attribute__((ext_vector_type(4))) float f32x4;
typedef __attribute__((ext_vector_type(4))) unsigned u32x4;
typedef __attribute__((ext_vector_type(2))) unsigned u32x2;

#define DEV __device__ __forceinline__

constexpr int L_ = 2048, D_ = 640, N_ = 128, F_ = 1728;
constexpr int KR_LEN = 2208;

constexpr size_t WS_MOD = 0;
constexpr size_t WS_NT = 16384;
constexpr size_t WS_XT = WS_NT + (size_t)D_ * L_ * 2;
constexpr size_t WS_KR = WS_XT + (size_t)D_ * L_ * 2;
constexpr size_t WS_X1T = WS_KR + (size_t)D_ * 2 * KR_LEN * 2;
constexpr size_t WS_H2 = WS_X1T + (size_t)D_ * L_ * 2;
constexpr size_t WS_W13 = WS_H2 + (size_t)D_ * L_ * 2;
constexpr size_t WS_W2 = WS_W13 + (size_t)2 * D_ * F_ * 2;
constexpr size_t WS_G = WS_W2 + (size_t)D_ * F_ * 2;
constexpr size_t WS_END = WS_G + (size_t)L_ * F_ * 2;

DEV float bf2f(u16 x) { return __uint_as_float(((unsigned)x) << 16); }
DEV u16 f2bf(float f) { return __builtin_bit_cast(u16, (__bf16)f); }
DEV float rbf(float f) { return bf2f(f2bf(f)); }
DEV unsigned pack2(float a, float b) { return (unsigned)f2bf(a) | ((unsigned)f2bf(b) << 16); }
DEV float lo16(unsigned u) { return __uint_as_float(u << 16); }
DEV float hi16(unsigned u) { return __uint_as_float(u & 0xffff0000u); }

DEV void cexp(float ar, float ai, float w, float& er, float& ei) {
  const float mag = __builtin_amdgcn_exp2f(ar * w);
  const float rev = __builtin_amdgcn_fractf(ai * w);
  er = mag * __builtin_amdgcn_cosf(rev);
  ei = mag * __builtin_amdgcn_sinf(rev);
}

template <int CTRL>
DEV float dpp_add(float x) {
  return x + __builtin_bit_cast(float, __builtin_amdgcn_update_dpp(0, __builtin_bit_cast(int, x), CTRL, 0xf, 0xf, false));
}
DEV float sum_x16(float x) {
  float a = x, b = x;
  asm volatile("s_nop 1\n\tv_permlane16_swap_b32 %0, %1" : "+v"(a), "+v"(b));
  return a + b;
}
DEV float sum_x32(float x) {
  float a = x, b = x;
  asm volatile("s_nop 1\n\tv_permlane32_swap_b32 %0, %1" : "+v"(a), "+v"(b));
  return a + b;
}
DEV float row_sum16(float x) {
  x = dpp_add<0xB1>(x);
  x = dpp_add<0x4E>(x);
  x = dpp_add<0x124>(x);
  x = dpp_add<0x128>(x);
  return x;
}
DEV float sum_mod4(float x) {
  x = dpp_add<0x124>(x);
  x = dpp_add<0x128>(x);
  x = sum_x16(x);
  x = sum_x32(x);
  return x;
}

struct Params {
  const u16 *u, *Are, *Aim, *Bre, *Bim, *Cre, *Cim, *logdt, *Dskip, *w1, *w3, *w2, *c, *wmod;
  u16* out;
  char* ws;
};

constexpr int A_KGEN = 640, A_LN = 64, A_MOD = 60, A_W2 = 135;
constexpr int A_TOTAL = A_KGEN + A_LN + A_MOD + A_W2;

__global__ __launch_bounds__(512) void kA(Params p) {
  __shared__ __attribute__((aligned(16))) char smem[41984];
  const int tid = threadIdx.x;
  int b = blockIdx.x;
  if (b < A_MOD) {
    float* red = (float*)smem;
    const int cg = tid & 7, rg = tid >> 3;
    float acc[8];
#pragma unroll
    for (int e = 0; e < 8; ++e) acc[e] = 0.f;
    u32x4 wv[10];
    float cv[10];
#pragma unroll
    for (int r = 0; r < 10; ++r) {
      wv[r] = *(const u32x4*)(p.wmod + (size_t)(rg * 10 + r) * 3840 + b * 64 + cg * 8);
      cv[r] = bf2f(p.c[rg * 10 + r]);
    }
#pragma unroll
    for (int r = 0; r < 10; ++r) {
      float s = rbf(cv[r] / (1.f + __expf(-cv[r])));
#pragma unroll
      for (int e = 0; e < 4; ++e) {
        acc[2 * e] += s * lo16(wv[r][e]);
        acc[2 * e + 1] += s * hi16(wv[r][e]);
      }
    }
    *(f32x4*)(red + rg * 64 + cg * 8) = (f32x4){acc[0], acc[1], acc[2], acc[3]};
    *(f32x4*)(red + rg * 64 + cg * 8 + 4) = (f32x4){acc[4], acc[5], acc[6], acc[7]};
    __syncthreads();
    {
      const int col = tid & 63, g = tid >> 6;
      float s = 0.f;
#pragma unroll
      for (int r = 0; r < 8; ++r) s += red[(g * 8 + r) * 64 + col];
      float* red2 = red + 4096;
      red2[g * 64 + col] = s;
      __syncthreads();
      if (tid < 64) {
        float t = 0.f;
#pragma unroll
        for (int r = 0; r < 8; ++r) t += red2[r * 64 + tid];
        ((float*)(p.ws + WS_MOD))[b * 64 + tid] = rbf(t);
      }
    }
    return;
  }
  b -= A_MOD;
  if (b < A_KGEN) {
    const int d = b;
    float* red = (float*)smem;
    float* Kv = (float*)(smem + 24576);
    f32x4* prm = (f32x4*)(smem + 32784);
    const int w = tid >> 6, l = tid & 63, i = l & 15, kq = l >> 4;
    if (tid < 128) {
      const int n = d * N_ + tid;
      const u16 a0 = p.Are[n], a1 = p.Aim[n], b0 = p.Bre[n], b1 = p.Bim[n], c0 = p.Cre[n], c1 = p.Cim[n];
      const float dt = rbf(__expf(bf2f(p.logdt[d])));
      const float dBr = dt * bf2f(b0), dBi = dt * bf2f(b1);
      const float Cr = bf2f(c0), Ci = bf2f(c1);
      prm[tid] = (f32x4){dt * bf2f(a0) * 1.44269504088896341f, dt * bf2f(a1) * 0.15915494309189535f, Cr * dBr - Ci * dBi, Cr * dBi + Ci * dBr};
    }
    const int o0 = (i & 7) + 128 * (i >> 3);
    const int o1 = (i < 8) ? 256 + 2 * (i & 3) + 128 * (i >> 2) : 512 + 4 * (i & 1) + 128 * ((i >> 1) & 3);
    const int o2 = 1024 + 128 * i;
    const float fo0 = (float)o0, fo1 = (float)o1, fo2 = (float)o2, fj = (float)(8 * i);
    __syncthreads();
    f32x4 acc[3];
#pragma unroll
    for (int s = 0; s < 3; ++s) acc[s] = (f32x4){0.f, 0.f, 0.f, 0.f};
#pragma unroll 2
    for (int kk = 0; kk < 4; ++kk) {
      const f32x4 pr = prm[16 * w + 4 * kk + kq];
      const float ar = pr[0], ai = pr[1], CBr = pr[2], CBi = pr[3];
      float yr, yi;
      cexp(ar, ai, fj, yr, yi);
#pragma unroll
      for (int s = 0; s < 3; ++s) {
        float zr, zi;
        cexp(ar, ai, s == 0 ? fo0 : (s == 1 ? fo1 : fo2), zr, zi);
        const float xr = CBr * zr - CBi * zi, xin = -(CBr * zi + CBi * zr);
        acc[s] = __builtin_amdgcn_mfma_f32_16x16x4f32(yr, xr, acc[s], 0, 0, 0);
        acc[s] = __builtin_amdgcn_mfma_f32_16x16x4f32(yi, xin, acc[s], 0, 0, 0);
      }
    }
#pragma unroll
    for (int s = 0; s < 3; ++s) *(f32x4*)(red + (w * 3 + s) * 256 + i * 16 + 4 * kq) = acc[s];
    __syncthreads();
    {
      const int t8 = tid & 255, cI = t8 >> 4, j = t8 & 15, tl = tid >> 8;
      const int p0 = (cI & 7) + 128 * (cI >> 3);
      const int p1 = (cI < 8) ? 256 + 2 * (cI & 3) + 128 * (cI >> 2) : 512 + 4 * (cI & 1) + 128 * ((cI >> 1) & 3);
      float sum = 0.f;
#pragma unroll
      for (int ww = 0; ww < 8; ++ww) sum += red[(ww * 3 + tl) * 256 + t8];
      Kv[(tl ? p1 : p0) + 8 * j] = sum;
      if (tid <= 128) {
        float s2 = 0.f;
#pragma unroll
        for (int ww = 0; ww < 8; ++ww) s2 += red[(ww * 3 + 2) * 256 + tid];
        Kv[1024 + 128 * cI + 8 * j] = s2;
      }
    }
    __syncthreads();
    u16* Kr = (u16*)(p.ws + WS_KR) + (size_t)d * 2 * KR_LEN;
    for (int c = tid; c < KR_LEN / 4; c += 512) {
      unsigned hw[2], lw[2];
#pragma unroll
      for (int e2 = 0; e2 < 2; ++e2) {
        float kv[2];
#pragma unroll
        for (int z = 0; z < 2; ++z) {
          const int k = 2080 - (c * 4 + e2 * 2 + z);
          const int kc = min(max(k, 0), 2047);
          const float v = Kv[(int)rbf((float)kc)];
          kv[z] = (k == kc) ? v : 0.f;
        }
        float h0 = rbf(kv[0]), h1 = rbf(kv[1]);
        hw[e2] = pack2(h0, h1);
        lw[e2] = pack2(kv[0] - h0, kv[1] - h1);
      }
      *(u32x2*)(Kr + c * 4) = (u32x2){hw[0], hw[1]};
      *(u32x2*)(Kr + KR_LEN + c * 4) = (u32x2){lw[0], lw[1]};
    }
    return;
  }
  b -= A_KGEN;
  if (b < A_LN) {
    constexpr int XS = 648;
    u16* xs = (u16*)smem;
    float* sm = (float*)(smem + 32 * XS * 2);
    float* sd = sm + 32;
    const int row = tid >> 4, c = tid & 15;
    {
      u32x4 v[5];
#pragma unroll
      for (int q = 0; q < 5; ++q) v[q] = *(const u32x4*)(p.u + (size_t)(b * 32 + row) * D_ + (c + 16 * q) * 8);
      float s = 0.f;
#pragma unroll
      for (int q = 0; q < 5; ++q)
#pragma unroll
        for (int e = 0; e < 4; ++e) s += lo16(v[q][e]) + hi16(v[q][e]);
      s = row_sum16(s);
      const float mean = s * (1.f / 640.f);
      float s2 = 0.f;
#pragma unroll
      for (int q = 0; q < 5; ++q)
#pragma unroll
        for (int e = 0; e < 4; ++e) {
          float a = lo16(v[q][e]) - mean, bb = hi16(v[q][e]) - mean;
          s2 += a * a + bb * bb;
        }
      s2 = row_sum16(s2);
      if (c == 0) {
        float var = rbf(s2 * (1.f / 640.f));
        sm[row] = rbf(mean);
        sd[row] = __builtin_amdgcn_rcpf(rbf(__builtin_amdgcn_sqrtf(rbf(var + 1e-5f))));
      }
#pragma unroll
      for (int q = 0; q < 5; ++q) *(u32x4*)(xs + row * XS + (c + 16 * q) * 8) = v[q];
    }
    __syncthreads();
    u16* nT = (u16*)(p.ws + WS_NT);
    u16* xT = (u16*)(p.ws + WS_XT);
    const int tq = tid & 3;
    float mm[8], dd[8];
#pragma unroll
    for (int e = 0; e < 8; ++e) { mm[e] = sm[tq * 8 + e]; dd[e] = sd[tq * 8 + e]; }
#pragma unroll
    for (int it = 0; it < 5; ++it) {
      const int d = (tid >> 2) + 128 * it;
      unsigned xo[4], no[4];
#pragma unroll
      for (int e2 = 0; e2 < 4; ++e2) {
        const int r0 = tq * 8 + e2 * 2;
        u16 a = xs[r0 * XS + d], bb = xs[(r0 + 1) * XS + d];
        xo[e2] = (unsigned)a | ((unsigned)bb << 16);
        float n0 = (bf2f(a) - mm[2 * e2]) * dd[2 * e2];
        float n1 = (bf2f(bb) - mm[2 * e2 + 1]) * dd[2 * e2 + 1];
        no[e2] = pack2(n0, n1);
      }
      const size_t o = (size_t)d * L_ + b * 32 + tq * 8;
      *(u32x4*)(xT + o) = (u32x4){xo[0], xo[1], xo[2], xo[3]};
      *(u32x4*)(nT + o) = (u32x4){no[0], no[1], no[2], no[3]};
    }
    return;
  }
  b -= A_LN;
  {
    const int ks0 = (b / 5) * 2, dc = b % 5;
    u16* ts = (u16*)smem;
    {
      const int r = tid >> 3, cq = tid & 7;
      const u16* src = p.w2 + (size_t)(ks0 * 32 + r) * D_ + dc * 128 + cq * 16;
      u32x4 a = *(const u32x4*)src, bb = *(const u32x4*)(src + 8);
      *(u32x4*)(ts + r * 136 + cq * 16) = a;
      *(u32x4*)(ts + r * 136 + cq * 16 + 8) = bb;
    }
    __syncthreads();
    u16* dst = (u16*)(p.ws + WS_W2);
#pragma unroll
    for (int it = 0; it < 2; ++it) {
      const int item = tid + 512 * it;
      const int lane = item & 63, dtl = (item >> 6) & 7, kh = item >> 9;
      const int dd = dtl * 16 + (lane & 15), k0 = kh * 32 + (lane >> 4) * 8;
      unsigned o[4];
#pragma unroll
      for (int e2 = 0; e2 < 4; ++e2)
        o[e2] = (unsigned)ts[(k0 + 2 * e2) * 136 + dd] | ((unsigned)ts[(k0 + 2 * e2 + 1) * 136 + dd] << 16);
      const int dt = dc * 8 + dtl;
      *(u32x4*)(dst + ((size_t)(dt * 54 + ks0 + kh) * 64 + lane) * 8) = (u32x4){o[0], o[1], o[2], o[3]};
    }
  }
}

constexpr int B_HB = 2 * KR_LEN * 2;
constexpr int B_HS = 4640;
constexpr int B_RED = B_HB + B_HS;
constexpr int B_LDS = B_RED + 32768;

template <int UMAX>
DEV void conv_residue(const char* smem, int o0, int lane, f32x4 (&acc)[8]) {
  const int r = lane & 15, kg = lane >> 4;
  const int rho = lane & 7, cb = (lane >> 3) & 1;
  bf16x8 bf[UMAX + 1];
  const int bbase = B_HB + rho * B_HS + 2 * (160 - o0 + 128 * cb + 8 * kg);
#pragma unroll
  for (int u = 0; u <= UMAX; ++u) bf[u] = *(const bf16x8*)(smem + bbase + 512 * u);
  const int abase = 2 * (2080 - o0 - 8 * r + 8 * kg);
#pragma unroll
  for (int i = 0; i < 8; ++i) {
    const bf16x8 ah = *(const bf16x8*)(smem + abase - 512 * i);
    const bf16x8 al = *(const bf16x8*)(smem + abase - 512 * i + KR_LEN * 2);
#pragma unroll
    for (int u = 0; u <= UMAX; ++u) {
      const int q = u + i;
      if (q < 8) {
        acc[q] = __builtin_amdgcn_mfma_f32_16x16x32_bf16(ah, bf[u], acc[q], 0, 0, 0);
        acc[q] = __builtin_amdgcn_mfma_f32_16x16x32_bf16(al, bf[u], acc[q], 0, 0, 0);
      }
    }
  }
}

DEV void h_mod(const u32x4 v, float t1, float sa, unsigned* wv) {
#pragma unroll
  for (int e = 0; e < 4; ++e) {
    float a = lo16(v[e]) * t1 + sa, bb = hi16(v[e]) * t1 + sa;
    wv[e] = pack2(a, bb);
  }
}
DEV void h_store(char* smem, int c, const unsigned* wv) {
#pragma unroll
  for (int rho = 0; rho < 8; ++rho) {
    u32x4 o;
#pragma unroll
    for (int e = 0; e < 4; ++e) {
      const int k = (rho >> 1) + e;
      o[e] = (rho & 1) ? __builtin_amdgcn_alignbit(wv[k + 1], wv[k], 16) : wv[k];
    }
    *(u32x4*)(smem + B_HB + rho * B_HS + c * 16) = o;
  }
}

__global__ __launch_bounds__(256, 3) void kB(Params p) {
  __shared__ __attribute__((aligned(16))) char smem[B_LDS];
  const int tid = threadIdx.x;
  const float* mod = (const float*)(p.ws + WS_MOD);
  if (blockIdx.x < 108) {
    const int fc = blockIdx.x >> 1, kh = blockIdx.x & 1;
    const int mat = tid >> 7, r = (tid >> 1) & 63, hc = tid & 1;
    const u16* src = (mat ? p.w3 : p.w1) + (size_t)(kh * 320 + r) * F_ + fc * 32 + hc * 16;
    u32x4 va[5], vb[5];
    float t1[5], sb[5];
#pragma unroll
    for (int j = 0; j < 5; ++j) {
      va[j] = *(const u32x4*)(src + (size_t)j * 64 * F_);
      vb[j] = *(const u32x4*)(src + (size_t)j * 64 * F_ + 8);
      t1[j] = mod[4 * 640 + kh * 320 + j * 64 + r];
      sb[j] = mod[3 * 640 + kh * 320 + j * 64 + r];
    }
    u16* ts = (u16*)smem;
    float* cs = (float*)(smem + 10240);
    float S[16], Bc[16];
#pragma unroll
    for (int e = 0; e < 16; ++e) { S[e] = 0.f; Bc[e] = 0.f; }
    u16* dst = (u16*)(p.ws + WS_W13);
#pragma unroll
    for (int j = 0; j < 5; ++j) {
      const float tt = rbf(1.f + t1[j]);
      u32x4 oa, ob;
#pragma unroll
      for (int e = 0; e < 4; ++e) {
        const float a0 = lo16(va[j][e]), a1 = hi16(va[j][e]), b0 = lo16(vb[j][e]), b1 = hi16(vb[j][e]);
        oa[e] = pack2(a0 * tt, a1 * tt);
        ob[e] = pack2(b0 * tt, b1 * tt);
        S[2 * e] += lo16(oa[e]); S[2 * e + 1] += hi16(oa[e]); S[8 + 2 * e] += lo16(ob[e]); S[8 + 2 * e + 1] += hi16(ob[e]);
        Bc[2 * e] += sb[j] * a0; Bc[2 * e + 1] += sb[j] * a1; Bc[8 + 2 * e] += sb[j] * b0; Bc[8 + 2 * e + 1] += sb[j] * b1;
      }
      if (j) __syncthreads();
      *(u32x4*)(ts + (mat * 64 + r) * 40 + hc * 16) = oa;
      *(u32x4*)(ts + (mat * 64 + r) * 40 + hc * 16 + 8) = ob;
      __syncthreads();
#pragma unroll
      for (int it = 0; it < 2; ++it) {
        const int item = tid + 256 * it;
        const int lane = item & 63, m2 = (item >> 6) & 1, ftl = (item >> 7) & 1, ksl = item >> 8;
        const u16* tb = ts + (m2 * 64 + ksl * 32 + (lane >> 4) * 8) * 40 + ftl * 16 + (lane & 15);
        unsigned o[4];
#pragma unroll
        for (int e2 = 0; e2 < 4; ++e2) o[e2] = (unsigned)tb[(2 * e2) * 40] | ((unsigned)tb[(2 * e2 + 1) * 40] << 16);
        const int ft = fc * 2 + ftl, ks = kh * 10 + j * 2 + ksl;
        *(u32x4*)(dst + ((size_t)((ft * 20 + ks) * 2 + m2) * 64 + lane) * 8) = (u32x4){o[0], o[1], o[2], o[3]};
      }
    }
#pragma unroll
    for (int e = 0; e < 4; ++e) {
      *(f32x4*)(cs + ((0 * 2 + mat) * 64 + r) * 32 + hc * 16 + 4 * e) = (f32x4){S[4 * e], S[4 * e + 1], S[4 * e + 2], S[4 * e + 3]};
      *(f32x4*)(cs + ((1 * 2 + mat) * 64 + r) * 32 + hc * 16 + 4 * e) = (f32x4){Bc[4 * e], Bc[4 * e + 1], Bc[4 * e + 2], Bc[4 * e + 3]};
    }
    __syncthreads();
    if (tid < 128) {
      const int col = tid & 31, sm2 = tid >> 5;
      float a = 0.f;
#pragma unroll 8
      for (int rr = 0; rr < 64; ++rr) a += cs[(sm2 * 64 + rr) * 32 + col];
      ((float*)(p.ws + WS_H2))[(kh * 4 + sm2) * F_ + fc * 32 + col] = a;
    }
    return;
  }
  const int d = blockIdx.x - 108;
  const u16* nT = (const u16*)(p.ws + WS_NT) + (size_t)d * L_;
  const u32x4* ksrc = (const u32x4*)((const u16*)(p.ws + WS_KR) + (size_t)d * 2 * KR_LEN);
  const u32x4 n0 = *(const u32x4*)(nT + tid * 8);
  const u32x4 n1 = *(const u32x4*)(nT + (tid < 255 ? tid * 8 + 8 : 0));
  const u32x4 k0 = ksrc[tid], k1 = ksrc[tid + 256];
  const u32x4 k2 = ksrc[tid < 40 ? tid + 512 : 0];
  const u32x4 xv = *(const u32x4*)((const u16*)(p.ws + WS_XT) + (size_t)d * L_ + tid * 8);
  const float sa = mod[d], cav = mod[640 + d], ga = mod[1280 + d];
  const float Dk = bf2f(p.Dskip[d]);
  const float t1 = rbf(1.f + cav);
  *(u32x4*)(smem + tid * 16) = k0;
  *(u32x4*)(smem + (tid + 256) * 16) = k1;
  if (tid < 40) *(u32x4*)(smem + (tid + 512) * 16) = k2;
  {
    unsigned wv[8];
    h_mod(n0, t1, sa, wv);
    h_mod(n1, t1, sa, wv + 4);
    if (tid == 255) { wv[4] = 0u; wv[5] = 0u; wv[6] = 0u; wv[7] = 0u; }
    h_store(smem, tid + 20, wv);
    unsigned zv[8];
#pragma unroll
    for (int e = 0; e < 4; ++e) {
      const unsigned f0 = __builtin_amdgcn_readfirstlane(wv[e]);
      zv[e] = 0u;
      zv[4 + e] = (tid == 19) ? f0 : 0u;
    }
    if (tid < 20) h_store(smem, tid, zv);
  }
  __syncthreads();
  const int w = __builtin_amdgcn_readfirstlane(tid >> 6), lane = tid & 63;
  f32x4 acc[8];
#pragma unroll
  for (int q = 0; q < 8; ++q) acc[q] = (f32x4){0.f, 0.f, 0.f, 0.f};
  conv_residue<7>(smem, -96 + 32 * w, lane, acc);
  conv_residue<7>(smem, -96 + 32 * (w + 4), lane, acc);
  if (w == 3) conv_residue<0>(smem, 160, lane, acc);
  __syncthreads();
  {
    const int rho = lane & 7, cb = (lane >> 3) & 1, rq = lane >> 4;
    const int sw = (rq & 1) | (cb << 1);
    float* red = (float*)(smem + B_RED) + w * 2048 + 128 * cb + 32 * rq + rho;
#pragma unroll
    for (int g = 0; g < 4; ++g) {
      float* rg = red + 8 * (g ^ sw);
#pragma unroll
      for (int q = 0; q < 8; ++q) rg[256 * q] = acc[q][g];
    }
  }
  __syncthreads();
  {
    const int t0 = tid * 8;
    const int pos = t0 ^ (((t0 >> 5) & 1) << 3) ^ (((t0 >> 7) & 1) << 4);
    const float* red = (const float*)(smem + B_RED);
    float y[8];
#pragma unroll
    for (int e = 0; e < 8; ++e) y[e] = 0.f;
#pragma unroll
    for (int ww = 0; ww < 4; ++ww) {
      f32x4 a = *(const f32x4*)(red + ww * 2048 + pos), bb = *(const f32x4*)(red + ww * 2048 + pos + 4);
#pragma unroll
      for (int e = 0; e < 4; ++e) { y[e] += a[e]; y[4 + e] += bb[e]; }
    }
    const u32x4 hv = *(const u32x4*)(smem + B_HB + 2 * (160 + t0));
    u32x4 o;
#pragma unroll
    for (int e = 0; e < 4; ++e) {
      float h0 = lo16(hv[e]), h1 = hi16(hv[e]);
      float y0 = rbf(rbf(y[2 * e]) + rbf(h0 * Dk)), y1 = rbf(rbf(y[2 * e + 1]) + rbf(h1 * Dk));
      float x0 = lo16(xv[e]) + rbf(ga * y0), x1 = hi16(xv[e]) + rbf(ga * y1);
      o[e] = pack2(x0, x1);
    }
    *(u32x4*)((u16*)(p.ws + WS_X1T) + ((size_t)(t0 >> 6) * D_ + d) * 64 + (t0 & 63)) = o;
  }
}

DEV int d_off(int k, int c) {
  const int f = ((k >> 1) & 1) | (((k >> 3) & 1) << 1);
  return k * 128 + ((((c >> 1) ^ f)) << 5) + ((c & 1) << 4);
}
DEV bf16x8 d_frag(const char* smem, int lane, int mi, int ks) {
  const int g = lane >> 4, q = (lane & 15) >> 2, pp = lane & 3;
  const int k0 = 32 * ks + 8 * g + q;
  const int c = 2 * mi + (pp >> 1);
  typedef __attribute__((address_space(3))) s16x4* lp;
  s16x4 a = __builtin_amdgcn_ds_read_tr16_b64_v4i16((lp)(smem + d_off(k0, c) + 8 * (pp & 1)));
  s16x4 bq = __builtin_amdgcn_ds_read_tr16_b64_v4i16((lp)(smem + d_off(k0 + 4, c) + 8 * (pp & 1)));
  return __builtin_shufflevector(a, bq, 0, 1, 2, 3, 4, 5, 6, 7);
}

DEV void d_load(const Params& p, int lane, int ft, int half, bf16x8 (&wf)[10][2]) {
  const bf16x8* wsrc = (const bf16x8*)(p.ws + WS_W13) + (size_t)(ft * 20 + half * 10) * 2 * 64 + lane;
#pragma unroll
  for (int ks = 0; ks < 10; ++ks) {
    wf[ks][0] = wsrc[(ks * 2) * 64];
    wf[ks][1] = wsrc[(ks * 2 + 1) * 64];
  }
}

DEV const bf16x8* d_wptr(const Params& p, int lane, int ft, int half) {
  return (const bf16x8*)(p.ws + WS_W13) + (size_t)(ft * 20 + half * 10) * 2 * 64 + lane;
}
template <int MT, bool RELOAD>
DEV void d_half(const char* smem, int lane, int mi0, int half, const bf16x8 (&wf)[10][2], f32x4 (&acc)[2][4],
                bf16x8 (&wn)[10][2], const bf16x8* nsrc) {
#pragma unroll
  for (int ks = 0; ks < 10; ++ks) {
    if (RELOAD) {
      __builtin_amdgcn_sched_barrier(0);
      wn[ks][0] = nsrc[(ks * 2) * 64];
      wn[ks][1] = nsrc[(ks * 2 + 1) * 64];
      __builtin_amdgcn_sched_barrier(0);
    }
#pragma unroll
    for (int m = 0; m < MT; ++m) {
      const bf16x8 hf = d_frag(smem, lane, mi0 + m, half * 10 + ks);
      acc[0][m] = __builtin_amdgcn_mfma_f32_16x16x32_bf16(wf[ks][0], hf, acc[0][m], 0, 0, 0);
      acc[1][m] = __builtin_amdgcn_mfma_f32_16x16x32_bf16(wf[ks][1], hf, acc[1][m], 0, 0, 0);
    }
  }
}

struct ColSums { f32x4 S[2], Bc[2]; };
DEV void d_cs(const Params& p, int lane, int ft, ColSums& c) {
  const float* cs = (const float*)(p.ws + WS_H2) + ft * 16 + 4 * (lane >> 4);
#pragma unroll
  for (int mat = 0; mat < 2; ++mat) {
    c.S[mat] = *(const f32x4*)(cs + (0 + mat) * F_) + *(const f32x4*)(cs + (4 + mat) * F_);
    c.Bc[mat] = *(const f32x4*)(cs + (2 + mat) * F_) + *(const f32x4*)(cs + (6 + mat) * F_);
  }
}
template <int MT>
DEV void d_epi(const Params& p, const float* st, const ColSums& c, int lane, int ft, int mi0, int mt, f32x4 (&acc)[2][4]) {
  const int rq = lane >> 4;
  u16* gdst = (u16*)(p.ws + WS_G);
#pragma unroll
  for (int m = 0; m < MT; ++m) {
    const float rs = st[64 + (mi0 + m) * 16 + (lane & 15)], mr = st[(mi0 + m) * 16 + (lane & 15)] * rs;
    float gv[4];
#pragma unroll
    for (int g = 0; g < 4; ++g) {
      const float gate = acc[0][m][g] * rs + (c.Bc[0][g] - mr * c.S[0][g]);
      const float up = acc[1][m][g] * rs + (c.Bc[1][g] - mr * c.S[1][g]);
      gv[g] = gate * up * __builtin_amdgcn_rcpf(1.f + __builtin_amdgcn_exp2f(gate * -1.44269504088896341f));
    }
    const int mtile = mt * 4 + mi0 + m;
    const int lane2 = (lane & 15) + 16 * (2 * (ft & 1) + (rq >> 1));
    u32x2 o = {pack2(gv[0], gv[1]), pack2(gv[2], gv[3])};
    *(u32x2*)(gdst + ((size_t)(mtile * 54 + (ft >> 1)) * 64 + lane2) * 8 + 4 * (rq & 1)) = o;
  }
#pragma unroll
  for (int m = 0; m < 4; ++m) { acc[0][m] = (f32x4){0.f, 0.f, 0.f, 0.f}; acc[1][m] = (f32x4){0.f, 0.f, 0.f, 0.f}; }
}

#define SB0 __builtin_amdgcn_sched_barrier(0)
#define RAWBAR() do { asm volatile("s_waitcnt lgkmcnt(0)" ::: "memory"); __builtin_amdgcn_s_barrier(); asm volatile("" ::: "memory"); } while (0)

DEV float sum_mod8(float x) {
  x = dpp_add<0x128>(x);
  x = sum_x16(x);
  x = sum_x32(x);
  return x;
}
typedef __attribute__((ext_vector_type(2))) float f32x2;
DEV f32x2 unpk(unsigned u) { return (f32x2){lo16(u), hi16(u)}; }

__global__ __launch_bounds__(512) void kD(Params p) {
  __shared__ __attribute__((aligned(16))) char smem[81920 + 4096 + 4096];
  const int tid = threadIdx.x;
  const int xc = blockIdx.x & 7, jj = blockIdx.x >> 3;
#define D_MAP 0
  const int mt = (xc & 1) * 16 + (jj & 15), fpart = (xc >> 1) * 2 + (jj >> 4);
  const int w = __builtin_amdgcn_readfirstlane(tid >> 6), lane = tid & 63;
  float* ps = (float*)(smem + 81920);
  const float* mod = (const float*)(p.ws + WS_MOD);
  const int c = tid & 7, d0 = tid >> 3;
  const int fs = (27 * fpart) >> 1, fe = (27 * (fpart + 1)) >> 1;
  bf16x8 wA[10][2], wB[10][2];
  char* vdst = smem + d_off(d0, c);
  float* st = (float*)(smem + 81920 + 4096) + w * 128;
  u32x4 v[10], pv;
  {
    const u16* src = (const u16*)(p.ws + WS_X1T) + (size_t)mt * D_ * 64 + d0 * 64 + c * 8;
#pragma unroll
    for (int it = 0; it < 10; ++it) v[it] = *(const u32x4*)(src + it * 4096);
    pv = *(const u32x4*)(src - d0 * 64);
  }
  SB0;
#pragma unroll
  for (int it = 0; it < 10; ++it) *(u32x4*)(vdst + 8192 * it) = v[it];
  SB0;
  d_load(p, lane, fs + w, 0, wA);
  SB0;
  {
    f32x2 piv[4], s1[4], s2[4];
#pragma unroll
    for (int e = 0; e < 4; ++e) { piv[e] = unpk(pv[e]); s1[e] = (f32x2){0.f, 0.f}; s2[e] = (f32x2){0.f, 0.f}; }
#pragma unroll
    for (int it = 0; it < 10; ++it)
#pragma unroll
      for (int e = 0; e < 4; ++e) { const f32x2 a = unpk(v[it][e]) - piv[e]; s1[e] += a; s2[e] += a * a; }
    float s[8], q[8];
#pragma unroll
    for (int e = 0; e < 8; ++e) { s[e] = sum_mod8(s1[e >> 1][e & 1]); q[e] = sum_mod8(s2[e >> 1][e & 1]); }
    if (lane < 8) {
      *(f32x4*)(ps + w * 64 + lane * 8) = (f32x4){s[0], s[1], s[2], s[3]};
      *(f32x4*)(ps + w * 64 + lane * 8 + 4) = (f32x4){s[4], s[5], s[6], s[7]};
      *(f32x4*)(ps + 512 + w * 64 + lane * 8) = (f32x4){q[0], q[1], q[2], q[3]};
      *(f32x4*)(ps + 512 + w * 64 + lane * 8 + 4) = (f32x4){q[4], q[5], q[6], q[7]};
    }
  }
  SB0;
  RAWBAR();
  {
    float a = 0.f, a2 = 0.f;
#pragma unroll
    for (int ww = 0; ww < 8; ++ww) { a += ps[ww * 64 + lane]; a2 += ps[512 + ww * 64 + lane]; }
    const float m1 = a * (1.f / 640.f), m2 = a2 * (1.f / 640.f);
    const float var = rbf(fmaxf(m2 - m1 * m1, 0.f));
    const float pvt = bf2f(*(const u16*)(smem + d_off(0, lane >> 3) + (lane & 7) * 2));
    st[lane] = rbf(pvt + m1);
    st[64 + lane] = __builtin_amdgcn_rcpf(rbf(__builtin_amdgcn_sqrtf(rbf(var + 1e-5f))));
  }
  const bool big = (fe - fs == 14);
  f32x4 acc[2][4];
#pragma unroll
  for (int m = 0; m < 4; ++m) { acc[0][m] = (f32x4){0.f, 0.f, 0.f, 0.f}; acc[1][m] = (f32x4){0.f, 0.f, 0.f, 0.f}; }
  const int ft2 = (w < 4) ? fs + 8 + w : (big ? fs + 12 + ((w & 3) >> 1) : fs + 12);
  ColSums cs1, cs2;
  d_cs(p, lane, fs + w, cs1);
  d_cs(p, lane, ft2, cs2);
  SB0;
  d_half<4, true>(smem, lane, 0, 0, wA, acc, wB, d_wptr(p, lane, fs + w, 1));
  d_half<4, true>(smem, lane, 0, 1, wB, acc, wA, d_wptr(p, lane, ft2, 0));
  d_epi<4>(p, st, cs1, lane, fs + w, 0, mt, acc);
  SB0;
  int ln = lane;
  asm volatile("" : "+v"(ln));
  if (w < 4) {
    d_half<4, true>(smem, ln, 0, 0, wA, acc, wB, d_wptr(p, ln, ft2, 1));
    d_half<4, false>(smem, ln, 0, 1, wB, acc, wA, nullptr);
    d_epi<4>(p, st, cs2, ln, ft2, 0, mt, acc);
  } else if (big) {
    const int mi0 = 2 * (w & 1);
    d_half<2, true>(smem, ln, mi0, 0, wA, acc, wB, d_wptr(p, ln, ft2, 1));
    d_half<2, false>(smem, ln, mi0, 1, wB, acc, wA, nullptr);
    d_epi<2>(p, st, cs2, ln, ft2, mi0, mt, acc);
  } else {
    const int mi0 = w & 3;
    d_half<1, true>(smem, ln, mi0, 0, wA, acc, wB, d_wptr(p, ln, ft2, 1));
    d_half<1, false>(smem, ln, mi0, 1, wB, acc, wA, nullptr);
    d_epi<1>(p, st, cs2, ln, ft2, mi0, mt, acc);
  }
}

constexpr int E_RS = 84;
#define E_DEPTH_ 5
constexpr int E_DEPTH = E_DEPTH_;
__global__ __launch_bounds__(256) void kE(Params p) {
  __shared__ __attribute__((aligned(16))) float red[4 * 64 * E_RS + 80 * 36];
  const int tid = threadIdx.x, xc = blockIdx.x & 7, j = blockIdx.x >> 3;
#define E_MAP 0
  const int mt = (xc >> 1) * 8 + (j & 7), np = (xc & 1) * 4 + (j >> 3);
  const int w = __builtin_amdgcn_readfirstlane(tid >> 6), lane = tid & 63;
  const int kstart = (w < 2) ? 14 * w : 28 + 13 * (w - 2);
  const int kcnt = (w < 2) ? 14 : 13;
  const bf16x8* gsrc = (const bf16x8*)(p.ws + WS_G) + (size_t)(mt * 4) * 54 * 64 + kstart * 64 + lane;
  const bf16x8* wsrc = (const bf16x8*)(p.ws + WS_W2) + (size_t)(np * 5) * 54 * 64 + kstart * 64 + lane;
  f32x4 acc[5][4];
#pragma unroll
  for (int i = 0; i < 5; ++i)
#pragma unroll
    for (int m = 0; m < 4; ++m) acc[i][m] = (f32x4){0.f, 0.f, 0.f, 0.f};
  bf16x8 gf[E_DEPTH][4], wf[E_DEPTH][5];
#pragma unroll
  for (int s = 0; s < E_DEPTH; ++s) {
#pragma unroll
    for (int m = 0; m < 4; ++m) gf[s][m] = gsrc[(m * 54 + s) * 64];
#pragma unroll
    for (int i = 0; i < 5; ++i) wf[s][i] = wsrc[(i * 54 + s) * 64];
  }
  const float* mod = (const float*)(p.ws + WS_MOD);
  u16* xs = (u16*)(red + 4 * 64 * E_RS);
  u32x4 xa, xb, xc2;
  {
    const u16* x1t = (const u16*)(p.ws + WS_X1T) + ((size_t)mt * D_ + np * 80) * 64;
    xa = *(const u32x4*)(x1t + tid * 8);
    xb = *(const u32x4*)(x1t + (tid + 256) * 8);
    xc2 = *(const u32x4*)(x1t + (tid < 128 ? tid + 512 : 0) * 8);
  }
  SB0;
#pragma unroll
  for (int kk = 0; kk < 14; ++kk) {
    if (kk < kcnt) {
#pragma unroll
      for (int i = 0; i < 5; ++i)
#pragma unroll
        for (int m = 0; m < 4; ++m)
          acc[i][m] = __builtin_amdgcn_mfma_f32_16x16x32_bf16(wf[kk % E_DEPTH][i], gf[kk % E_DEPTH][m], acc[i][m], 0, 0, 0);
    }
    SB0;
    if (kk + E_DEPTH < kcnt) {
#pragma unroll
      for (int m = 0; m < 4; ++m) gf[kk % E_DEPTH][m] = gsrc[(m * 54 + kk + E_DEPTH) * 64];
#pragma unroll
      for (int i = 0; i < 5; ++i) wf[kk % E_DEPTH][i] = wsrc[(i * 54 + kk + E_DEPTH) * 64];
    }
    SB0;
  }
  {
    const int rq = lane >> 4;
#pragma unroll
    for (int i = 0; i < 5; ++i)
#pragma unroll
      for (int m = 0; m < 4; ++m)
        *(f32x4*)(red + (w * 64 + m * 16 + (lane & 15)) * E_RS + i * 16 + 4 * rq) = acc[i][m];
    *(u32x4*)(xs + (tid >> 3) * 72 + (tid & 7) * 8) = xa;
    *(u32x4*)(xs + ((tid + 256) >> 3) * 72 + (tid & 7) * 8) = xb;
    if (tid < 128) *(u32x4*)(xs + ((tid + 512) >> 3) * 72 + (tid & 7) * 8) = xc2;
  }
  __syncthreads();
  for (int item = tid; item < 640; item += 256) {
    const int t = item / 10, cc = item % 10;
    const int dcol = np * 80 + cc * 8;
    const size_t o = (size_t)(mt * 64 + t) * D_ + dcol;
    float xv[8];
#pragma unroll
    for (int e = 0; e < 8; ++e) xv[e] = bf2f(xs[(cc * 8 + e) * 72 + t]);
    const f32x4 g0 = *(const f32x4*)(mod + 5 * 640 + dcol), g1 = *(const f32x4*)(mod + 5 * 640 + dcol + 4);
    float y[8];
#pragma unroll
    for (int e = 0; e < 8; ++e) y[e] = 0.f;
#pragma unroll
    for (int ww = 0; ww < 4; ++ww) {
      f32x4 a = *(const f32x4*)(red + (ww * 64 + t) * E_RS + cc * 8), bb = *(const f32x4*)(red + (ww * 64 + t) * E_RS + cc * 8 + 4);
#pragma unroll
      for (int e = 0; e < 4; ++e) { y[e] += a[e]; y[4 + e] += bb[e]; }
    }
    u32x4 ov;
#pragma unroll
    for (int e = 0; e < 4; ++e) {
      const float ga = (e < 2) ? g0[2 * e] : g1[2 * e - 4], gb = (e < 2) ? g0[2 * e + 1] : g1[2 * e - 3];
      float r0 = xv[2 * e] + rbf(ga * rbf(y[2 * e])), r1 = xv[2 * e + 1] + rbf(gb * rbf(y[2 * e + 1]));
      ov[e] = pack2(r0, r1);
    }
    *(u32x4*)(p.out + o) = ov;
  }
}

extern "C" void kernel_launch(void* const* d_in, const int* in_sizes, int n_in,
                              void* d_out, int out_size, void* d_ws, size_t ws_size,
                              hipStream_t stream) {
  Params p;
  p.u = (const u16*)d_in[0];
  p.Are = (const u16*)d_in[1];
  p.Aim = (const u16*)d_in[2];
  p.Bre = (const u16*)d_in[3];
  p.Bim = (const u16*)d_in[4];
  p.Cre = (const u16*)d_in[5];
  p.Cim = (const u16*)d_in[6];
  p.logdt = (const u16*)d_in[7];
  p.Dskip = (const u16*)d_in[8];
  p.w1 = (const u16*)d_in[10];
  p.w3 = (const u16*)d_in[11];
  p.w2 = (const u16*)d_in[12];
  p.c = (const u16*)d_in[13];
  p.wmod = (const u16*)d_in[14];
  p.out = (u16*)d_out;
  p.ws = (char*)d_ws;
  kA<<<A_TOTAL, 512, 0, stream>>>(p);
  kB<<<748, 256, 0, stream>>>(p);
  kD<<<256, 512, 0, stream>>>(p);
  kE<<<256, 256, 0, stream>>>(p);
}
```

```cpp
#include <hip/hip_runtime.h>
#include <hip/hip_bf16.h>

typedef unsigned short u16;
typedef __attribute__((ext_vector_type(8))) short bf16x8;
typedef __attribute__((ext_vector_type(4))) short s16x4;
typedef __attribute__((ext_vector_type(4))) float f32x4;
typedef __attribute__((ext_vector_type(4))) unsigned u32x4;
typedef __attribute__((ext_vector_type(2))) unsigned u32x2;

#define DEV __device__ __forceinline__

constexpr int L_ = 2048, D_ = 640, N_ = 128, F_ = 1728;
constexpr int KR_LEN = 2208;

constexpr size_t WS_MOD = 0;
constexpr size_t WS_NT = 16384;
constexpr size_t WS_XT = WS_NT + (size_t)D_ * L_ * 2;
constexpr size_t WS_KR = WS_XT + (size_t)D_ * L_ * 2;
constexpr size_t WS_X1T = WS_KR + (size_t)D_ * 2 * KR_LEN * 2;
constexpr size_t WS_H2 = WS_X1T + (size_t)D_ * L_ * 2;
constexpr size_t WS_W13 = WS_H2 + (size_t)D_ * L_ * 2;
constexpr size_t WS_W2 = WS_W13 + (size_t)2 * D_ * F_ * 2;
constexpr size_t WS_G = WS_W2 + (size_t)D_ * F_ * 2;
constexpr size_t WS_END = WS_G + (size_t)L_ * F_ * 2;

DEV float bf2f(u16 x) { return __uint_as_float(((unsigned)x) << 16); }
DEV u16 f2bf(float f) { return __builtin_bit_cast(u16, (__bf16)f); }
DEV float rbf(float f) { return bf2f(f2bf(f)); }
DEV unsigned pack2(float a, float b) { return (unsigned)f2bf(a) | ((unsigned)f2bf(b) << 16); }
DEV float lo16(unsigned u) { return __uint_as_float(u << 16); }
DEV float hi16(unsigned u) { return __uint_as_float(u & 0xffff0000u); }

DEV void cexp(float ar, float ai, float w, float& er, float& ei) {
  const float mag = __builtin_amdgcn_exp2f(ar * w);
  const float rev = __builtin_amdgcn_fractf(ai * w);
  er = mag * __builtin_amdgcn_cosf(rev);
  ei = mag * __builtin_amdgcn_sinf(rev);
}

template <int CTRL>
DEV float dpp_add(float x) {
  return x + __builtin_bit_cast(float, __builtin_amdgcn_update_dpp(0, __builtin_bit_cast(int, x), CTRL, 0xf, 0xf, false));
}
DEV float sum_x16(float x) {
  float a = x, b = x;
  asm volatile("s_nop 1\n\tv_permlane16_swap_b32 %0, %1" : "+v"(a), "+v"(b));
  return a + b;
}
DEV float sum_x32(float x) {
  float a = x, b = x;
  asm volatile("s_nop 1\n\tv_permlane32_swap_b32 %0, %1" : "+v"(a), "+v"(b));
  return a + b;
}
DEV float row_sum16(float x) {
  x = dpp_add<0xB1>(x);
  x = dpp_add<0x4E>(x);
  x = dpp_add<0x124>(x);
  x = dpp_add<0x128>(x);
  return x;
}
DEV float sum_mod4(float x) {
  x = dpp_add<0x124>(x);
  x = dpp_add<0x128>(x);
  x = sum_x16(x);
  x = sum_x32(x);
  return x;
}

struct Params {
  const u16 *u, *Are, *Aim, *Bre, *Bim, *Cre, *Cim, *logdt, *Dskip, *w1, *w3, *w2, *c, *wmod;
  u16* out;
  char* ws;
};

constexpr int A_KGEN = 640, A_LN = 64, A_MOD = 60, A_W2 = 135;
constexpr int A_TOTAL = A_KGEN + A_LN + A_MOD + A_W2;

__global__ __launch_bounds__(512) void kA(Params p) {
  __shared__ __attribute__((aligned(16))) char smem[41984];
  const int tid = threadIdx.x;
  int b = blockIdx.x;
  if (b < A_MOD) {
    float* red = (float*)smem;
    const int cg = tid & 7, rg = tid >> 3;
    float acc[8];
#pragma unroll
    for (int e = 0; e < 8; ++e) acc[e] = 0.f;
    u32x4 wv[10];
    float cv[10];
#pragma unroll
    for (int r = 0; r < 10; ++r) {
      wv[r] = *(const u32x4*)(p.wmod + (size_t)(rg * 10 + r) * 3840 + b * 64 + cg * 8);
      cv[r] = bf2f(p.c[rg * 10 + r]);
    }
#pragma unroll
    for (int r = 0; r < 10; ++r) {
      float s = rbf(cv[r] / (1.f + __expf(-cv[r])));
#pragma unroll
      for (int e = 0; e < 4; ++e) {
        acc[2 * e] += s * lo16(wv[r][e]);
        acc[2 * e + 1] += s * hi16(wv[r][e]);
      }
    }
    *(f32x4*)(red + rg * 64 + cg * 8) = (f32x4){acc[0], acc[1], acc[2], acc[3]};
    *(f32x4*)(red + rg * 64 + cg * 8 + 4) = (f32x4){acc[4], acc[5], acc[6], acc[7]};
    __syncthreads();
    {
      const int col = tid & 63, g = tid >> 6;
      float s = 0.f;
#pragma unroll
      for (int r = 0; r < 8; ++r) s += red[(g * 8 + r) * 64 + col];
      float* red2 = red + 4096;
      red2[g * 64 + col] = s;
      __syncthreads();
      if (tid < 64) {
        float t = 0.f;
#pragma unroll
        for (int r = 0; r < 8; ++r) t += red2[r * 64 + tid];
        ((float*)(p.ws + WS_MOD))[b * 64 + tid] = rbf(t);
      }
    }
    return;
  }
  b -= A_MOD;
  if (b < A_KGEN) {
    const int d = b;
    float* red = (float*)smem;
    f32x4* prm = (f32x4*)(smem + 24576);
    const int w = tid >> 6, l = tid & 63, i = l & 15, kq = l >> 4;
    if (tid < 128) {
      const int n = d * N_ + tid;
      const u16 a0 = p.Are[n], a1 = p.Aim[n], b0 = p.Bre[n], b1 = p.Bim[n], c0 = p.Cre[n], c1 = p.Cim[n];
      const float dt = rbf(__expf(bf2f(p.logdt[d])));
      const float dBr = dt * bf2f(b0), dBi = dt * bf2f(b1);
      const float Cr = bf2f(c0), Ci = bf2f(c1);
      prm[tid] = (f32x4){dt * bf2f(a0) * 1.44269504088896341f, dt * bf2f(a1) * 0.15915494309189535f, Cr * dBr - Ci * dBi, Cr * dBi + Ci * dBr};
    }
    const int o0 = (i & 7) + 128 * (i >> 3);
    const int o1 = (i < 8) ? 256 + 2 * (i & 3) + 128 * (i >> 2) : 512 + 4 * (i & 1) + 128 * ((i >> 1) & 3);
    const int o2 = 1024 + 128 * i;
    const float fo0 = (float)o0, fo1 = (float)o1, fo2 = (float)o2, fj = (float)(8 * i);
    __syncthreads();
    f32x4 acc[3];
#pragma unroll
    for (int s = 0; s < 3; ++s) acc[s] = (f32x4){0.f, 0.f, 0.f, 0.f};
#pragma unroll 2
    for (int kk = 0; kk < 4; ++kk) {
      const f32x4 pr = prm[16 * w + 4 * kk + kq];
      const float ar = pr[0], ai = pr[1], CBr = pr[2], CBi = pr[3];
      float yr, yi;
      cexp(ar, ai, fj, yr, yi);
#pragma unroll
      for (int s = 0; s < 3; ++s) {
        float zr, zi;
        cexp(ar, ai, s == 0 ? fo0 : (s == 1 ? fo1 : fo2), zr, zi);
        const float xr = CBr * zr - CBi * zi, xin = -(CBr * zi + CBi * zr);
        acc[s] = __builtin_amdgcn_mfma_f32_16x16x4f32(yr, xr, acc[s], 0, 0, 0);
        acc[s] = __builtin_amdgcn_mfma_f32_16x16x4f32(yi, xin, acc[s], 0, 0, 0);
      }
    }
#pragma unroll
    for (int s = 0; s < 3; ++s) *(f32x4*)(red + (w * 3 + s) * 256 + i * 16 + 4 * kq) = acc[s];
    __syncthreads();
    {
      const int t8 = tid & 255, cI = t8 >> 4, j = t8 & 15, tl = tid >> 8;
      const int i0 = (cI & 7) + 128 * (cI >> 3) + 8 * j;
      const int i1 = (cI < 8) ? 256 + (cI & 3) + 64 * (cI >> 2) + 4 * j
                              : 384 + (cI & 1) + 32 * ((cI >> 1) & 3) + 2 * j;
      float* Kc = (float*)(p.ws + WS_KR) + (size_t)d * 644;
      float sum = 0.f;
#pragma unroll
      for (int ww = 0; ww < 8; ++ww) sum += red[(ww * 3 + tl) * 256 + t8];
      Kc[tl ? i1 : i0] = sum;
      if (tid <= 128) {
        float s2 = 0.f;
#pragma unroll
        for (int ww = 0; ww < 8; ++ww) s2 += red[(ww * 3 + 2) * 256 + tid];
        Kc[512 + 16 * cI + j] = s2;
      }
    }
    return;
  }
  b -= A_KGEN;
  if (b < A_LN) {
    constexpr int XS = 648;
    u16* xs = (u16*)smem;
    float* sm = (float*)(smem + 32 * XS * 2);
    float* sd = sm + 32;
    const int row = tid >> 4, c = tid & 15;
    {
      u32x4 v[5];
#pragma unroll
      for (int q = 0; q < 5; ++q) v[q] = *(const u32x4*)(p.u + (size_t)(b * 32 + row) * D_ + (c + 16 * q) * 8);
      float s = 0.f;
#pragma unroll
      for (int q = 0; q < 5; ++q)
#pragma unroll
        for (int e = 0; e < 4; ++e) s += lo16(v[q][e]) + hi16(v[q][e]);
      s = row_sum16(s);
      const float mean = s * (1.f / 640.f);
      float s2 = 0.f;
#pragma unroll
      for (int q = 0; q < 5; ++q)
#pragma unroll
        for (int e = 0; e < 4; ++e) {
          float a = lo16(v[q][e]) - mean, bb = hi16(v[q][e]) - mean;
          s2 += a * a + bb * bb;
        }
      s2 = row_sum16(s2);
      if (c == 0) {
        float var = rbf(s2 * (1.f / 640.f));
        sm[row] = rbf(mean);
        sd[row] = __builtin_amdgcn_rcpf(rbf(__builtin_amdgcn_sqrtf(rbf(var + 1e-5f))));
      }
#pragma unroll
      for (int q = 0; q < 5; ++q) *(u32x4*)(xs + row * XS + (c + 16 * q) * 8) = v[q];
    }
    __syncthreads();
    u16* nT = (u16*)(p.ws + WS_NT);
    u16* xT = (u16*)(p.ws + WS_XT);
    const int tq = tid & 3;
    float mm[8], dd[8];
#pragma unroll
    for (int e = 0; e < 8; ++e) { mm[e] = sm[tq * 8 + e]; dd[e] = sd[tq * 8 + e]; }
#pragma unroll
    for (int it = 0; it < 5; ++it) {
      const int d = (tid >> 2) + 128 * it;
      unsigned xo[4], no[4];
#pragma unroll
      for (int e2 = 0; e2 < 4; ++e2) {
        const int r0 = tq * 8 + e2 * 2;
        u16 a = xs[r0 * XS + d], bb = xs[(r0 + 1) * XS + d];
        xo[e2] = (unsigned)a | ((unsigned)bb << 16);
        float n0 = (bf2f(a) - mm[2 * e2]) * dd[2 * e2];
        float n1 = (bf2f(bb) - mm[2 * e2 + 1]) * dd[2 * e2 + 1];
        no[e2] = pack2(n0, n1);
      }
      const size_t o = (size_t)d * L_ + b * 32 + tq * 8;
      *(u32x4*)(xT + o) = (u32x4){xo[0], xo[1], xo[2], xo[3]};
      *(u32x4*)(nT + o) = (u32x4){no[0], no[1], no[2], no[3]};
    }
    return;
  }
  b -= A_LN;
  {
    const int ks0 = (b / 5) * 2, dc = b % 5;
    u16* ts = (u16*)smem;
    {
      const int r = tid >> 3, cq = tid & 7;
      const u16* src = p.w2 + (size_t)(ks0 * 32 + r) * D_ + dc * 128 + cq * 16;
      u32x4 a = *(const u32x4*)src, bb = *(const u32x4*)(src + 8);
      *(u32x4*)(ts + r * 136 + cq * 16) = a;
      *(u32x4*)(ts + r * 136 + cq * 16 + 8) = bb;
    }
    __syncthreads();
    u16* dst = (u16*)(p.ws + WS_W2);
#pragma unroll
    for (int it = 0; it < 2; ++it) {
      const int item = tid + 512 * it;
      const int lane = item & 63, dtl = (item >> 6) & 7, kh = item >> 9;
      const int dd = dtl * 16 + (lane & 15), k0 = kh * 32 + (lane >> 4) * 8;
      unsigned o[4];
#pragma unroll
      for (int e2 = 0; e2 < 4; ++e2)
        o[e2] = (unsigned)ts[(k0 + 2 * e2) * 136 + dd] | ((unsigned)ts[(k0 + 2 * e2 + 1) * 136 + dd] << 16);
      const int dt = dc * 8 + dtl;
      *(u32x4*)(dst + ((size_t)(dt * 54 + ks0 + kh) * 64 + lane) * 8) = (u32x4){o[0], o[1], o[2], o[3]};
    }
  }
}

constexpr int B_HB = 2 * KR_LEN * 2;
constexpr int B_HS = 4640;
constexpr int B_RED = B_HB + B_HS;
constexpr int B_LDS = B_RED + 32768;

template <int UMAX>
DEV void conv_residue(const char* smem, int o0, int lane, f32x4 (&acc)[8]) {
  const int r = lane & 15, kg = lane >> 4;
  const int rho = lane & 7, cb = (lane >> 3) & 1;
  bf16x8 bf[UMAX + 1];
  const int bbase = B_HB + rho * B_HS + 2 * (160 - o0 + 128 * cb + 8 * kg);
#pragma unroll
  for (int u = 0; u <= UMAX; ++u) bf[u] = *(const bf16x8*)(smem + bbase + 512 * u);
  const int abase = 2 * (2080 - o0 - 8 * r + 8 * kg);
#pragma unroll
  for (int i = 0; i < 8; ++i) {
    const bf16x8 ah = *(const bf16x8*)(smem + abase - 512 * i);
    const bf16x8 al = *(const bf16x8*)(smem + abase - 512 * i + KR_LEN * 2);
#pragma unroll
    for (int u = 0; u <= UMAX; ++u) {
      const int q = u + i;
      if (q < 8) {
        acc[q] = __builtin_amdgcn_mfma_f32_16x16x32_bf16(ah, bf[u], acc[q], 0, 0, 0);
        acc[q] = __builtin_amdgcn_mfma_f32_16x16x32_bf16(al, bf[u], acc[q], 0, 0, 0);
      }
    }
  }
}

DEV void h_mod(const u32x4 v, float t1, float sa, unsigned* wv) {
#pragma unroll
  for (int e = 0; e < 4; ++e) {
    float a = lo16(v[e]) * t1 + sa, bb = hi16(v[e]) * t1 + sa;
    wv[e] = pack2(a, bb);
  }
}
DEV void h_store(char* smem, int c, const unsigned* wv) {
#pragma unroll
  for (int rho = 0; rho < 8; ++rho) {
    u32x4 o;
#pragma unroll
    for (int e = 0; e < 4; ++e) {
      const int k = (rho >> 1) + e;
      o[e] = (rho & 1) ? __builtin_amdgcn_alignbit(wv[k + 1], wv[k], 16) : wv[k];
    }
    *(u32x4*)(smem + B_HB + rho * B_HS + c * 16) = o;
  }
}

__global__ __launch_bounds__(256, 3) void kB(Params p) {
  __shared__ __attribute__((aligned(16))) char smem[B_LDS + 2576];
  const int tid = threadIdx.x;
  const float* mod = (const float*)(p.ws + WS_MOD);
  if (blockIdx.x < 108) {
    const int fc = blockIdx.x >> 1, kh = blockIdx.x & 1;
    const int mat = tid >> 7, r = (tid >> 1) & 63, hc = tid & 1;
    const u16* src = (mat ? p.w3 : p.w1) + (size_t)(kh * 320 + r) * F_ + fc * 32 + hc * 16;
    u32x4 va[5], vb[5];
    float t1[5], sb[5];
#pragma unroll
    for (int j = 0; j < 5; ++j) {
      va[j] = *(const u32x4*)(src + (size_t)j * 64 * F_);
      vb[j] = *(const u32x4*)(src + (size_t)j * 64 * F_ + 8);
      t1[j] = mod[4 * 640 + kh * 320 + j * 64 + r];
      sb[j] = mod[3 * 640 + kh * 320 + j * 64 + r];
    }
    u16* ts = (u16*)smem;
    float* cs = (float*)(smem + 10240);
    float S[16], Bc[16];
#pragma unroll
    for (int e = 0; e < 16; ++e) { S[e] = 0.f; Bc[e] = 0.f; }
    u16* dst = (u16*)(p.ws + WS_W13);
#pragma unroll
    for (int j = 0; j < 5; ++j) {
      const float tt = rbf(1.f + t1[j]);
      u32x4 oa, ob;
#pragma unroll
      for (int e = 0; e < 4; ++e) {
        const float a0 = lo16(va[j][e]), a1 = hi16(va[j][e]), b0 = lo16(vb[j][e]), b1 = hi16(vb[j][e]);
        oa[e] = pack2(a0 * tt, a1 * tt);
        ob[e] = pack2(b0 * tt, b1 * tt);
        S[2 * e] += lo16(oa[e]); S[2 * e + 1] += hi16(oa[e]); S[8 + 2 * e] += lo16(ob[e]); S[8 + 2 * e + 1] += hi16(ob[e]);
        Bc[2 * e] += sb[j] * a0; Bc[2 * e + 1] += sb[j] * a1; Bc[8 + 2 * e] += sb[j] * b0; Bc[8 + 2 * e + 1] += sb[j] * b1;
      }
      if (j) __syncthreads();
      *(u32x4*)(ts + (mat * 64 + r) * 40 + hc * 16) = oa;
      *(u32x4*)(ts + (mat * 64 + r) * 40 + hc * 16 + 8) = ob;
      __syncthreads();
#pragma unroll
      for (int it = 0; it < 2; ++it) {
        const int item = tid + 256 * it;
        const int lane = item & 63, m2 = (item >> 6) & 1, ftl = (item >> 7) & 1, ksl = item >> 8;
        const u16* tb = ts + (m2 * 64 + ksl * 32 + (lane >> 4) * 8) * 40 + ftl * 16 + (lane & 15);
        unsigned o[4];
#pragma unroll
        for (int e2 = 0; e2 < 4; ++e2) o[e2] = (unsigned)tb[(2 * e2) * 40] | ((unsigned)tb[(2 * e2 + 1) * 40] << 16);
        const int ft = fc * 2 + ftl, ks = kh * 10 + j * 2 + ksl;
        *(u32x4*)(dst + ((size_t)((ft * 20 + ks) * 2 + m2) * 64 + lane) * 8) = (u32x4){o[0], o[1], o[2], o[3]};
      }
    }
#pragma unroll
    for (int e = 0; e < 4; ++e) {
      *(f32x4*)(cs + ((0 * 2 + mat) * 64 + r) * 32 + hc * 16 + 4 * e) = (f32x4){S[4 * e], S[4 * e + 1], S[4 * e + 2], S[4 * e + 3]};
      *(f32x4*)(cs + ((1 * 2 + mat) * 64 + r) * 32 + hc * 16 + 4 * e) = (f32x4){Bc[4 * e], Bc[4 * e + 1], Bc[4 * e + 2], Bc[4 * e + 3]};
    }
    __syncthreads();
    if (tid < 128) {
      const int col = tid & 31, sm2 = tid >> 5;
      float a = 0.f;
#pragma unroll 8
      for (int rr = 0; rr < 64; ++rr) a += cs[(sm2 * 64 + rr) * 32 + col];
      ((float*)(p.ws + WS_H2))[(kh * 4 + sm2) * F_ + fc * 32 + col] = a;
    }
    return;
  }
  const int d = blockIdx.x - 108;
  const u16* nT = (const u16*)(p.ws + WS_NT) + (size_t)d * L_;
  const f32x4* ksrc = (const f32x4*)((const float*)(p.ws + WS_KR) + (size_t)d * 644);
  const u32x4 n0 = *(const u32x4*)(nT + tid * 8);
  const u32x4 n1 = *(const u32x4*)(nT + (tid < 255 ? tid * 8 + 8 : 0));
  const f32x4 k0 = ksrc[tid < 161 ? tid : 0];
  const u32x4 xv = *(const u32x4*)((const u16*)(p.ws + WS_XT) + (size_t)d * L_ + tid * 8);
  const float sa = mod[d], cav = mod[640 + d], ga = mod[1280 + d];
  const float Dk = bf2f(p.Dskip[d]);
  const float t1 = rbf(1.f + cav);
  float* Kc = (float*)(smem + B_LDS);
  if (tid < 161) *(f32x4*)(Kc + tid * 4) = k0;
  {
    unsigned wv[8];
    h_mod(n0, t1, sa, wv);
    h_mod(n1, t1, sa, wv + 4);
    if (tid == 255) { wv[4] = 0u; wv[5] = 0u; wv[6] = 0u; wv[7] = 0u; }
    h_store(smem, tid + 20, wv);
    unsigned zv[8];
#pragma unroll
    for (int e = 0; e < 4; ++e) {
      const unsigned f0 = __builtin_amdgcn_readfirstlane(wv[e]);
      zv[e] = 0u;
      zv[4 + e] = (tid == 19) ? f0 : 0u;
    }
    if (tid < 20) h_store(smem, tid, zv);
  }
  __syncthreads();
  for (int cc = tid; cc < KR_LEN / 8; cc += 256) {
    unsigned hw[4], lw[4];
#pragma unroll
    for (int e2 = 0; e2 < 4; ++e2) {
      float kv[2];
#pragma unroll
      for (int z = 0; z < 2; ++z) {
        const int k = 2080 - (cc * 8 + e2 * 2 + z);
        const int kc = min(max(k, 0), 2047);
        const int v = (int)rbf((float)kc);
        const int idx = v < 256 ? v : (v < 512 ? 128 + (v >> 1) : (v < 1024 ? 256 + (v >> 2) : 384 + (v >> 3)));
        const float val = Kc[idx];
        kv[z] = (k == kc) ? val : 0.f;
      }
      const float h0 = rbf(kv[0]), h1 = rbf(kv[1]);
      hw[e2] = pack2(h0, h1);
      lw[e2] = pack2(kv[0] - h0, kv[1] - h1);
    }
    *(u32x4*)(smem + cc * 16) = (u32x4){hw[0], hw[1], hw[2], hw[3]};
    *(u32x4*)(smem + KR_LEN * 2 + cc * 16) = (u32x4){lw[0], lw[1], lw[2], lw[3]};
  }
  __syncthreads();
  const int w = __builtin_amdgcn_readfirstlane(tid >> 6), lane = tid & 63;
  f32x4 acc[8];
#pragma unroll
  for (int q = 0; q < 8; ++q) acc[q] = (f32x4){0.f, 0.f, 0.f, 0.f};
  conv_residue<7>(smem, -96 + 32 * w, lane, acc);
  conv_residue<7>(smem, -96 + 32 * (w + 4), lane, acc);
  if (w == 3) conv_residue<0>(smem, 160, lane, acc);
  __syncthreads();
  {
    const int rho = lane & 7, cb = (lane >> 3) & 1, rq = lane >> 4;
    const int sw = (rq & 1) | (cb << 1);
    float* red = (float*)(smem + B_RED) + w * 2048 + 128 * cb + 32 * rq + rho;
#pragma unroll
    for (int g = 0; g < 4; ++g) {
      float* rg = red + 8 * (g ^ sw);
#pragma unroll
      for (int q = 0; q < 8; ++q) rg[256 * q] = acc[q][g];
    }
  }
  __syncthreads();
  {
    const int t0 = tid * 8;
    const int pos = t0 ^ (((t0 >> 5) & 1) << 3) ^ (((t0 >> 7) & 1) << 4);
    const float* red = (const float*)(smem + B_RED);
    float y[8];
#pragma unroll
    for (int e = 0; e < 8; ++e) y[e] = 0.f;
#pragma unroll
    for (int ww = 0; ww < 4; ++ww) {
      f32x4 a = *(const f32x4*)(red + ww * 2048 + pos), bb = *(const f32x4*)(red + ww * 2048 + pos + 4);
#pragma unroll
      for (int e = 0; e < 4; ++e) { y[e] += a[e]; y[4 + e] += bb[e]; }
    }
    const u32x4 hv = *(const u32x4*)(smem + B_HB + 2 * (160 + t0));
    u32x4 o;
#pragma unroll
    for (int e = 0; e < 4; ++e) {
      float h0 = lo16(hv[e]), h1 = hi16(hv[e]);
      float y0 = rbf(rbf(y[2 * e]) + rbf(h0 * Dk)), y1 = rbf(rbf(y[2 * e + 1]) + rbf(h1 * Dk));
      float x0 = lo16(xv[e]) + rbf(ga * y0), x1 = hi16(xv[e]) + rbf(ga * y1);
      o[e] = pack2(x0, x1);
    }
    *(u32x4*)((u16*)(p.ws + WS_X1T) + ((size_t)(t0 >> 6) * D_ + d) * 64 + (t0 & 63)) = o;
  }
}

DEV int d_off(int k, int c) {
  const int f = ((k >> 1) & 1) | (((k >> 3) & 1) << 1);
  return k * 128 + ((((c >> 1) ^ f)) << 5) + ((c & 1) << 4);
}
DEV bf16x8 d_frag(const char* smem, int lane, int mi, int ks) {
  const int g = lane >> 4, q = (lane & 15) >> 2, pp = lane & 3;
  const int k0 = 32 * ks + 8 * g + q;
  const int c = 2 * mi + (pp >> 1);
  typedef __attribute__((address_space(3))) s16x4* lp;
  s16x4 a = __builtin_amdgcn_ds_read_tr16_b64_v4i16((lp)(smem + d_off(k0, c) + 8 * (pp & 1)));
  s16x4 bq = __builtin_amdgcn_ds_read_tr16_b64_v4i16((lp)(smem + d_off(k0 + 4, c) + 8 * (pp & 1)));
  return __builtin_shufflevector(a, bq, 0, 1, 2, 3, 4, 5, 6, 7);
}

DEV void d_load(const Params& p, int lane, int ft, int half, bf16x8 (&wf)[10][2]) {
  const bf16x8* wsrc = (const bf16x8*)(p.ws + WS_W13) + (size_t)(ft * 20 + half * 10) * 2 * 64 + lane;
#pragma unroll
  for (int ks = 0; ks < 10; ++ks) {
    wf[ks][0] = wsrc[(ks * 2) * 64];
    wf[ks][1] = wsrc[(ks * 2 + 1) * 64];
  }
}

DEV const bf16x8* d_wptr(const Params& p, int lane, int ft, int half) {
  return (const bf16x8*)(p.ws + WS_W13) + (size_t)(ft * 20 + half * 10) * 2 * 64 + lane;
}
template <int MT, bool RELOAD>
DEV void d_half(const char* smem, int lane, int mi0, int half, const bf16x8 (&wf)[10][2], f32x4 (&acc)[2][4],
                bf16x8 (&wn)[10][2], const bf16x8* nsrc) {
#pragma unroll
  for (int ks = 0; ks < 10; ++ks) {
    if (RELOAD) {
      __builtin_amdgcn_sched_barrier(0);
      wn[ks][0] = nsrc[(ks * 2) * 64];
      wn[ks][1] = nsrc[(ks * 2 + 1) * 64];
      __builtin_amdgcn_sched_barrier(0);
    }
#pragma unroll
    for (int m = 0; m < MT; ++m) {
      const bf16x8 hf = d_frag(smem, lane, mi0 + m, half * 10 + ks);
      acc[0][m] = __builtin_amdgcn_mfma_f32_16x16x32_bf16(wf[ks][0], hf, acc[0][m], 0, 0, 0);
      acc[1][m] = __builtin_amdgcn_mfma_f32_16x16x32_bf16(wf[ks][1], hf, acc[1][m], 0, 0, 0);
    }
  }
}

struct ColSums { f32x4 S[2], Bc[2]; };
DEV void d_cs(const Params& p, int lane, int ft, ColSums& c) {
  const float* cs = (const float*)(p.ws + WS_H2) + ft * 16 + 4 * (lane >> 4);
#pragma unroll
  for (int mat = 0; mat < 2; ++mat) {
    c.S[mat] = *(const f32x4*)(cs + (0 + mat) * F_) + *(const f32x4*)(cs + (4 + mat) * F_);
    c.Bc[mat] = *(const f32x4*)(cs + (2 + mat) * F_) + *(const f32x4*)(cs + (6 + mat) * F_);
  }
}
template <int MT>
DEV void d_epi(const Params& p, const float* st, const ColSums& c, int lane, int ft, int mi0, int mt, f32x4 (&acc)[2][4]) {
  const int rq = lane >> 4;
  u16* gdst = (u16*)(p.ws + WS_G);
#pragma unroll
  for (int m = 0; m < MT; ++m) {
    const float rs = st[64 + (mi0 + m) * 16 + (lane & 15)], mr = st[(mi0 + m) * 16 + (lane & 15)] * rs;
    float gv[4];
#pragma unroll
    for (int g = 0; g < 4; ++g) {
      const float gate = acc[0][m][g] * rs + (c.Bc[0][g] - mr * c.S[0][g]);
      const float up = acc[1][m][g] * rs + (c.Bc[1][g] - mr * c.S[1][g]);
      gv[g] = gate * up * __builtin_amdgcn_rcpf(1.f + __builtin_amdgcn_exp2f(gate * -1.44269504088896341f));
    }
    const int mtile = mt * 4 + mi0 + m;
    const int lane2 = (lane & 15) + 16 * (2 * (ft & 1) + (rq >> 1));
    u32x2 o = {pack2(gv[0], gv[1]), pack2(gv[2], gv[3])};
    *(u32x2*)(gdst + ((size_t)(mtile * 54 + (ft >> 1)) * 64 + lane2) * 8 + 4 * (rq & 1)) = o;
  }
#pragma unroll
  for (int m = 0; m < 4; ++m) { acc[0][m] = (f32x4){0.f, 0.f, 0.f, 0.f}; acc[1][m] = (f32x4){0.f, 0.f, 0.f, 0.f}; }
}

#define SB0 __builtin_amdgcn_sched_barrier(0)
#define RAWBAR() do { asm volatile("s_waitcnt lgkmcnt(0)" ::: "memory"); __builtin_amdgcn_s_barrier(); asm volatile("" ::: "memory"); } while (0)

DEV float sum_mod8(float x) {
  x = dpp_add<0x128>(x);
  x = sum_x16(x);
  x = sum_x32(x);
  return x;
}
typedef __attribute__((ext_vector_type(2))) float f32x2;
DEV f32x2 unpk(unsigned u) { return (f32x2){lo16(u), hi16(u)}; }

__global__ __launch_bounds__(512) void kD(Params p) {
  __shared__ __attribute__((aligned(16))) char smem[81920 + 4096 + 4096];
  const int tid = threadIdx.x;
  const int xc = blockIdx.x & 7, jj = blockIdx.x >> 3;
  const int mt = (xc & 1) * 16 + (jj & 15), fpart = (xc >> 1) * 2 + (jj >> 4);
  const int w = __builtin_amdgcn_readfirstlane(tid >> 6), lane = tid & 63;
  float* ps = (float*)(smem + 81920);
  const float* mod = (const float*)(p.ws + WS_MOD);
  const int c = tid & 7, d0 = tid >> 3;
  const int fs = (27 * fpart) >> 1, fe = (27 * (fpart + 1)) >> 1;
  bf16x8 wA[10][2], wB[10][2];
  char* vdst = smem + d_off(d0, c);
  float* st = (float*)(smem + 81920 + 4096) + w * 128;
  u32x4 v[10], pv;
  {
    const u16* src = (const u16*)(p.ws + WS_X1T) + (size_t)mt * D_ * 64 + d0 * 64 + c * 8;
#pragma unroll
    for (int it = 0; it < 10; ++it) v[it] = *(const u32x4*)(src + it * 4096);
    pv = *(const u32x4*)(src - d0 * 64);
  }
  SB0;
#pragma unroll
  for (int it = 0; it < 10; ++it) *(u32x4*)(vdst + 8192 * it) = v[it];
  SB0;
  d_load(p, lane, fs + w, 0, wA);
  SB0;
  {
    f32x2 piv[4], s1[4], s2[4];
#pragma unroll
    for (int e = 0; e < 4; ++e) { piv[e] = unpk(pv[e]); s1[e] = (f32x2){0.f, 0.f}; s2[e] = (f32x2){0.f, 0.f}; }
#pragma unroll
    for (int it = 0; it < 10; ++it)
#pragma unroll
      for (int e = 0; e < 4; ++e) { const f32x2 a = unpk(v[it][e]) - piv[e]; s1[e] += a; s2[e] += a * a; }
    float s[8], q[8];
#pragma unroll
    for (int e = 0; e < 8; ++e) { s[e] = sum_mod8(s1[e >> 1][e & 1]); q[e] = sum_mod8(s2[e >> 1][e & 1]); }
    if (lane < 8) {
      *(f32x4*)(ps + w * 64 + lane * 8) = (f32x4){s[0], s[1], s[2], s[3]};
      *(f32x4*)(ps + w * 64 + lane * 8 + 4) = (f32x4){s[4], s[5], s[6], s[7]};
      *(f32x4*)(ps + 512 + w * 64 + lane * 8) = (f32x4){q[0], q[1], q[2], q[3]};
      *(f32x4*)(ps + 512 + w * 64 + lane * 8 + 4) = (f32x4){q[4], q[5], q[6], q[7]};
    }
  }
  SB0;
  RAWBAR();
  {
    float a = 0.f, a2 = 0.f;
#pragma unroll
    for (int ww = 0; ww < 8; ++ww) { a += ps[ww * 64 + lane]; a2 += ps[512 + ww * 64 + lane]; }
    const float m1 = a * (1.f / 640.f), m2 = a2 * (1.f / 640.f);
    const float var = rbf(fmaxf(m2 - m1 * m1, 0.f));
    const float pvt = bf2f(*(const u16*)(smem + d_off(0, lane >> 3) + (lane & 7) * 2));
    st[lane] = rbf(pvt + m1);
    st[64 + lane] = __builtin_amdgcn_rcpf(rbf(__builtin_amdgcn_sqrtf(rbf(var + 1e-5f))));
  }
  const bool big = (fe - fs == 14);
  f32x4 acc[2][4];
#pragma unroll
  for (int m = 0; m < 4; ++m) { acc[0][m] = (f32x4){0.f, 0.f, 0.f, 0.f}; acc[1][m] = (f32x4){0.f, 0.f, 0.f, 0.f}; }
  const int ft2 = (w < 4) ? fs + 8 + w : (big ? fs + 12 + ((w & 3) >> 1) : fs + 12);
  ColSums cs1, cs2;
  d_cs(p, lane, fs + w, cs1);
  d_cs(p, lane, ft2, cs2);
  SB0;
  d_half<4, true>(smem, lane, 0, 0, wA, acc, wB, d_wptr(p, lane, fs + w, 1));
  d_half<4, true>(smem, lane, 0, 1, wB, acc, wA, d_wptr(p, lane, ft2, 0));
  d_epi<4>(p, st, cs1, lane, fs + w, 0, mt, acc);
  SB0;
  int ln = lane;
  asm volatile("" : "+v"(ln));
  if (w < 4) {
    d_half<4, true>(smem, ln, 0, 0, wA, acc, wB, d_wptr(p, ln, ft2, 1));
    d_half<4, false>(smem, ln, 0, 1, wB, acc, wA, nullptr);
    d_epi<4>(p, st, cs2, ln, ft2, 0, mt, acc);
  } else if (big) {
    const int mi0 = 2 * (w & 1);
    d_half<2, true>(smem, ln, mi0, 0, wA, acc, wB, d_wptr(p, ln, ft2, 1));
    d_half<2, false>(smem, ln, mi0, 1, wB, acc, wA, nullptr);
    d_epi<2>(p, st, cs2, ln, ft2, mi0, mt, acc);
  } else {
    const int mi0 = w & 3;
    d_half<1, true>(smem, ln, mi0, 0, wA, acc, wB, d_wptr(p, ln, ft2, 1));
    d_half<1, false>(smem, ln, mi0, 1, wB, acc, wA, nullptr);
    d_epi<1>(p, st, cs2, ln, ft2, mi0, mt, acc);
  }
}

constexpr int E_RS = 84;
constexpr int E_DEPTH = 5;
__global__ __launch_bounds__(256) void kE(Params p) {
  __shared__ __attribute__((aligned(16))) float red[4 * 64 * E_RS + 80 * 36];
  const int tid = threadIdx.x, xc = blockIdx.x & 7, j = blockIdx.x >> 3;
  const int mt = (xc >> 1) * 8 + (j & 7), np = (xc & 1) * 4 + (j >> 3);
  const int w = __builtin_amdgcn_readfirstlane(tid >> 6), lane = tid & 63;
  const int kstart = (w < 2) ? 14 * w : 28 + 13 * (w - 2);
  const int kcnt = (w < 2) ? 14 : 13;
  const bf16x8* gsrc = (const bf16x8*)(p.ws + WS_G) + (size_t)(mt * 4) * 54 * 64 + kstart * 64 + lane;
  const bf16x8* wsrc = (const bf16x8*)(p.ws + WS_W2) + (size_t)(np * 5) * 54 * 64 + kstart * 64 + lane;
  f32x4 acc[5][4];
#pragma unroll
  for (int i = 0; i < 5; ++i)
#pragma unroll
    for (int m = 0; m < 4; ++m) acc[i][m] = (f32x4){0.f, 0.f, 0.f, 0.f};
  bf16x8 gf[E_DEPTH][4], wf[E_DEPTH][5];
#pragma unroll
  for (int s = 0; s < E_DEPTH; ++s) {
#pragma unroll
    for (int m = 0; m < 4; ++m) gf[s][m] = gsrc[(m * 54 + s) * 64];
#pragma unroll
    for (int i = 0; i < 5; ++i) wf[s][i] = wsrc[(i * 54 + s) * 64];
  }
  const float* mod = (const float*)(p.ws + WS_MOD);
  u16* xs = (u16*)(red + 4 * 64 * E_RS);
  u32x4 xa, xb, xc2;
  {
    const u16* x1t = (const u16*)(p.ws + WS_X1T) + ((size_t)mt * D_ + np * 80) * 64;
    xa = *(const u32x4*)(x1t + tid * 8);
    xb = *(const u32x4*)(x1t + (tid + 256) * 8);
    xc2 = *(const u32x4*)(x1t + (tid < 128 ? tid + 512 : 0) * 8);
  }
  SB0;
#pragma unroll
  for (int kk = 0; kk < 14; ++kk) {
    if (kk < kcnt) {
#pragma unroll
      for (int i = 0; i < 5; ++i)
#pragma unroll
        for (int m = 0; m < 4; ++m)
          acc[i][m] = __builtin_amdgcn_mfma_f32_16x16x32_bf16(wf[kk % E_DEPTH][i], gf[kk % E_DEPTH][m], acc[i][m], 0, 0, 0);
    }
    SB0;
    if (kk + E_DEPTH < kcnt) {
#pragma unroll
      for (int m = 0; m < 4; ++m) gf[kk % E_DEPTH][m] = gsrc[(m * 54 + kk + E_DEPTH) * 64];
#pragma unroll
      for (int i = 0; i < 5; ++i) wf[kk % E_DEPTH][i] = wsrc[(i * 54 + kk + E_DEPTH) * 64];
    }
    SB0;
  }
  {
    const int rq = lane >> 4;
#pragma unroll
    for (int i = 0; i < 5; ++i)
#pragma unroll
      for (int m = 0; m < 4; ++m)
        *(f32x4*)(red + (w * 64 + m * 16 + (lane & 15)) * E_RS + i * 16 + 4 * rq) = acc[i][m];
    *(u32x4*)(xs + (tid >> 3) * 72 + (tid & 7) * 8) = xa;
    *(u32x4*)(xs + ((tid + 256) >> 3) * 72 + (tid & 7) * 8) = xb;
    if (tid < 128) *(u32x4*)(xs + ((tid + 512) >> 3) * 72 + (tid & 7) * 8) = xc2;
  }
  __syncthreads();
  for (int item = tid; item < 640; item += 256) {
    const int t = item / 10, cc = item % 10;
    const int dcol = np * 80 + cc * 8;
    const size_t o = (size_t)(mt * 64 + t) * D_ + dcol;
    float xv[8];
#pragma unroll
    for (int e = 0; e < 8; ++e) xv[e] = bf2f(xs[(cc * 8 + e) * 72 + t]);
    const f32x4 g0 = *(const f32x4*)(mod + 5 * 640 + dcol), g1 = *(const f32x4*)(mod + 5 * 640 + dcol + 4);
    float y[8];
#pragma unroll
    for (int e = 0; e < 8; ++e) y[e] = 0.f;
#pragma unroll
    for (int ww = 0; ww < 4; ++ww) {
      f32x4 a = *(const f32x4*)(red + (ww * 64 + t) * E_RS + cc * 8), bb = *(const f32x4*)(red + (ww * 64 + t) * E_RS + cc * 8 + 4);
#pragma unroll
      for (int e = 0; e < 4; ++e) { y[e] += a[e]; y[4 + e] += bb[e]; }
    }
    u32x4 ov;
#pragma unroll
    for (int e = 0; e < 4; ++e) {
      const float ga = (e < 2) ? g0[2 * e] : g1[2 * e - 4], gb = (e < 2) ? g0[2 * e + 1] : g1[2 * e - 3];
      float r0 = xv[2 * e] + rbf(ga * rbf(y[2 * e])), r1 = xv[2 * e + 1] + rbf(gb * rbf(y[2 * e + 1]));
      ov[e] = pack2(r0, r1);
    }
    *(u32x4*)(p.out + o) = ov;
  }
}

extern "C" void kernel_launch(void* const* d_in, const int* in_sizes, int n_in,
                              void* d_out, int out_size, void* d_ws, size_t ws_size,
                              hipStream_t stream) {
  Params p;
  p.u = (const u16*)d_in[0];
  p.Are = (const u16*)d_in[1];
  p.Aim = (const u16*)d_in[2];
  p.Bre = (const u16*)d_in[3];
  p.Bim = (const u16*)d_in[4];
  p.Cre = (const u16*)d_in[5];
  p.Cim = (const u16*)d_in[6];
  p.logdt = (const u16*)d_in[7];
  p.Dskip = (const u16*)d_in[8];
  p.w1 = (const u16*)d_in[10];
  p.w3 = (const u16*)d_in[11];
  p.w2 = (const u16*)d_in[12];
  p.c = (const u16*)d_in[13];
  p.wmod = (const u16*)d_in[14];
  p.out = (u16*)d_out;
  p.ws = (char*)d_ws;
  kA<<<A_TOTAL, 512, 0, stream>>>(p);
  kB<<<748, 256, 0, stream>>>(p);
  kD<<<256, 512, 0, stream>>>(p);
  kE<<<256, 256, 0, stream>>>(p);
}
```

```cpp
#include <hip/hip_runtime.h>
#include <hip/hip_bf16.h>

typedef unsigned short u16;
typedef __attribute__((ext_vector_type(8))) short bf16x8;
typedef __attribute__((ext_vector_type(4))) short s16x4;
typedef __attribute__((ext_vector_type(4))) float f32x4;
typedef __attribute__((ext_vector_type(4))) unsigned u32x4;
typedef __attribute__((ext_vector_type(2))) unsigned u32x2;

#define DEV __device__ __forceinline__

constexpr int L_ = 2048, D_ = 640, N_ = 128, F_ = 1728;
constexpr int KR_LEN = 2208;

constexpr size_t WS_MOD = 0;
constexpr size_t WS_NT = 16384;
constexpr size_t WS_XT = WS_NT + (size_t)D_ * L_ * 2;
constexpr size_t WS_KR = WS_XT + (size_t)D_ * L_ * 2;
constexpr size_t WS_X1T = WS_KR + (size_t)D_ * 644 * 4;
constexpr size_t WS_H2 = WS_X1T + (size_t)D_ * L_ * 2;
constexpr size_t WS_W13 = WS_H2 + (size_t)8 * F_ * 4;
constexpr size_t WS_W2 = WS_W13 + (size_t)2 * D_ * F_ * 2;
constexpr size_t WS_G = WS_W2 + (size_t)D_ * F_ * 2;
constexpr size_t WS_END = WS_G + (size_t)L_ * F_ * 2;

DEV float bf2f(u16 x) { return __uint_as_float(((unsigned)x) << 16); }
DEV u16 f2bf(float f) { return __builtin_bit_cast(u16, (__bf16)f); }
DEV float rbf(float f) { return bf2f(f2bf(f)); }
DEV unsigned pack2(float a, float b) { return (unsigned)f2bf(a) | ((unsigned)f2bf(b) << 16); }
DEV float lo16(unsigned u) { return __uint_as_float(u << 16); }
DEV float hi16(unsigned u) { return __uint_as_float(u & 0xffff0000u); }

DEV void cexp(float ar, float ai, float w, float& er, float& ei) {
  const float mag = __builtin_amdgcn_exp2f(ar * w);
  const float rev = __builtin_amdgcn_fractf(ai * w);
  er = mag * __builtin_amdgcn_cosf(rev);
  ei = mag * __builtin_amdgcn_sinf(rev);
}

template <int CTRL>
DEV float dpp_add(float x) {
  return x + __builtin_bit_cast(float, __builtin_amdgcn_update_dpp(0, __builtin_bit_cast(int, x), CTRL, 0xf, 0xf, false));
}
DEV float sum_x16(float x) {
  float a = x, b = x;
  asm volatile("s_nop 1\n\tv_permlane16_swap_b32 %0, %1" : "+v"(a), "+v"(b));
  return a + b;
}
DEV float sum_x32(float x) {
  float a = x, b = x;
  asm volatile("s_nop 1\n\tv_permlane32_swap_b32 %0, %1" : "+v"(a), "+v"(b));
  return a + b;
}
DEV float row_sum16(float x) {
  x = dpp_add<0xB1>(x);
  x = dpp_add<0x4E>(x);
  x = dpp_add<0x124>(x);
  x = dpp_add<0x128>(x);
  return x;
}
DEV float sum_mod4(float x) {
  x = dpp_add<0x124>(x);
  x = dpp_add<0x128>(x);
  x = sum_x16(x);
  x = sum_x32(x);
  return x;
}

struct Params {
  const u16 *u, *Are, *Aim, *Bre, *Bim, *Cre, *Cim, *logdt, *Dskip, *w1, *w3, *w2, *c, *wmod;
  u16* out;
  char* ws;
};

constexpr int A_KGEN = 640, A_LN = 64, A_MOD = 60, A_W2 = 135, A_PF = 54;
constexpr int A_TOTAL = A_KGEN + A_LN + A_MOD + A_W2 + A_PF;

__global__ __launch_bounds__(512) void kA(Params p) {
  __shared__ __attribute__((aligned(16))) char smem[41984];
  const int tid = threadIdx.x;
  int b = blockIdx.x;
  if (b < A_MOD) {
    float* red = (float*)smem;
    const int cg = tid & 7, rg = tid >> 3;
    float acc[8];
#pragma unroll
    for (int e = 0; e < 8; ++e) acc[e] = 0.f;
    u32x4 wv[10];
    float cv[10];
#pragma unroll
    for (int r = 0; r < 10; ++r) {
      wv[r] = *(const u32x4*)(p.wmod + (size_t)(rg * 10 + r) * 3840 + b * 64 + cg * 8);
      cv[r] = bf2f(p.c[rg * 10 + r]);
    }
#pragma unroll
    for (int r = 0; r < 10; ++r) {
      float s = rbf(cv[r] / (1.f + __expf(-cv[r])));
#pragma unroll
      for (int e = 0; e < 4; ++e) {
        acc[2 * e] += s * lo16(wv[r][e]);
        acc[2 * e + 1] += s * hi16(wv[r][e]);
      }
    }
    *(f32x4*)(red + rg * 64 + cg * 8) = (f32x4){acc[0], acc[1], acc[2], acc[3]};
    *(f32x4*)(red + rg * 64 + cg * 8 + 4) = (f32x4){acc[4], acc[5], acc[6], acc[7]};
    __syncthreads();
    {
      const int col = tid & 63, g = tid >> 6;
      float s = 0.f;
#pragma unroll
      for (int r = 0; r < 8; ++r) s += red[(g * 8 + r) * 64 + col];
      float* red2 = red + 4096;
      red2[g * 64 + col] = s;
      __syncthreads();
      if (tid < 64) {
        float t = 0.f;
#pragma unroll
        for (int r = 0; r < 8; ++r) t += red2[r * 64 + tid];
        ((float*)(p.ws + WS_MOD))[b * 64 + tid] = rbf(t);
      }
    }
    return;
  }
  b -= A_MOD;
  if (b < A_KGEN) {
    const int d = b;
    float* red = (float*)smem;
    f32x4* prm = (f32x4*)(smem + 24576);
    const int w = tid >> 6, l = tid & 63, i = l & 15, kq = l >> 4;
    if (tid < 128) {
      const int n = d * N_ + tid;
      const u16 a0 = p.Are[n], a1 = p.Aim[n], b0 = p.Bre[n], b1 = p.Bim[n], c0 = p.Cre[n], c1 = p.Cim[n];
      const float dt = rbf(__expf(bf2f(p.logdt[d])));
      const float dBr = dt * bf2f(b0), dBi = dt * bf2f(b1);
      const float Cr = bf2f(c0), Ci = bf2f(c1);
      prm[tid] = (f32x4){dt * bf2f(a0) * 1.44269504088896341f, dt * bf2f(a1) * 0.15915494309189535f, Cr * dBr - Ci * dBi, Cr * dBi + Ci * dBr};
    }
    const int o0 = (i & 7) + 128 * (i >> 3);
    const int o1 = (i < 8) ? 256 + 2 * (i & 3) + 128 * (i >> 2) : 512 + 4 * (i & 1) + 128 * ((i >> 1) & 3);
    const int o2 = 1024 + 128 * i;
    const float fo0 = (float)o0, fo1 = (float)o1, fo2 = (float)o2, fj = (float)(8 * i);
    __syncthreads();
    f32x4 acc[3];
#pragma unroll
    for (int s = 0; s < 3; ++s) acc[s] = (f32x4){0.f, 0.f, 0.f, 0.f};
#pragma unroll 2
    for (int kk = 0; kk < 4; ++kk) {
      const f32x4 pr = prm[16 * w + 4 * kk + kq];
      const float ar = pr[0], ai = pr[1], CBr = pr[2], CBi = pr[3];
      float yr, yi;
      cexp(ar, ai, fj, yr, yi);
#pragma unroll
      for (int s = 0; s < 3; ++s) {
        float zr, zi;
        cexp(ar, ai, s == 0 ? fo0 : (s == 1 ? fo1 : fo2), zr, zi);
        const float xr = CBr * zr - CBi * zi, xin = -(CBr * zi + CBi * zr);
        acc[s] = __builtin_amdgcn_mfma_f32_16x16x4f32(yr, xr, acc[s], 0, 0, 0);
        acc[s] = __builtin_amdgcn_mfma_f32_16x16x4f32(yi, xin, acc[s], 0, 0, 0);
      }
    }
#pragma unroll
    for (int s = 0; s < 3; ++s) *(f32x4*)(red + (w * 3 + s) * 256 + i * 16 + 4 * kq) = acc[s];
    __syncthreads();
    {
      const int t8 = tid & 255, cI = t8 >> 4, j = t8 & 15, tl = tid >> 8;
      const int i0 = (cI & 7) + 128 * (cI >> 3) + 8 * j;
      const int i1 = (cI < 8) ? 256 + (cI & 3) + 64 * (cI >> 2) + 4 * j
                              : 384 + (cI & 1) + 32 * ((cI >> 1) & 3) + 2 * j;
      float* Kc = (float*)(p.ws + WS_KR) + (size_t)d * 644;
      float sum = 0.f;
#pragma unroll
      for (int ww = 0; ww < 8; ++ww) sum += red[(ww * 3 + tl) * 256 + t8];
      Kc[tl ? i1 : i0] = sum;
      if (tid <= 128) {
        float s2 = 0.f;
#pragma unroll
        for (int ww = 0; ww < 8; ++ww) s2 += red[(ww * 3 + 2) * 256 + tid];
        Kc[512 + 16 * cI + j] = s2;
      }
    }
    return;
  }
  b -= A_KGEN;
  if (b < A_LN) {
    constexpr int XS = 648;
    u16* xs = (u16*)smem;
    float* sm = (float*)(smem + 32 * XS * 2);
    float* sd = sm + 32;
    const int row = tid >> 4, c = tid & 15;
    {
      u32x4 v[5];
#pragma unroll
      for (int q = 0; q < 5; ++q) v[q] = *(const u32x4*)(p.u + (size_t)(b * 32 + row) * D_ + (c + 16 * q) * 8);
      float s = 0.f;
#pragma unroll
      for (int q = 0; q < 5; ++q)
#pragma unroll
        for (int e = 0; e < 4; ++e) s += lo16(v[q][e]) + hi16(v[q][e]);
      s = row_sum16(s);
      const float mean = s * (1.f / 640.f);
      float s2 = 0.f;
#pragma unroll
      for (int q = 0; q < 5; ++q)
#pragma unroll
        for (int e = 0; e < 4; ++e) {
          float a = lo16(v[q][e]) - mean, bb = hi16(v[q][e]) - mean;
          s2 += a * a + bb * bb;
        }
      s2 = row_sum16(s2);
      if (c == 0) {
        float var = rbf(s2 * (1.f / 640.f));
        sm[row] = rbf(mean);
        sd[row] = __builtin_amdgcn_rcpf(rbf(__builtin_amdgcn_sqrtf(rbf(var + 1e-5f))));
      }
#pragma unroll
      for (int q = 0; q < 5; ++q) *(u32x4*)(xs + row * XS + (c + 16 * q) * 8) = v[q];
    }
    __syncthreads();
    u16* nT = (u16*)(p.ws + WS_NT);
    u16* xT = (u16*)(p.ws + WS_XT);
    const int tq = tid & 3;
    float mm[8], dd[8];
#pragma unroll
    for (int e = 0; e < 8; ++e) { mm[e] = sm[tq * 8 + e]; dd[e] = sd[tq * 8 + e]; }
#pragma unroll
    for (int it = 0; it < 5; ++it) {
      const int d = (tid >> 2) + 128 * it;
      unsigned xo[4], no[4];
#pragma unroll
      for (int e2 = 0; e2 < 4; ++e2) {
        const int r0 = tq * 8 + e2 * 2;
        u16 a = xs[r0 * XS + d], bb = xs[(r0 + 1) * XS + d];
        xo[e2] = (unsigned)a | ((unsigned)bb << 16);
        float n0 = (bf2f(a) - mm[2 * e2]) * dd[2 * e2];
        float n1 = (bf2f(bb) - mm[2 * e2 + 1]) * dd[2 * e2 + 1];
        no[e2] = pack2(n0, n1);
      }
      const size_t o = (size_t)d * L_ + b * 32 + tq * 8;
      *(u32x4*)(xT + o) = (u32x4){xo[0], xo[1], xo[2], xo[3]};
      *(u32x4*)(nT + o) = (u32x4){no[0], no[1], no[2], no[3]};
    }
    return;
  }
  b -= A_LN;
  if (b >= A_W2) {
    const int pb = b - A_W2;
    const u32x4* s1 = (const u32x4*)(p.w1 + (size_t)pb * 20480) + tid;
    const u32x4* s3 = (const u32x4*)(p.w3 + (size_t)pb * 20480) + tid;
    u32x4 a = s1[0] ^ s3[0];
#pragma unroll
    for (int j = 1; j < 5; ++j) a ^= s1[j * 512] ^ s3[j * 512];
    asm volatile("" ::"v"(a));
    return;
  }
  {
    const int ks0 = (b / 5) * 2, dc = b % 5;
    u16* ts = (u16*)smem;
    {
      const int r = tid >> 3, cq = tid & 7;
      const u16* src = p.w2 + (size_t)(ks0 * 32 + r) * D_ + dc * 128 + cq * 16;
      u32x4 a = *(const u32x4*)src, bb = *(const u32x4*)(src + 8);
      *(u32x4*)(ts + r * 136 + cq * 16) = a;
      *(u32x4*)(ts + r * 136 + cq * 16 + 8) = bb;
    }
    __syncthreads();
    u16* dst = (u16*)(p.ws + WS_W2);
#pragma unroll
    for (int it = 0; it < 2; ++it) {
      const int item = tid + 512 * it;
      const int lane = item & 63, dtl = (item >> 6) & 7, kh = item >> 9;
      const int dd = dtl * 16 + (lane & 15), k0 = kh * 32 + (lane >> 4) * 8;
      unsigned o[4];
#pragma unroll
      for (int e2 = 0; e2 < 4; ++e2)
        o[e2] = (unsigned)ts[(k0 + 2 * e2) * 136 + dd] | ((unsigned)ts[(k0 + 2 * e2 + 1) * 136 + dd] << 16);
      const int dt = dc * 8 + dtl;
      *(u32x4*)(dst + ((size_t)(dt * 54 + ks0 + kh) * 64 + lane) * 8) = (u32x4){o[0], o[1], o[2], o[3]};
    }
  }
}

constexpr int B_HB = 2 * KR_LEN * 2;
constexpr int B_HS = 4640;
constexpr int B_RED = B_HB + B_HS;
constexpr int B_LDS = B_RED + 32768;

template <int UMAX>
DEV void conv_residue(const char* smem, int o0, int lane, f32x4 (&acc)[8]) {
  const int r = lane & 15, kg = lane >> 4;
  const int rho = lane & 7, cb = (lane >> 3) & 1;
  bf16x8 bf[UMAX + 1];
  const int bbase = B_HB + rho * B_HS + 2 * (160 - o0 + 128 * cb + 8 * kg);
#pragma unroll
  for (int u = 0; u <= UMAX; ++u) bf[u] = *(const bf16x8*)(smem + bbase + 512 * u);
  const int abase = 2 * (2080 - o0 - 8 * r + 8 * kg);
#pragma unroll
  for (int i = 0; i < 8; ++i) {
    const bf16x8 ah = *(const bf16x8*)(smem + abase - 512 * i);
    const bf16x8 al = *(const bf16x8*)(smem + abase - 512 * i + KR_LEN * 2);
#pragma unroll
    for (int u = 0; u <= UMAX; ++u) {
      const int q = u + i;
      if (q < 8) {
        acc[q] = __builtin_amdgcn_mfma_f32_16x16x32_bf16(ah, bf[u], acc[q], 0, 0, 0);
        acc[q] = __builtin_amdgcn_mfma_f32_16x16x32_bf16(al, bf[u], acc[q], 0, 0, 0);
      }
    }
  }
}

DEV void h_mod(const u32x4 v, float t1, float sa, unsigned* wv) {
#pragma unroll
  for (int e = 0; e < 4; ++e) {
    float a = lo16(v[e]) * t1 + sa, bb = hi16(v[e]) * t1 + sa;
    wv[e] = pack2(a, bb);
  }
}
DEV void h_store(char* smem, int c, const unsigned* wv) {
#pragma unroll
  for (int rho = 0; rho < 8; ++rho) {
    u32x4 o;
#pragma unroll
    for (int e = 0; e < 4; ++e) {
      const int k = (rho >> 1) + e;
      o[e] = (rho & 1) ? __builtin_amdgcn_alignbit(wv[k + 1], wv[k], 16) : wv[k];
    }
    *(u32x4*)(smem + B_HB + rho * B_HS + c * 16) = o;
  }
}

__global__ __launch_bounds__(256, 3) void kB(Params p) {
  __shared__ __attribute__((aligned(16))) char smem[B_LDS + 2576];
  const int tid = threadIdx.x;
  const float* mod = (const float*)(p.ws + WS_MOD);
  if (blockIdx.x < 108) {
    const int fc = blockIdx.x >> 1, kh = blockIdx.x & 1;
    const int mat = tid >> 7, r = (tid >> 1) & 63, hc = tid & 1;
    const u16* src = (mat ? p.w3 : p.w1) + (size_t)(kh * 320 + r) * F_ + fc * 32 + hc * 16;
    u32x4 va[5], vb[5];
    float t1[5], sb[5];
#pragma unroll
    for (int j = 0; j < 5; ++j) {
      va[j] = *(const u32x4*)(src + (size_t)j * 64 * F_);
      vb[j] = *(const u32x4*)(src + (size_t)j * 64 * F_ + 8);
      t1[j] = mod[4 * 640 + kh * 320 + j * 64 + r];
      sb[j] = mod[3 * 640 + kh * 320 + j * 64 + r];
    }
    u16* ts = (u16*)smem;
    float* cs = (float*)(smem + 10240);
    float S[16], Bc[16];
#pragma unroll
    for (int e = 0; e < 16; ++e) { S[e] = 0.f; Bc[e] = 0.f; }
    u16* dst = (u16*)(p.ws + WS_W13);
#pragma unroll
    for (int j = 0; j < 5; ++j) {
      const float tt = rbf(1.f + t1[j]);
      u32x4 oa, ob;
#pragma unroll
      for (int e = 0; e < 4; ++e) {
        const float a0 = lo16(va[j][e]), a1 = hi16(va[j][e]), b0 = lo16(vb[j][e]), b1 = hi16(vb[j][e]);
        oa[e] = pack2(a0 * tt, a1 * tt);
        ob[e] = pack2(b0 * tt, b1 * tt);
        S[2 * e] += lo16(oa[e]); S[2 * e + 1] += hi16(oa[e]); S[8 + 2 * e] += lo16(ob[e]); S[8 + 2 * e + 1] += hi16(ob[e]);
        Bc[2 * e] += sb[j] * a0; Bc[2 * e + 1] += sb[j] * a1; Bc[8 + 2 * e] += sb[j] * b0; Bc[8 + 2 * e + 1] += sb[j] * b1;
      }
      if (j) __syncthreads();
      *(u32x4*)(ts + (mat * 64 + r) * 40 + hc * 16) = oa;
      *(u32x4*)(ts + (mat * 64 + r) * 40 + hc * 16 + 8) = ob;
      __syncthreads();
#pragma unroll
      for (int it = 0; it < 2; ++it) {
        const int item = tid + 256 * it;
        const int lane = item & 63, m2 = (item >> 6) & 1, ftl = (item >> 7) & 1, ksl = item >> 8;
        const u16* tb = ts + (m2 * 64 + ksl * 32 + (lane >> 4) * 8) * 40 + ftl * 16 + (lane & 15);
        unsigned o[4];
#pragma unroll
        for (int e2 = 0; e2 < 4; ++e2) o[e2] = (unsigned)tb[(2 * e2) * 40] | ((unsigned)tb[(2 * e2 + 1) * 40] << 16);
        const int ft = fc * 2 + ftl, ks = kh * 10 + j * 2 + ksl;
        *(u32x4*)(dst + ((size_t)((ft * 20 + ks) * 2 + m2) * 64 + lane) * 8) = (u32x4){o[0], o[1], o[2], o[3]};
      }
    }
#pragma unroll
    for (int e = 0; e < 4; ++e) {
      *(f32x4*)(cs + ((0 * 2 + mat) * 64 + r) * 32 + hc * 16 + 4 * e) = (f32x4){S[4 * e], S[4 * e + 1], S[4 * e + 2], S[4 * e + 3]};
      *(f32x4*)(cs + ((1 * 2 + mat) * 64 + r) * 32 + hc * 16 + 4 * e) = (f32x4){Bc[4 * e], Bc[4 * e + 1], Bc[4 * e + 2], Bc[4 * e + 3]};
    }
    __syncthreads();
    if (tid < 128) {
      const int col = tid & 31, sm2 = tid >> 5;
      float a = 0.f;
#pragma unroll 8
      for (int rr = 0; rr < 64; ++rr) a += cs[(sm2 * 64 + rr) * 32 + col];
      ((float*)(p.ws + WS_H2))[(kh * 4 + sm2) * F_ + fc * 32 + col] = a;
    }
    return;
  }
  const int d = blockIdx.x - 108;
  const u16* nT = (const u16*)(p.ws + WS_NT) + (size_t)d * L_;
  const f32x4* ksrc = (const f32x4*)((const float*)(p.ws + WS_KR) + (size_t)d * 644);
  const u32x4 n0 = *(const u32x4*)(nT + tid * 8);
  const u32x4 n1 = *(const u32x4*)(nT + (tid < 255 ? tid * 8 + 8 : 0));
  const f32x4 k0 = ksrc[tid < 161 ? tid : 0];
  const u32x4 xv = *(const u32x4*)((const u16*)(p.ws + WS_XT) + (size_t)d * L_ + tid * 8);
  const float sa = mod[d], cav = mod[640 + d], ga = mod[1280 + d];
  const float Dk = bf2f(p.Dskip[d]);
  const float t1 = rbf(1.f + cav);
  float* Kc = (float*)(smem + B_LDS);
  if (tid < 161) *(f32x4*)(Kc + tid * 4) = k0;
  {
    unsigned wv[8];
    h_mod(n0, t1, sa, wv);
    h_mod(n1, t1, sa, wv + 4);
    if (tid == 255) { wv[4] = 0u; wv[5] = 0u; wv[6] = 0u; wv[7] = 0u; }
    h_store(smem, tid + 20, wv);
    unsigned zv[8];
#pragma unroll
    for (int e = 0; e < 4; ++e) {
      const unsigned f0 = __builtin_amdgcn_readfirstlane(wv[e]);
      zv[e] = 0u;
      zv[4 + e] = (tid == 19) ? f0 : 0u;
    }
    if (tid < 20) h_store(smem, tid, zv);
  }
  __syncthreads();
  for (int cc = tid; cc < KR_LEN / 8; cc += 256) {
    unsigned hw[4], lw[4];
#pragma unroll
    for (int e2 = 0; e2 < 4; ++e2) {
      float kv[2];
#pragma unroll
      for (int z = 0; z < 2; ++z) {
        const int k = 2080 - (cc * 8 + e2 * 2 + z);
        const int kc = min(max(k, 0), 2047);
        const int v = (int)rbf((float)kc);
        const int idx = v < 256 ? v : (v < 512 ? 128 + (v >> 1) : (v < 1024 ? 256 + (v >> 2) : 384 + (v >> 3)));
        const float val = Kc[idx];
        kv[z] = (k == kc) ? val : 0.f;
      }
      const float h0 = rbf(kv[0]), h1 = rbf(kv[1]);
      hw[e2] = pack2(h0, h1);
      lw[e2] = pack2(kv[0] - h0, kv[1] - h1);
    }
    *(u32x4*)(smem + cc * 16) = (u32x4){hw[0], hw[1], hw[2], hw[3]};
    *(u32x4*)(smem + KR_LEN * 2 + cc * 16) = (u32x4){lw[0], lw[1], lw[2], lw[3]};
  }
  __syncthreads();
  const int w = __builtin_amdgcn_readfirstlane(tid >> 6), lane = tid & 63;
  f32x4 acc[8];
#pragma unroll
  for (int q = 0; q < 8; ++q) acc[q] = (f32x4){0.f, 0.f, 0.f, 0.f};
  conv_residue<7>(smem, -96 + 32 * w, lane, acc);
  conv_residue<7>(smem, -96 + 32 * (w + 4), lane, acc);
  if (w == 3) conv_residue<0>(smem, 160, lane, acc);
  __syncthreads();
  {
    const int rho = lane & 7, cb = (lane >> 3) & 1, rq = lane >> 4;
    const int sw = (rq & 1) | (cb << 1);
    float* red = (float*)(smem + B_RED) + w * 2048 + 128 * cb + 32 * rq + rho;
#pragma unroll
    for (int g = 0; g < 4; ++g) {
      float* rg = red + 8 * (g ^ sw);
#pragma unroll
      for (int q = 0; q < 8; ++q) rg[256 * q] = acc[q][g];
    }
  }
  __syncthreads();
  {
    const int t0 = tid * 8;
    const int pos = t0 ^ (((t0 >> 5) & 1) << 3) ^ (((t0 >> 7) & 1) << 4);
    const float* red = (const float*)(smem + B_RED);
    float y[8];
#pragma unroll
    for (int e = 0; e < 8; ++e) y[e] = 0.f;
#pragma unroll
    for (int ww = 0; ww < 4; ++ww) {
      f32x4 a = *(const f32x4*)(red + ww * 2048 + pos), bb = *(const f32x4*)(red + ww * 2048 + pos + 4);
#pragma unroll
      for (int e = 0; e < 4; ++e) { y[e] += a[e]; y[4 + e] += bb[e]; }
    }
    const u32x4 hv = *(const u32x4*)(smem + B_HB + 2 * (160 + t0));
    u32x4 o;
#pragma unroll
    for (int e = 0; e < 4; ++e) {
      float h0 = lo16(hv[e]), h1 = hi16(hv[e]);
      float y0 = rbf(rbf(y[2 * e]) + rbf(h0 * Dk)), y1 = rbf(rbf(y[2 * e + 1]) + rbf(h1 * Dk));
      float x0 = lo16(xv[e]) + rbf(ga * y0), x1 = hi16(xv[e]) + rbf(ga * y1);
      o[e] = pack2(x0, x1);
    }
    *(u32x4*)((u16*)(p.ws + WS_X1T) + ((size_t)(t0 >> 6) * D_ + d) * 64 + (t0 & 63)) = o;
  }
}

DEV int d_off(int k, int c) {
  const int f = ((k >> 1) & 1) | (((k >> 3) & 1) << 1);
  return k * 128 + ((((c >> 1) ^ f)) << 5) + ((c & 1) << 4);
}
DEV bf16x8 d_frag(const char* smem, int lane, int mi, int ks) {
  const int g = lane >> 4, q = (lane & 15) >> 2, pp = lane & 3;
  const int k0 = 32 * ks + 8 * g + q;
  const int c = 2 * mi + (pp >> 1);
  typedef __attribute__((address_space(3))) s16x4* lp;
  s16x4 a = __builtin_amdgcn_ds_read_tr16_b64_v4i16((lp)(smem + d_off(k0, c) + 8 * (pp & 1)));
  s16x4 bq = __builtin_amdgcn_ds_read_tr16_b64_v4i16((lp)(smem + d_off(k0 + 4, c) + 8 * (pp & 1)));
  return __builtin_shufflevector(a, bq, 0, 1, 2, 3, 4, 5, 6, 7);
}

DEV void d_load(const Params& p, int lane, int ft, int half, bf16x8 (&wf)[10][2]) {
  const bf16x8* wsrc = (const bf16x8*)(p.ws + WS_W13) + (size_t)(ft * 20 + half * 10) * 2 * 64 + lane;
#pragma unroll
  for (int ks = 0; ks < 10; ++ks) {
    wf[ks][0] = wsrc[(ks * 2) * 64];
    wf[ks][1] = wsrc[(ks * 2 + 1) * 64];
  }
}

DEV const bf16x8* d_wptr(const Params& p, int lane, int ft, int half) {
  return (const bf16x8*)(p.ws + WS_W13) + (size_t)(ft * 20 + half * 10) * 2 * 64 + lane;
}
template <int MT, bool RELOAD>
DEV void d_half(const char* smem, int lane, int mi0, int half, const bf16x8 (&wf)[10][2], f32x4 (&acc)[2][4],
                bf16x8 (&wn)[10][2], const bf16x8* nsrc) {
#pragma unroll
  for (int ks = 0; ks < 10; ++ks) {
    if (RELOAD) {
      __builtin_amdgcn_sched_barrier(0);
      wn[ks][0] = nsrc[(ks * 2) * 64];
      wn[ks][1] = nsrc[(ks * 2 + 1) * 64];
      __builtin_amdgcn_sched_barrier(0);
    }
#pragma unroll
    for (int m = 0; m < MT; ++m) {
      const bf16x8 hf = d_frag(smem, lane, mi0 + m, half * 10 + ks);
      acc[0][m] = __builtin_amdgcn_mfma_f32_16x16x32_bf16(wf[ks][0], hf, acc[0][m], 0, 0, 0);
      acc[1][m] = __builtin_amdgcn_mfma_f32_16x16x32_bf16(wf[ks][1], hf, acc[1][m], 0, 0, 0);
    }
  }
}

struct ColSums { f32x4 S[2], Bc[2]; };
DEV void d_cs(const Params& p, int lane, int ft, ColSums& c) {
  const float* cs = (const float*)(p.ws + WS_H2) + ft * 16 + 4 * (lane >> 4);
#pragma unroll
  for (int mat = 0; mat < 2; ++mat) {
    c.S[mat] = *(const f32x4*)(cs + (0 + mat) * F_) + *(const f32x4*)(cs + (4 + mat) * F_);
    c.Bc[mat] = *(const f32x4*)(cs + (2 + mat) * F_) + *(const f32x4*)(cs + (6 + mat) * F_);
  }
}
template <int MT>
DEV void d_epi(const Params& p, const float* st, const ColSums& c, int lane, int ft, int mi0, int mt, f32x4 (&acc)[2][4]) {
  const int rq = lane >> 4;
  u16* gdst = (u16*)(p.ws + WS_G);
#pragma unroll
  for (int m = 0; m < MT; ++m) {
    const float rs = st[64 + (mi0 + m) * 16 + (lane & 15)], mr = st[(mi0 + m) * 16 + (lane & 15)] * rs;
    float gv[4];
#pragma unroll
    for (int g = 0; g < 4; ++g) {
      const float gate = acc[0][m][g] * rs + (c.Bc[0][g] - mr * c.S[0][g]);
      const float up = acc[1][m][g] * rs + (c.Bc[1][g] - mr * c.S[1][g]);
      gv[g] = gate * up * __builtin_amdgcn_rcpf(1.f + __builtin_amdgcn_exp2f(gate * -1.44269504088896341f));
    }
    const int mtile = mt * 4 + mi0 + m;
    const int lane2 = (lane & 15) + 16 * (2 * (ft & 1) + (rq >> 1));
    u32x2 o = {pack2(gv[0], gv[1]), pack2(gv[2], gv[3])};
    *(u32x2*)(gdst + ((size_t)(mtile * 54 + (ft >> 1)) * 64 + lane2) * 8 + 4 * (rq & 1)) = o;
  }
#pragma unroll
  for (int m = 0; m < 4; ++m) { acc[0][m] = (f32x4){0.f, 0.f, 0.f, 0.f}; acc[1][m] = (f32x4){0.f, 0.f, 0.f, 0.f}; }
}

#define SB0 __builtin_amdgcn_sched_barrier(0)
#define RAWBAR() do { asm volatile("s_waitcnt lgkmcnt(0)" ::: "memory"); __builtin_amdgcn_s_barrier(); asm volatile("" ::: "memory"); } while (0)

DEV float sum_mod8(float x) {
  x = dpp_add<0x128>(x);
  x = sum_x16(x);
  x = sum_x32(x);
  return x;
}
typedef __attribute__((ext_vector_type(2))) float f32x2;
DEV f32x2 unpk(unsigned u) { return (f32x2){lo16(u), hi16(u)}; }

__global__ __launch_bounds__(512) void kD(Params p) {
  __shared__ __attribute__((aligned(16))) char smem[81920 + 4096 + 4096];
  const int tid = threadIdx.x;
  const int xc = blockIdx.x & 7, jj = blockIdx.x >> 3;
  const int mt = (xc & 1) * 16 + (jj & 15), fpart = (xc >> 1) * 2 + (jj >> 4);
  const int w = __builtin_amdgcn_readfirstlane(tid >> 6), lane = tid & 63;
  float* ps = (float*)(smem + 81920);
  const float* mod = (const float*)(p.ws + WS_MOD);
  const int c = tid & 7, d0 = tid >> 3;
  const int fs = (27 * fpart) >> 1, fe = (27 * (fpart + 1)) >> 1;
  bf16x8 wA[10][2], wB[10][2];
  char* vdst = smem + d_off(d0, c);
  float* st = (float*)(smem + 81920 + 4096) + w * 128;
  u32x4 v[10], pv;
  {
    const u16* src = (const u16*)(p.ws + WS_X1T) + (size_t)mt * D_ * 64 + d0 * 64 + c * 8;
#pragma unroll
    for (int it = 0; it < 10; ++it) v[it] = *(const u32x4*)(src + it * 4096);
    pv = *(const u32x4*)(src - d0 * 64);
  }
  SB0;
#pragma unroll
  for (int it = 0; it < 10; ++it) *(u32x4*)(vdst + 8192 * it) = v[it];
  SB0;
  d_load(p, lane, fs + w, 0, wA);
  SB0;
  {
    f32x2 piv[4], s1[4], s2[4];
#pragma unroll
    for (int e = 0; e < 4; ++e) { piv[e] = unpk(pv[e]); s1[e] = (f32x2){0.f, 0.f}; s2[e] = (f32x2){0.f, 0.f}; }
#pragma unroll
    for (int it = 0; it < 10; ++it)
#pragma unroll
      for (int e = 0; e < 4; ++e) { const f32x2 a = unpk(v[it][e]) - piv[e]; s1[e] += a; s2[e] += a * a; }
    float s[8], q[8];
#pragma unroll
    for (int e = 0; e < 8; ++e) { s[e] = sum_mod8(s1[e >> 1][e & 1]); q[e] = sum_mod8(s2[e >> 1][e & 1]); }
    if (lane < 8) {
      *(f32x4*)(ps + w * 64 + lane * 8) = (f32x4){s[0], s[1], s[2], s[3]};
      *(f32x4*)(ps + w * 64 + lane * 8 + 4) = (f32x4){s[4], s[5], s[6], s[7]};
      *(f32x4*)(ps + 512 + w * 64 + lane * 8) = (f32x4){q[0], q[1], q[2], q[3]};
      *(f32x4*)(ps + 512 + w * 64 + lane * 8 + 4) = (f32x4){q[4], q[5], q[6], q[7]};
    }
  }
  SB0;
  RAWBAR();
  {
    float a = 0.f, a2 = 0.f;
#pragma unroll
    for (int ww = 0; ww < 8; ++ww) { a += ps[ww * 64 + lane]; a2 += ps[512 + ww * 64 + lane]; }
    const float m1 = a * (1.f / 640.f), m2 = a2 * (1.f / 640.f);
    const float var = rbf(fmaxf(m2 - m1 * m1, 0.f));
    const float pvt = bf2f(*(const u16*)(smem + d_off(0, lane >> 3) + (lane & 7) * 2));
    st[lane] = rbf(pvt + m1);
    st[64 + lane] = __builtin_amdgcn_rcpf(rbf(__builtin_amdgcn_sqrtf(rbf(var + 1e-5f))));
  }
  const bool big = (fe - fs == 14);
  f32x4 acc[2][4];
#pragma unroll
  for (int m = 0; m < 4; ++m) { acc[0][m] = (f32x4){0.f, 0.f, 0.f, 0.f}; acc[1][m] = (f32x4){0.f, 0.f, 0.f, 0.f}; }
  const int ft2 = (w < 4) ? fs + 8 + w : (big ? fs + 12 + ((w & 3) >> 1) : fs + 12);
  ColSums cs1, cs2;
  d_cs(p, lane, fs + w, cs1);
  d_cs(p, lane, ft2, cs2);
  SB0;
  d_half<4, true>(smem, lane, 0, 0, wA, acc, wB, d_wptr(p, lane, fs + w, 1));
  d_half<4, true>(smem, lane, 0, 1, wB, acc, wA, d_wptr(p, lane, ft2, 0));
  d_epi<4>(p, st, cs1, lane, fs + w, 0, mt, acc);
  SB0;
  int ln = lane;
  asm volatile("" : "+v"(ln));
  if (w < 4) {
    d_half<4, true>(smem, ln, 0, 0, wA, acc, wB, d_wptr(p, ln, ft2, 1));
    d_half<4, false>(smem, ln, 0, 1, wB, acc, wA, nullptr);
    d_epi<4>(p, st, cs2, ln, ft2, 0, mt, acc);
  } else if (big) {
    const int mi0 = 2 * (w & 1);
    d_half<2, true>(smem, ln, mi0, 0, wA, acc, wB, d_wptr(p, ln, ft2, 1));
    d_half<2, false>(smem, ln, mi0, 1, wB, acc, wA, nullptr);
    d_epi<2>(p, st, cs2, ln, ft2, mi0, mt, acc);
  } else {
    const int mi0 = w & 3;
    d_half<1, true>(smem, ln, mi0, 0, wA, acc, wB, d_wptr(p, ln, ft2, 1));
    d_half<1, false>(smem, ln, mi0, 1, wB, acc, wA, nullptr);
    d_epi<1>(p, st, cs2, ln, ft2, mi0, mt, acc);
  }
}

constexpr int E_RS = 84;
constexpr int E_DEPTH = 5;
__global__ __launch_bounds__(256) void kE(Params p) {
  __shared__ __attribute__((aligned(16))) float red[4 * 64 * E_RS + 80 * 36];
  const int tid = threadIdx.x, xc = blockIdx.x & 7, j = blockIdx.x >> 3;
  const int mt = (xc >> 1) * 8 + (j & 7), np = (xc & 1) * 4 + (j >> 3);
  const int w = __builtin_amdgcn_readfirstlane(tid >> 6), lane = tid & 63;
  const int kstart = (w < 2) ? 14 * w : 28 + 13 * (w - 2);
  const int kcnt = (w < 2) ? 14 : 13;
  const bf16x8* gsrc = (const bf16x8*)(p.ws + WS_G) + (size_t)(mt * 4) * 54 * 64 + kstart * 64 + lane;
  const bf16x8* wsrc = (const bf16x8*)(p.ws + WS_W2) + (size_t)(np * 5) * 54 * 64 + kstart * 64 + lane;
  f32x4 acc[5][4];
#pragma unroll
  for (int i = 0; i < 5; ++i)
#pragma unroll
    for (int m = 0; m < 4; ++m) acc[i][m] = (f32x4){0.f, 0.f, 0.f, 0.f};
  bf16x8 gf[E_DEPTH][4], wf[E_DEPTH][5];
#pragma unroll
  for (int s = 0; s < E_DEPTH; ++s) {
#pragma unroll
    for (int m = 0; m < 4; ++m) gf[s][m] = gsrc[(m * 54 + s) * 64];
#pragma unroll
    for (int i = 0; i < 5; ++i) wf[s][i] = wsrc[(i * 54 + s) * 64];
  }
  const float* mod = (const float*)(p.ws + WS_MOD);
  u16* xs = (u16*)(red + 4 * 64 * E_RS);
  u32x4 xa, xb, xc2;
  {
    const u16* x1t = (const u16*)(p.ws + WS_X1T) + ((size_t)mt * D_ + np * 80) * 64;
    xa = *(const u32x4*)(x1t + tid * 8);
    xb = *(const u32x4*)(x1t + (tid + 256) * 8);
    xc2 = *(const u32x4*)(x1t + (tid < 128 ? tid + 512 : 0) * 8);
  }
  SB0;
#pragma unroll
  for (int kk = 0; kk < 14; ++kk) {
    if (kk < kcnt) {
#pragma unroll
      for (int i = 0; i < 5; ++i)
#pragma unroll
        for (int m = 0; m < 4; ++m)
          acc[i][m] = __builtin_amdgcn_mfma_f32_16x16x32_bf16(wf[kk % E_DEPTH][i], gf[kk % E_DEPTH][m], acc[i][m], 0, 0, 0);
    }
    SB0;
    if (kk + E_DEPTH < kcnt) {
#pragma unroll
      for (int m = 0; m < 4; ++m) gf[kk % E_DEPTH][m] = gsrc[(m * 54 + kk + E_DEPTH) * 64];
#pragma unroll
      for (int i = 0; i < 5; ++i) wf[kk % E_DEPTH][i] = wsrc[(i * 54 + kk + E_DEPTH) * 64];
    }
    SB0;
  }
  {
    const int rq = lane >> 4;
#pragma unroll
    for (int i = 0; i < 5; ++i)
#pragma unroll
      for (int m = 0; m < 4; ++m)
        *(f32x4*)(red + (w * 64 + m * 16 + (lane & 15)) * E_RS + i * 16 + 4 * rq) = acc[i][m];
    *(u32x4*)(xs + (tid >> 3) * 72 + (tid & 7) * 8) = xa;
    *(u32x4*)(xs + ((tid + 256) >> 3) * 72 + (tid & 7) * 8) = xb;
    if (tid < 128) *(u32x4*)(xs + ((tid + 512) >> 3) * 72 + (tid & 7) * 8) = xc2;
  }
  __syncthreads();
  for (int item = tid; item < 640; item += 256) {
    const int t = item / 10, cc = item % 10;
    const int dcol = np * 80 + cc * 8;
    const size_t o = (size_t)(mt * 64 + t) * D_ + dcol;
    float xv[8];
#pragma unroll
    for (int e = 0; e < 8; ++e) xv[e] = bf2f(xs[(cc * 8 + e) * 72 + t]);
    const f32x4 g0 = *(const f32x4*)(mod + 5 * 640 + dcol), g1 = *(const f32x4*)(mod + 5 * 640 + dcol + 4);
    float y[8];
#pragma unroll
    for (int e = 0; e < 8; ++e) y[e] = 0.f;
#pragma unroll
    for (int ww = 0; ww < 4; ++ww) {
      f32x4 a = *(const f32x4*)(red + (ww * 64 + t) * E_RS + cc * 8), bb = *(const f32x4*)(red + (ww * 64 + t) * E_RS + cc * 8 + 4);
#pragma unroll
      for (int e = 0; e < 4; ++e) { y[e] += a[e]; y[4 + e] += bb[e]; }
    }
    u32x4 ov;
#pragma unroll
    for (int e = 0; e < 4; ++e) {
      const float ga = (e < 2) ? g0[2 * e] : g1[2 * e - 4], gb = (e < 2) ? g0[2 * e + 1] : g1[2 * e - 3];
      float r0 = xv[2 * e] + rbf(ga * rbf(y[2 * e])), r1 = xv[2 * e + 1] + rbf(gb * rbf(y[2 * e + 1]));
      ov[e] = pack2(r0, r1);
    }
    *(u32x4*)(p.out + o) = ov;
  }
}

extern "C" void kernel_launch(void* const* d_in, const int* in_sizes, int n_in,
                              void* d_out, int out_size, void* d_ws, size_t ws_size,
                              hipStream_t stream) {
  Params p;
  p.u = (const u16*)d_in[0];
  p.Are = (const u16*)d_in[1];
  p.Aim = (const u16*)d_in[2];
  p.Bre = (const u16*)d_in[3];
  p.Bim = (const u16*)d_in[4];
  p.Cre = (const u16*)d_in[5];
  p.Cim = (const u16*)d_in[6];
  p.logdt = (const u16*)d_in[7];
  p.Dskip = (const u16*)d_in[8];
  p.w1 = (const u16*)d_in[10];
  p.w3 = (const u16*)d_in[11];
  p.w2 = (const u16*)d_in[12];
  p.c = (const u16*)d_in[13];
  p.wmod = (const u16*)d_in[14];
  p.out = (u16*)d_out;
  p.ws = (char*)d_ws;
  kA<<<A_TOTAL, 512, 0, stream>>>(p);
  kB<<<748, 256, 0, stream>>>(p);
  kD<<<256, 512, 0, stream>>>(p);
  kE<<<256, 256, 0, stream>>>(p);
}
```

```cpp
#include <hip/hip_runtime.h>
#include <hip/hip_bf16.h>

typedef unsigned short u16;
typedef __attribute__((ext_vector_type(8))) short bf16x8;
typedef __attribute__((ext_vector_type(4))) short s16x4;
typedef __attribute__((ext_vector_type(4))) float f32x4;
typedef __attribute__((ext_vector_type(4))) unsigned u32x4;
typedef __attribute__((ext_vector_type(2))) unsigned u32x2;

#define DEV __device__ __forceinline__

constexpr int L_ = 2048, D_ = 640, N_ = 128, F_ = 1728;
constexpr int KR_LEN = 2208;

constexpr size_t WS_MOD = 0;
constexpr size_t WS_NT = 16384;
constexpr size_t WS_XT = WS_NT + (size_t)D_ * L_ * 2;
constexpr size_t WS_KR = WS_XT + (size_t)D_ * L_ * 2;
constexpr size_t WS_X1T = WS_KR + (size_t)D_ * 644 * 4;
constexpr size_t WS_H2 = WS_X1T + (size_t)D_ * L_ * 2;
constexpr size_t WS_W13 = WS_H2 + (size_t)8 * F_ * 4;
constexpr size_t WS_W2 = WS_W13 + (size_t)2 * D_ * F_ * 2;
constexpr size_t WS_G = WS_W2 + (size_t)D_ * F_ * 2;
constexpr size_t WS_END = WS_G + (size_t)L_ * F_ * 2;

DEV float bf2f(u16 x) { return __uint_as_float(((unsigned)x) << 16); }
DEV u16 f2bf(float f) { return __builtin_bit_cast(u16, (__bf16)f); }
DEV float rbf(float f) { return bf2f(f2bf(f)); }
DEV unsigned pack2(float a, float b) { return (unsigned)f2bf(a) | ((unsigned)f2bf(b) << 16); }
DEV float lo16(unsigned u) { return __uint_as_float(u << 16); }
DEV float hi16(unsigned u) { return __uint_as_float(u & 0xffff0000u); }

DEV void cexp(float ar, float ai, float w, float& er, float& ei) {
  const float mag = __builtin_amdgcn_exp2f(ar * w);
  const float rev = __builtin_amdgcn_fractf(ai * w);
  er = mag * __builtin_amdgcn_cosf(rev);
  ei = mag * __builtin_amdgcn_sinf(rev);
}

template <int CTRL>
DEV float dpp_add(float x) {
  return x + __builtin_bit_cast(float, __builtin_amdgcn_update_dpp(0, __builtin_bit_cast(int, x), CTRL, 0xf, 0xf, false));
}
DEV float sum_x16(float x) {
  float a = x, b = x;
  asm volatile("s_nop 1\n\tv_permlane16_swap_b32 %0, %1" : "+v"(a), "+v"(b));
  return a + b;
}
DEV float sum_x32(float x) {
  float a = x, b = x;
  asm volatile("s_nop 1\n\tv_permlane32_swap_b32 %0, %1" : "+v"(a), "+v"(b));
  return a + b;
}
DEV float row_sum16(float x) {
  x = dpp_add<0xB1>(x);
  x = dpp_add<0x4E>(x);
  x = dpp_add<0x124>(x);
  x = dpp_add<0x128>(x);
  return x;
}
DEV float sum_mod4(float x) {
  x = dpp_add<0x124>(x);
  x = dpp_add<0x128>(x);
  x = sum_x16(x);
  x = sum_x32(x);
  return x;
}

struct Params {
  const u16 *u, *Are, *Aim, *Bre, *Bim, *Cre, *Cim, *logdt, *Dskip, *w1, *w3, *w2, *c, *wmod;
  u16* out;
  char* ws;
};

constexpr int A_KGEN = 640, A_LN = 64, A_MOD = 60, A_W2 = 135;
constexpr int A_TOTAL = A_KGEN + A_LN + A_MOD + A_W2;

__global__ __launch_bounds__(512) void kA(Params p) {
  __shared__ __attribute__((aligned(16))) char smem[41984];
  const int tid = threadIdx.x;
  int b = blockIdx.x;
  if (b < A_MOD) {
    float* red = (float*)smem;
    const int cg = tid & 7, rg = tid >> 3;
    float acc[8];
#pragma unroll
    for (int e = 0; e < 8; ++e) acc[e] = 0.f;
    u32x4 wv[10];
    float cv[10];
#pragma unroll
    for (int r = 0; r < 10; ++r) {
      wv[r] = *(const u32x4*)(p.wmod + (size_t)(rg * 10 + r) * 3840 + b * 64 + cg * 8);
      cv[r] = bf2f(p.c[rg * 10 + r]);
    }
#pragma unroll
    for (int r = 0; r < 10; ++r) {
      float s = rbf(cv[r] / (1.f + __expf(-cv[r])));
#pragma unroll
      for (int e = 0; e < 4; ++e) {
        acc[2 * e] += s * lo16(wv[r][e]);
        acc[2 * e + 1] += s * hi16(wv[r][e]);
      }
    }
    *(f32x4*)(red + rg * 64 + cg * 8) = (f32x4){acc[0], acc[1], acc[2], acc[3]};
    *(f32x4*)(red + rg * 64 + cg * 8 + 4) = (f32x4){acc[4], acc[5], acc[6], acc[7]};
    __syncthreads();
    {
      const int col = tid & 63, g = tid >> 6;
      float s = 0.f;
#pragma unroll
      for (int r = 0; r < 8; ++r) s += red[(g * 8 + r) * 64 + col];
      float* red2 = red + 4096;
      red2[g * 64 + col] = s;
      __syncthreads();
      if (tid < 64) {
        float t = 0.f;
#pragma unroll
        for (int r = 0; r < 8; ++r) t += red2[r * 64 + tid];
        ((float*)(p.ws + WS_MOD))[b * 64 + tid] = rbf(t);
      }
    }
    return;
  }
  b -= A_MOD;
  if (b < A_KGEN) {
    const int d = b;
    float* red = (float*)smem;
    f32x4* prm = (f32x4*)(smem + 24576);
    const int w = tid >> 6, l = tid & 63, i = l & 15, kq = l >> 4;
    if (tid < 128) {
      const int n = d * N_ + tid;
      const u16 a0 = p.Are[n], a1 = p.Aim[n], b0 = p.Bre[n], b1 = p.Bim[n], c0 = p.Cre[n], c1 = p.Cim[n];
      const float dt = rbf(__expf(bf2f(p.logdt[d])));
      const float dBr = dt * bf2f(b0), dBi = dt * bf2f(b1);
      const float Cr = bf2f(c0), Ci = bf2f(c1);
      prm[tid] = (f32x4){dt * bf2f(a0) * 1.44269504088896341f, dt * bf2f(a1) * 0.15915494309189535f, Cr * dBr - Ci * dBi, Cr * dBi + Ci * dBr};
    }
    const int o0 = (i & 7) + 128 * (i >> 3);
    const int o1 = (i < 8) ? 256 + 2 * (i & 3) + 128 * (i >> 2) : 512 + 4 * (i & 1) + 128 * ((i >> 1) & 3);
    const int o2 = 1024 + 128 * i;
    const float fo0 = (float)o0, fo1 = (float)o1, fo2 = (float)o2, fj = (float)(8 * i);
    __syncthreads();
    f32x4 acc[3];
#pragma unroll
    for (int s = 0; s < 3; ++s) acc[s] = (f32x4){0.f, 0.f, 0.f, 0.f};
#pragma unroll 2
    for (int kk = 0; kk < 4; ++kk) {
      const f32x4 pr = prm[16 * w + 4 * kk + kq];
      const float ar = pr[0], ai = pr[1], CBr = pr[2], CBi = pr[3];
      float yr, yi;
      cexp(ar, ai, fj, yr, yi);
#pragma unroll
      for (int s = 0; s < 3; ++s) {
        float zr, zi;
        cexp(ar, ai, s == 0 ? fo0 : (s == 1 ? fo1 : fo2), zr, zi);
        const float xr = CBr * zr - CBi * zi, xin = -(CBr * zi + CBi * zr);
        acc[s] = __builtin_amdgcn_mfma_f32_16x16x4f32(yr, xr, acc[s], 0, 0, 0);
        acc[s] = __builtin_amdgcn_mfma_f32_16x16x4f32(yi, xin, acc[s], 0, 0, 0);
      }
    }
#pragma unroll
    for (int s = 0; s < 3; ++s) *(f32x4*)(red + (w * 3 + s) * 256 + i * 16 + 4 * kq) = acc[s];
    __syncthreads();
    {
      const int t8 = tid & 255, cI = t8 >> 4, j = t8 & 15, tl = tid >> 8;
      const int i0 = (cI & 7) + 128 * (cI >> 3) + 8 * j;
      const int i1 = (cI < 8) ? 256 + (cI & 3) + 64 * (cI >> 2) + 4 * j
                              : 384 + (cI & 1) + 32 * ((cI >> 1) & 3) + 2 * j;
      float* Kc = (float*)(p.ws + WS_KR) + (size_t)d * 644;
      float sum = 0.f;
#pragma unroll
      for (int ww = 0; ww < 8; ++ww) sum += red[(ww * 3 + tl) * 256 + t8];
      Kc[tl ? i1 : i0] = sum;
      if (tid <= 128) {
        float s2 = 0.f;
#pragma unroll
        for (int ww = 0; ww < 8; ++ww) s2 += red[(ww * 3 + 2) * 256 + tid];
        Kc[512 + 16 * cI + j] = s2;
      }
    }
    return;
  }
  b -= A_KGEN;
  if (b < A_LN) {
    constexpr int XS = 648;
    u16* xs = (u16*)smem;
    float* sm = (float*)(smem + 32 * XS * 2);
    float* sd = sm + 32;
    const int row = tid >> 4, c = tid & 15;
    {
      u32x4 v[5];
#pragma unroll
      for (int q = 0; q < 5; ++q) v[q] = *(const u32x4*)(p.u + (size_t)(b * 32 + row) * D_ + (c + 16 * q) * 8);
      float s = 0.f;
#pragma unroll
      for (int q = 0; q < 5; ++q)
#pragma unroll
        for (int e = 0; e < 4; ++e) s += lo16(v[q][e]) + hi16(v[q][e]);
      s = row_sum16(s);
      const float mean = s * (1.f / 640.f);
      float s2 = 0.f;
#pragma unroll
      for (int q = 0; q < 5; ++q)
#pragma unroll
        for (int e = 0; e < 4; ++e) {
          float a = lo16(v[q][e]) - mean, bb = hi16(v[q][e]) - mean;
          s2 += a * a + bb * bb;
        }
      s2 = row_sum16(s2);
      if (c == 0) {
        float var = rbf(s2 * (1.f / 640.f));
        sm[row] = rbf(mean);
        sd[row] = __builtin_amdgcn_rcpf(rbf(__builtin_amdgcn_sqrtf(rbf(var + 1e-5f))));
      }
#pragma unroll
      for (int q = 0; q < 5; ++q) *(u32x4*)(xs + row * XS + (c + 16 * q) * 8) = v[q];
    }
    __syncthreads();
    u16* nT = (u16*)(p.ws + WS_NT);
    u16* xT = (u16*)(p.ws + WS_XT);
    const int tq = tid & 3;
    float mm[8], dd[8];
#pragma unroll
    for (int e = 0; e < 8; ++e) { mm[e] = sm[tq * 8 + e]; dd[e] = sd[tq * 8 + e]; }
#pragma unroll
    for (int it = 0; it < 5; ++it) {
      const int d = (tid >> 2) + 128 * it;
      unsigned xo[4], no[4];
#pragma unroll
      for (int e2 = 0; e2 < 4; ++e2) {
        const int r0 = tq * 8 + e2 * 2;
        u16 a = xs[r0 * XS + d], bb = xs[(r0 + 1) * XS + d];
        xo[e2] = (unsigned)a | ((unsigned)bb << 16);
        float n0 = (bf2f(a) - mm[2 * e2]) * dd[2 * e2];
        float n1 = (bf2f(bb) - mm[2 * e2 + 1]) * dd[2 * e2 + 1];
        no[e2] = pack2(n0, n1);
      }
      const size_t o = (size_t)d * L_ + b * 32 + tq * 8;
      *(u32x4*)(xT + o) = (u32x4){xo[0], xo[1], xo[2], xo[3]};
      *(u32x4*)(nT + o) = (u32x4){no[0], no[1], no[2], no[3]};
    }
    return;
  }
  b -= A_LN;
  {
    const int ks0 = (b / 5) * 2, dc = b % 5;
    u16* ts = (u16*)smem;
    {
      const int r = tid >> 3, cq = tid & 7;
      const u16* src = p.w2 + (size_t)(ks0 * 32 + r) * D_ + dc * 128 + cq * 16;
      u32x4 a = *(const u32x4*)src, bb = *(const u32x4*)(src + 8);
      *(u32x4*)(ts + r * 136 + cq * 16) = a;
      *(u32x4*)(ts + r * 136 + cq * 16 + 8) = bb;
    }
    __syncthreads();
    u16* dst = (u16*)(p.ws + WS_W2);
#pragma unroll
    for (int it = 0; it < 2; ++it) {
      const int item = tid + 512 * it;
      const int lane = item & 63, dtl = (item >> 6) & 7, kh = item >> 9;
      const int dd = dtl * 16 + (lane & 15), k0 = kh * 32 + (lane >> 4) * 8;
      unsigned o[4];
#pragma unroll
      for (int e2 = 0; e2 < 4; ++e2)
        o[e2] = (unsigned)ts[(k0 + 2 * e2) * 136 + dd] | ((unsigned)ts[(k0 + 2 * e2 + 1) * 136 + dd] << 16);
      const int dt = dc * 8 + dtl;
      *(u32x4*)(dst + ((size_t)(dt * 54 + ks0 + kh) * 64 + lane) * 8) = (u32x4){o[0], o[1], o[2], o[3]};
    }
  }
}

constexpr int B_HB = 2 * KR_LEN * 2;
constexpr int B_HS = 4640;
constexpr int B_RED = B_HB + B_HS;
constexpr int B_LDS = B_RED + 32768;

template <int UMAX>
DEV void conv_residue(const char* smem, int o0, int lane, f32x4 (&acc)[8]) {
  const int r = lane & 15, kg = lane >> 4;
  const int rho = lane & 7, cb = (lane >> 3) & 1;
  bf16x8 bf[UMAX + 1];
  const int bbase = B_HB + rho * B_HS + 2 * (160 - o0 + 128 * cb + 8 * kg);
#pragma unroll
  for (int u = 0; u <= UMAX; ++u) bf[u] = *(const bf16x8*)(smem + bbase + 512 * u);
  const int abase = 2 * (2080 - o0 - 8 * r + 8 * kg);
#pragma unroll
  for (int i = 0; i < 8; ++i) {
    const bf16x8 ah = *(const bf16x8*)(smem + abase - 512 * i);
    const bf16x8 al = *(const bf16x8*)(smem + abase - 512 * i + KR_LEN * 2);
#pragma unroll
    for (int u = 0; u <= UMAX; ++u) {
      const int q = u + i;
      if (q < 8) {
        acc[q] = __builtin_amdgcn_mfma_f32_16x16x32_bf16(ah, bf[u], acc[q], 0, 0, 0);
        acc[q] = __builtin_amdgcn_mfma_f32_16x16x32_bf16(al, bf[u], acc[q], 0, 0, 0);
      }
    }
  }
}

DEV void h_mod(const u32x4 v, float t1, float sa, unsigned* wv) {
#pragma unroll
  for (int e = 0; e < 4; ++e) {
    float a = lo16(v[e]) * t1 + sa, bb = hi16(v[e]) * t1 + sa;
    wv[e] = pack2(a, bb);
  }
}
DEV void h_store(char* smem, int c, const unsigned* wv) {
#pragma unroll
  for (int rho = 0; rho < 8; ++rho) {
    u32x4 o;
#pragma unroll
    for (int e = 0; e < 4; ++e) {
      const int k = (rho >> 1) + e;
      o[e] = (rho & 1) ? __builtin_amdgcn_alignbit(wv[k + 1], wv[k], 16) : wv[k];
    }
    *(u32x4*)(smem + B_HB + rho * B_HS + c * 16) = o;
  }
}

__global__ __launch_bounds__(256, 3) void kB(Params p) {
  __shared__ __attribute__((aligned(16))) char smem[B_LDS + 2576];
  const int tid = threadIdx.x;
  const float* mod = (const float*)(p.ws + WS_MOD);
  if (blockIdx.x < 108) {
    const int fc = blockIdx.x >> 1, kh = blockIdx.x & 1;
    const int mat = tid >> 7, r = (tid >> 1) & 63, hc = tid & 1;
    const u16* src = (mat ? p.w3 : p.w1) + (size_t)(kh * 320 + r) * F_ + fc * 32 + hc * 16;
    u32x4 va[5], vb[5];
    float t1[5], sb[5];
#pragma unroll
    for (int j = 0; j < 5; ++j) {
      va[j] = *(const u32x4*)(src + (size_t)j * 64 * F_);
      vb[j] = *(const u32x4*)(src + (size_t)j * 64 * F_ + 8);
      t1[j] = mod[4 * 640 + kh * 320 + j * 64 + r];
      sb[j] = mod[3 * 640 + kh * 320 + j * 64 + r];
    }
    u16* ts = (u16*)smem;
    float* cs = (float*)(smem + 10240);
    float S[16], Bc[16];
#pragma unroll
    for (int e = 0; e < 16; ++e) { S[e] = 0.f; Bc[e] = 0.f; }
    u16* dst = (u16*)(p.ws + WS_W13);
#pragma unroll
    for (int j = 0; j < 5; ++j) {
      const float tt = rbf(1.f + t1[j]);
      u32x4 oa, ob;
#pragma unroll
      for (int e = 0; e < 4; ++e) {
        const float a0 = lo16(va[j][e]), a1 = hi16(va[j][e]), b0 = lo16(vb[j][e]), b1 = hi16(vb[j][e]);
        oa[e] = pack2(a0 * tt, a1 * tt);
        ob[e] = pack2(b0 * tt, b1 * tt);
        S[2 * e] += lo16(oa[e]); S[2 * e + 1] += hi16(oa[e]); S[8 + 2 * e] += lo16(ob[e]); S[8 + 2 * e + 1] += hi16(ob[e]);
        Bc[2 * e] += sb[j] * a0; Bc[2 * e + 1] += sb[j] * a1; Bc[8 + 2 * e] += sb[j] * b0; Bc[8 + 2 * e + 1] += sb[j] * b1;
      }
      if (j) __syncthreads();
      *(u32x4*)(ts + (mat * 64 + r) * 40 + hc * 16) = oa;
      *(u32x4*)(ts + (mat * 64 + r) * 40 + hc * 16 + 8) = ob;
      __syncthreads();
#pragma unroll
      for (int it = 0; it < 2; ++it) {
        const int item = tid + 256 * it;
        const int lane = item & 63, m2 = (item >> 6) & 1, ftl = (item >> 7) & 1, ksl = item >> 8;
        const u16* tb = ts + (m2 * 64 + ksl * 32 + (lane >> 4) * 8) * 40 + ftl * 16 + (lane & 15);
        unsigned o[4];
#pragma unroll
        for (int e2 = 0; e2 < 4; ++e2) o[e2] = (unsigned)tb[(2 * e2) * 40] | ((unsigned)tb[(2 * e2 + 1) * 40] << 16);
        const int ft = fc * 2 + ftl, ks = kh * 10 + j * 2 + ksl;
        *(u32x4*)(dst + ((size_t)((ft * 20 + ks) * 2 + m2) * 64 + lane) * 8) = (u32x4){o[0], o[1], o[2], o[3]};
      }
    }
#pragma unroll
    for (int e = 0; e < 4; ++e) {
      *(f32x4*)(cs + ((0 * 2 + mat) * 64 + r) * 32 + hc * 16 + 4 * e) = (f32x4){S[4 * e], S[4 * e + 1], S[4 * e + 2], S[4 * e + 3]};
      *(f32x4*)(cs + ((1 * 2 + mat) * 64 + r) * 32 + hc * 16 + 4 * e) = (f32x4){Bc[4 * e], Bc[4 * e + 1], Bc[4 * e + 2], Bc[4 * e + 3]};
    }
    __syncthreads();
    if (tid < 128) {
      const int col = tid & 31, sm2 = tid >> 5;
      float a = 0.f;
#pragma unroll 8
      for (int rr = 0; rr < 64; ++rr) a += cs[(sm2 * 64 + rr) * 32 + col];
      ((float*)(p.ws + WS_H2))[(kh * 4 + sm2) * F_ + fc * 32 + col] = a;
    }
    return;
  }
  const int d = blockIdx.x - 108;
  const u16* nT = (const u16*)(p.ws + WS_NT) + (size_t)d * L_;
  const f32x4* ksrc = (const f32x4*)((const float*)(p.ws + WS_KR) + (size_t)d * 644);
  const u32x4 n0 = *(const u32x4*)(nT + tid * 8);
  const u32x4 n1 = *(const u32x4*)(nT + (tid < 255 ? tid * 8 + 8 : 0));
  const f32x4 k0 = ksrc[tid < 161 ? tid : 0];
  const u32x4 xv = *(const u32x4*)((const u16*)(p.ws + WS_XT) + (size_t)d * L_ + tid * 8);
  const float sa = mod[d], cav = mod[640 + d], ga = mod[1280 + d];
  const float Dk = bf2f(p.Dskip[d]);
  const float t1 = rbf(1.f + cav);
  float* Kc = (float*)(smem + B_LDS);
  if (tid < 161) *(f32x4*)(Kc + tid * 4) = k0;
  {
    unsigned wv[8];
    h_mod(n0, t1, sa, wv);
    h_mod(n1, t1, sa, wv + 4);
    if (tid == 255) { wv[4] = 0u; wv[5] = 0u; wv[6] = 0u; wv[7] = 0u; }
    h_store(smem, tid + 20, wv);
    unsigned zv[8];
#pragma unroll
    for (int e = 0; e < 4; ++e) {
      const unsigned f0 = __builtin_amdgcn_readfirstlane(wv[e]);
      zv[e] = 0u;
      zv[4 + e] = (tid == 19) ? f0 : 0u;
    }
    if (tid < 20) h_store(smem, tid, zv);
  }
  __syncthreads();
  for (int cc = tid; cc < KR_LEN / 8; cc += 256) {
    unsigned hw[4], lw[4];
#pragma unroll
    for (int e2 = 0; e2 < 4; ++e2) {
      float kv[2];
#pragma unroll
      for (int z = 0; z < 2; ++z) {
        const int k = 2080 - (cc * 8 + e2 * 2 + z);
        const int kc = min(max(k, 0), 2047);
        const int v = (int)rbf((float)kc);
        const int idx = v < 256 ? v : (v < 512 ? 128 + (v >> 1) : (v < 1024 ? 256 + (v >> 2) : 384 + (v >> 3)));
        const float val = Kc[idx];
        kv[z] = (k == kc) ? val : 0.f;
      }
      const float h0 = rbf(kv[0]), h1 = rbf(kv[1]);
      hw[e2] = pack2(h0, h1);
      lw[e2] = pack2(kv[0] - h0, kv[1] - h1);
    }
    *(u32x4*)(smem + cc * 16) = (u32x4){hw[0], hw[1], hw[2], hw[3]};
    *(u32x4*)(smem + KR_LEN * 2 + cc * 16) = (u32x4){lw[0], lw[1], lw[2], lw[3]};
  }
  __syncthreads();
  const int w = __builtin_amdgcn_readfirstlane(tid >> 6), lane = tid & 63;
  f32x4 acc[8];
#pragma unroll
  for (int q = 0; q < 8; ++q) acc[q] = (f32x4){0.f, 0.f, 0.f, 0.f};
  conv_residue<7>(smem, -96 + 32 * w, lane, acc);
  conv_residue<7>(smem, -96 + 32 * (w + 4), lane, acc);
  if (w == 3) conv_residue<0>(smem, 160, lane, acc);
  __syncthreads();
  {
    const int rho = lane & 7, cb = (lane >> 3) & 1, rq = lane >> 4;
    const int sw = (rq & 1) | (cb << 1);
    float* red = (float*)(smem + B_RED) + w * 2048 + 128 * cb + 32 * rq + rho;
#pragma unroll
    for (int g = 0; g < 4; ++g) {
      float* rg = red + 8 * (g ^ sw);
#pragma unroll
      for (int q = 0; q < 8; ++q) rg[256 * q] = acc[q][g];
    }
  }
  __syncthreads();
  {
    const int t0 = tid * 8;
    const int pos = t0 ^ (((t0 >> 5) & 1) << 3) ^ (((t0 >> 7) & 1) << 4);
    const float* red = (const float*)(smem + B_RED);
    float y[8];
#pragma unroll
    for (int e = 0; e < 8; ++e) y[e] = 0.f;
#pragma unroll
    for (int ww = 0; ww < 4; ++ww) {
      f32x4 a = *(const f32x4*)(red + ww * 2048 + pos), bb = *(const f32x4*)(red + ww * 2048 + pos + 4);
#pragma unroll
      for (int e = 0; e < 4; ++e) { y[e] += a[e]; y[4 + e] += bb[e]; }
    }
    const u32x4 hv = *(const u32x4*)(smem + B_HB + 2 * (160 + t0));
    u32x4 o;
#pragma unroll
    for (int e = 0; e < 4; ++e) {
      float h0 = lo16(hv[e]), h1 = hi16(hv[e]);
      float y0 = rbf(rbf(y[2 * e]) + rbf(h0 * Dk)), y1 = rbf(rbf(y[2 * e + 1]) + rbf(h1 * Dk));
      float x0 = lo16(xv[e]) + rbf(ga * y0), x1 = hi16(xv[e]) + rbf(ga * y1);
      o[e] = pack2(x0, x1);
    }
    *(u32x4*)((u16*)(p.ws + WS_X1T) + ((size_t)(t0 >> 6) * D_ + d) * 64 + (t0 & 63)) = o;
  }
}

DEV int d_off(int k, int c) {
  const int f = ((k >> 1) & 1) | (((k >> 3) & 1) << 1);
  return k * 128 + ((((c >> 1) ^ f)) << 5) + ((c & 1) << 4);
}
DEV bf16x8 d_frag(const char* smem, int lane, int mi, int ks) {
  const int g = lane >> 4, q = (lane & 15) >> 2, pp = lane & 3;
  const int k0 = 32 * ks + 8 * g + q;
  const int c = 2 * mi + (pp >> 1);
  typedef __attribute__((address_space(3))) s16x4* lp;
  s16x4 a = __builtin_amdgcn_ds_read_tr16_b64_v4i16((lp)(smem + d_off(k0, c) + 8 * (pp & 1)));
  s16x4 bq = __builtin_amdgcn_ds_read_tr16_b64_v4i16((lp)(smem + d_off(k0 + 4, c) + 8 * (pp & 1)));
  return __builtin_shufflevector(a, bq, 0, 1, 2, 3, 4, 5, 6, 7);
}

DEV void d_load(const Params& p, int lane, int ft, int half, bf16x8 (&wf)[10][2]) {
  const bf16x8* wsrc = (const bf16x8*)(p.ws + WS_W13) + (size_t)(ft * 20 + half * 10) * 2 * 64 + lane;
#pragma unroll
  for (int ks = 0; ks < 10; ++ks) {
    wf[ks][0] = wsrc[(ks * 2) * 64];
    wf[ks][1] = wsrc[(ks * 2 + 1) * 64];
  }
}

DEV const bf16x8* d_wptr(const Params& p, int lane, int ft, int half) {
  return (const bf16x8*)(p.ws + WS_W13) + (size_t)(ft * 20 + half * 10) * 2 * 64 + lane;
}
template <int MT, bool RELOAD>
DEV void d_half(const char* smem, int lane, int mi0, int half, const bf16x8 (&wf)[10][2], f32x4 (&acc)[2][4],
                bf16x8 (&wn)[10][2], const bf16x8* nsrc) {
#pragma unroll
  for (int ks = 0; ks < 10; ++ks) {
    if (RELOAD) {
      __builtin_amdgcn_sched_barrier(0);
      wn[ks][0] = nsrc[(ks * 2) * 64];
      wn[ks][1] = nsrc[(ks * 2 + 1) * 64];
      __builtin_amdgcn_sched_barrier(0);
    }
#pragma unroll
    for (int m = 0; m < MT; ++m) {
      const bf16x8 hf = d_frag(smem, lane, mi0 + m, half * 10 + ks);
      acc[0][m] = __builtin_amdgcn_mfma_f32_16x16x32_bf16(wf[ks][0], hf, acc[0][m], 0, 0, 0);
      acc[1][m] = __builtin_amdgcn_mfma_f32_16x16x32_bf16(wf[ks][1], hf, acc[1][m], 0, 0, 0);
    }
  }
}

struct ColSums { f32x4 S[2], Bc[2]; };
DEV void d_cs(const Params& p, int lane, int ft, ColSums& c) {
  const float* cs = (const float*)(p.ws + WS_H2) + ft * 16 + 4 * (lane >> 4);
#pragma unroll
  for (int mat = 0; mat < 2; ++mat) {
    c.S[mat] = *(const f32x4*)(cs + (0 + mat) * F_) + *(const f32x4*)(cs + (4 + mat) * F_);
    c.Bc[mat] = *(const f32x4*)(cs + (2 + mat) * F_) + *(const f32x4*)(cs + (6 + mat) * F_);
  }
}
template <int MT>
DEV void d_epi(const Params& p, const float* st, const ColSums& c, int lane, int ft, int mi0, int mt, f32x4 (&acc)[2][4]) {
  const int rq = lane >> 4;
  u16* gdst = (u16*)(p.ws + WS_G);
#pragma unroll
  for (int m = 0; m < MT; ++m) {
    const float rs = st[64 + (mi0 + m) * 16 + (lane & 15)], mr = st[(mi0 + m) * 16 + (lane & 15)] * rs;
    float gv[4];
#pragma unroll
    for (int g = 0; g < 4; ++g) {
      const float gate = acc[0][m][g] * rs + (c.Bc[0][g] - mr * c.S[0][g]);
      const float up = acc[1][m][g] * rs + (c.Bc[1][g] - mr * c.S[1][g]);
      gv[g] = gate * up * __builtin_amdgcn_rcpf(1.f + __builtin_amdgcn_exp2f(gate * -1.44269504088896341f));
    }
    const int mtile = mt * 4 + mi0 + m;
    const int lane2 = (lane & 15) + 16 * (2 * (ft & 1) + (rq >> 1));
    u32x2 o = {pack2(gv[0], gv[1]), pack2(gv[2], gv[3])};
    *(u32x2*)(gdst + ((size_t)(mtile * 54 + (ft >> 1)) * 64 + lane2) * 8 + 4 * (rq & 1)) = o;
  }
#pragma unroll
  for (int m = 0; m < 4; ++m) { acc[0][m] = (f32x4){0.f, 0.f, 0.f, 0.f}; acc[1][m] = (f32x4){0.f, 0.f, 0.f, 0.f}; }
}

#define SB0 __builtin_amdgcn_sched_barrier(0)
#define RAWBAR() do { asm volatile("s_waitcnt lgkmcnt(0)" ::: "memory"); __builtin_amdgcn_s_barrier(); asm volatile("" ::: "memory"); } while (0)

DEV float sum_mod8(float x) {
  x = dpp_add<0x128>(x);
  x = sum_x16(x);
  x = sum_x32(x);
  return x;
}
typedef __attribute__((ext_vector_type(2))) float f32x2;
DEV f32x2 unpk(unsigned u) { return (f32x2){lo16(u), hi16(u)}; }

__global__ __launch_bounds__(512) void kD(Params p) {
  __shared__ __attribute__((aligned(16))) char smem[81920 + 4096 + 4096];
  const int tid = threadIdx.x;
  const int xc = blockIdx.x & 7, jj = blockIdx.x >> 3;
  const int mt = (xc & 1) * 16 + (jj & 15), fpart = (xc >> 1) * 2 + (jj >> 4);
  const int w = __builtin_amdgcn_readfirstlane(tid >> 6), lane = tid & 63;
  float* ps = (float*)(smem + 81920);
  const float* mod = (const float*)(p.ws + WS_MOD);
  const int c = tid & 7, d0 = tid >> 3;
  const int fs = (27 * fpart) >> 1, fe = (27 * (fpart + 1)) >> 1;
  bf16x8 wA[10][2], wB[10][2];
  char* vdst = smem + d_off(d0, c);
  float* st = (float*)(smem + 81920 + 4096) + w * 128;
  u32x4 v[10], pv;
  {
    const u16* src = (const u16*)(p.ws + WS_X1T) + (size_t)mt * D_ * 64 + d0 * 64 + c * 8;
#pragma unroll
    for (int it = 0; it < 10; ++it) v[it] = *(const u32x4*)(src + it * 4096);
    pv = *(const u32x4*)(src - d0 * 64);
  }
  SB0;
#pragma unroll
  for (int it = 0; it < 10; ++it) *(u32x4*)(vdst + 8192 * it) = v[it];
  SB0;
  d_load(p, lane, fs + w, 0, wA);
  SB0;
  {
    f32x2 piv[4], s1[4], s2[4];
#pragma unroll
    for (int e = 0; e < 4; ++e) { piv[e] = unpk(pv[e]); s1[e] = (f32x2){0.f, 0.f}; s2[e] = (f32x2){0.f, 0.f}; }
#pragma unroll
    for (int it = 0; it < 10; ++it)
#pragma unroll
      for (int e = 0; e < 4; ++e) { const f32x2 a = unpk(v[it][e]) - piv[e]; s1[e] += a; s2[e] += a * a; }
    float s[8], q[8];
#pragma unroll
    for (int e = 0; e < 8; ++e) { s[e] = sum_mod8(s1[e >> 1][e & 1]); q[e] = sum_mod8(s2[e >> 1][e & 1]); }
    if (lane < 8) {
      *(f32x4*)(ps + w * 64 + lane * 8) = (f32x4){s[0], s[1], s[2], s[3]};
      *(f32x4*)(ps + w * 64 + lane * 8 + 4) = (f32x4){s[4], s[5], s[6], s[7]};
      *(f32x4*)(ps + 512 + w * 64 + lane * 8) = (f32x4){q[0], q[1], q[2], q[3]};
      *(f32x4*)(ps + 512 + w * 64 + lane * 8 + 4) = (f32x4){q[4], q[5], q[6], q[7]};
    }
  }
  SB0;
  RAWBAR();
  {
    float a = 0.f, a2 = 0.f;
#pragma unroll
    for (int ww = 0; ww < 8; ++ww) { a += ps[ww * 64 + lane]; a2 += ps[512 + ww * 64 + lane]; }
    const float m1 = a * (1.f / 640.f), m2 = a2 * (1.f / 640.f);
    const float var = rbf(fmaxf(m2 - m1 * m1, 0.f));
    const float pvt = bf2f(*(const u16*)(smem + d_off(0, lane >> 3) + (lane & 7) * 2));
    st[lane] = rbf(pvt + m1);
    st[64 + lane] = __builtin_amdgcn_rcpf(rbf(__builtin_amdgcn_sqrtf(rbf(var + 1e-5f))));
  }
  const bool big = (fe - fs == 14);
  f32x4 acc[2][4];
#pragma unroll
  for (int m = 0; m < 4; ++m) { acc[0][m] = (f32x4){0.f, 0.f, 0.f, 0.f}; acc[1][m] = (f32x4){0.f, 0.f, 0.f, 0.f}; }
  const int ft2 = (w < 4) ? fs + 8 + w : (big ? fs + 12 + ((w & 3) >> 1) : fs + 12);
  ColSums cs1, cs2;
  d_cs(p, lane, fs + w, cs1);
  d_cs(p, lane, ft2, cs2);
  SB0;
  d_half<4, true>(smem, lane, 0, 0, wA, acc, wB, d_wptr(p, lane, fs + w, 1));
  d_half<4, true>(smem, lane, 0, 1, wB, acc, wA, d_wptr(p, lane, ft2, 0));
  d_epi<4>(p, st, cs1, lane, fs + w, 0, mt, acc);
  SB0;
  int ln = lane;
  asm volatile("" : "+v"(ln));
  if (w < 4) {
    d_half<4, true>(smem, ln, 0, 0, wA, acc, wB, d_wptr(p, ln, ft2, 1));
    d_half<4, false>(smem, ln, 0, 1, wB, acc, wA, nullptr);
    d_epi<4>(p, st, cs2, ln, ft2, 0, mt, acc);
  } else if (big) {
    const int mi0 = 2 * (w & 1);
    d_half<2, true>(smem, ln, mi0, 0, wA, acc, wB, d_wptr(p, ln, ft2, 1));
    d_half<2, false>(smem, ln, mi0, 1, wB, acc, wA, nullptr);
    d_epi<2>(p, st, cs2, ln, ft2, mi0, mt, acc);
  } else {
    const int mi0 = w & 3;
    d_half<1, true>(smem, ln, mi0, 0, wA, acc, wB, d_wptr(p, ln, ft2, 1));
    d_half<1, false>(smem, ln, mi0, 1, wB, acc, wA, nullptr);
    d_epi<1>(p, st, cs2, ln, ft2, mi0, mt, acc);
  }
}

constexpr int E_RS = 84;
constexpr int E_DEPTH = 5;
__global__ __launch_bounds__(256) void kE(Params p) {
  __shared__ __attribute__((aligned(16))) float red[4 * 64 * E_RS + 80 * 36];
  const int tid = threadIdx.x, xc = blockIdx.x & 7, j = blockIdx.x >> 3;
  const int mt = (xc >> 1) * 8 + (j & 7), np = (xc & 1) * 4 + (j >> 3);
  const int w = __builtin_amdgcn_readfirstlane(tid >> 6), lane = tid & 63;
  const int kstart = (w < 2) ? 14 * w : 28 + 13 * (w - 2);
  const int kcnt = (w < 2) ? 14 : 13;
  const bf16x8* gsrc = (const bf16x8*)(p.ws + WS_G) + (size_t)(mt * 4) * 54 * 64 + kstart * 64 + lane;
  const bf16x8* wsrc = (const bf16x8*)(p.ws + WS_W2) + (size_t)(np * 5) * 54 * 64 + kstart * 64 + lane;
  f32x4 acc[5][4];
#pragma unroll
  for (int i = 0; i < 5; ++i)
#pragma unroll
    for (int m = 0; m < 4; ++m) acc[i][m] = (f32x4){0.f, 0.f, 0.f, 0.f};
  bf16x8 gf[E_DEPTH][4], wf[E_DEPTH][5];
#pragma unroll
  for (int s = 0; s < E_DEPTH; ++s) {
#pragma unroll
    for (int m = 0; m < 4; ++m) gf[s][m] = gsrc[(m * 54 + s) * 64];
#pragma unroll
    for (int i = 0; i < 5; ++i) wf[s][i] = wsrc[(i * 54 + s) * 64];
  }
  const float* mod = (const float*)(p.ws + WS_MOD);
  u16* xs = (u16*)(red + 4 * 64 * E_RS);
  u32x4 xa, xb, xc2;
  {
    const u16* x1t = (const u16*)(p.ws + WS_X1T) + ((size_t)mt * D_ + np * 80) * 64;
    xa = *(const u32x4*)(x1t + tid * 8);
    xb = *(const u32x4*)(x1t + (tid + 256) * 8);
    xc2 = *(const u32x4*)(x1t + (tid < 128 ? tid + 512 : 0) * 8);
  }
  SB0;
#pragma unroll
  for (int kk = 0; kk < 14; ++kk) {
    if (kk < kcnt) {
#pragma unroll
      for (int i = 0; i < 5; ++i)
#pragma unroll
        for (int m = 0; m < 4; ++m)
          acc[i][m] = __builtin_amdgcn_mfma_f32_16x16x32_bf16(wf[kk % E_DEPTH][i], gf[kk % E_DEPTH][m], acc[i][m], 0, 0, 0);
    }
    SB0;
    if (kk + E_DEPTH < kcnt) {
#pragma unroll
      for (int m = 0; m < 4; ++m) gf[kk % E_DEPTH][m] = gsrc[(m * 54 + kk + E_DEPTH) * 64];
#pragma unroll
      for (int i = 0; i < 5; ++i) wf[kk % E_DEPTH][i] = wsrc[(i * 54 + kk + E_DEPTH) * 64];
    }
    SB0;
  }
  {
    const int rq = lane >> 4;
#pragma unroll
    for (int i = 0; i < 5; ++i)
#pragma unroll
      for (int m = 0; m < 4; ++m)
        *(f32x4*)(red + (w * 64 + m * 16 + (lane & 15)) * E_RS + i * 16 + 4 * rq) = acc[i][m];
    *(u32x4*)(xs + (tid >> 3) * 72 + (tid & 7) * 8) = xa;
    *(u32x4*)(xs + ((tid + 256) >> 3) * 72 + (tid & 7) * 8) = xb;
    if (tid < 128) *(u32x4*)(xs + ((tid + 512) >> 3) * 72 + (tid & 7) * 8) = xc2;
  }
  __syncthreads();
  for (int item = tid; item < 640; item += 256) {
    const int t = item / 10, cc = item % 10;
    const int dcol = np * 80 + cc * 8;
    const size_t o = (size_t)(mt * 64 + t) * D_ + dcol;
    float xv[8];
#pragma unroll
    for (int e = 0; e < 8; ++e) xv[e] = bf2f(xs[(cc * 8 + e) * 72 + t]);
    const f32x4 g0 = *(const f32x4*)(mod + 5 * 640 + dcol), g1 = *(const f32x4*)(mod + 5 * 640 + dcol + 4);
    float y[8];
#pragma unroll
    for (int e = 0; e < 8; ++e) y[e] = 0.f;
#pragma unroll
    for (int ww = 0; ww < 4; ++ww) {
      f32x4 a = *(const f32x4*)(red + (ww * 64 + t) * E_RS + cc * 8), bb = *(const f32x4*)(red + (ww * 64 + t) * E_RS + cc * 8 + 4);
#pragma unroll
      for (int e = 0; e < 4; ++e) { y[e] += a[e]; y[4 + e] += bb[e]; }
    }
    u32x4 ov;
#pragma unroll
    for (int e = 0; e < 4; ++e) {
      const float ga = (e < 2) ? g0[2 * e] : g1[2 * e - 4], gb = (e < 2) ? g0[2 * e + 1] : g1[2 * e - 3];
      float r0 = xv[2 * e] + rbf(ga * rbf(y[2 * e])), r1 = xv[2 * e + 1] + rbf(gb * rbf(y[2 * e + 1]));
      ov[e] = pack2(r0, r1);
    }
    *(u32x4*)(p.out + o) = ov;
  }
}

extern "C" void kernel_launch(void* const* d_in, const int* in_sizes, int n_in,
                              void* d_out, int out_size, void* d_ws, size_t ws_size,
                              hipStream_t stream) {
  Params p;
  p.u = (const u16*)d_in[0];
  p.Are = (const u16*)d_in[1];
  p.Aim = (const u16*)d_in[2];
  p.Bre = (const u16*)d_in[3];
  p.Bim = (const u16*)d_in[4];
  p.Cre = (const u16*)d_in[5];
  p.Cim = (const u16*)d_in[6];
  p.logdt = (const u16*)d_in[7];
  p.Dskip = (const u16*)d_in[8];
  p.w1 = (const u16*)d_in[10];
  p.w3 = (const u16*)d_in[11];
  p.w2 = (const u16*)d_in[12];
  p.c = (const u16*)d_in[13];
  p.wmod = (const u16*)d_in[14];
  p.out = (u16*)d_out;
  p.ws = (char*)d_ws;
  kA<<<A_TOTAL, 512, 0, stream>>>(p);
  kB<<<748, 256, 0, stream>>>(p);
  kD<<<256, 512, 0, stream>>>(p);
  kE<<<256, 256, 0, stream>>>(p);
}
```
